```python
import torch

def _softmax(x, axis=-1):
    return torch.softmax(x, dim=axis)
def _gelu(x):
    return torch.nn.functional.gelu(x, approximate="tanh")
def _silu(x):
    return torch.nn.functional.silu(x)
def _sigmoid(x):
    return torch.sigmoid(x)
def _layernorm(x, axis=-1, eps=1e-5):
    m = x.mean(dim=axis, keepdim=True)
    v = x.var(dim=axis, keepdim=True, unbiased=False)
    return (x - m) / torch.sqrt(v + eps)
def _scatter_add(tgt, idx, src, axis=0):
    return tgt.clone().index_add_(axis, idx, src)
def _segment_sum(src, seg, n):
    out = torch.zeros((n,) + src.shape[1:], dtype=src.dtype)
    return out.index_add_(0, seg, src)
def _pad(x, pw):
    flat = tuple(v for pair in reversed(pw) for v in pair)
    return torch.nn.functional.pad(x, flat)

kernel_name = 'parallel_sum__ssm_conv_h3__glu_gated_glu__prompt'

def setup_inputs():

    _w = (lambda a: (lambda t: t.to(torch.bfloat16) if t.is_floating_point() else t)(torch.as_tensor(a)))
    def _A_setup():
        B, S, D = 1, 2048, 1024
        ks = 4
        return {
            "q": _w((lambda *s: torch.randn(*s, dtype=torch.bfloat16) * 0.05)(B, S, D)),
            "k": _w((lambda *s: torch.randn(*s, dtype=torch.bfloat16) * 0.8)(B, S, D)),
            "v": _w((lambda *s: torch.randn(*s, dtype=torch.bfloat16) * 0.8)(B, S, D)),
            "shift_k": _w((lambda *s: torch.randn(*s, dtype=torch.bfloat16) * 0.8)(D, ks) * 0.5),
            "ssm_k": _w((lambda *s: torch.randn(*s, dtype=torch.bfloat16) * 0.8)(D, S) * (1.0 / S ** 0.5)),
            "ssm_D": _w((lambda *s: torch.randn(*s, dtype=torch.bfloat16) * 0.8)(D)),
        }
    _A_in = _A_setup()

    def _B_setup():
        B, S, D, F = 1, 2048, 1024, 3584
        return {
            "x": _w((lambda *s: torch.randn(*s, dtype=torch.bfloat16) * 0.05)(B, S, D)),
            "w_gate": _w((lambda *s: torch.randn(*s, dtype=torch.bfloat16) * 0.05)(D, F)),
            "w_up": _w((lambda *s: torch.randn(*s, dtype=torch.bfloat16) * 0.05)(D, F)),
            "w_down": _w((lambda *s: torch.randn(*s, dtype=torch.bfloat16) * 0.05)(F, D)),
        }
    _B_in = _B_setup()

    return {
        "A_q": _A_in["q"],
        "A_k": _A_in["k"],
        "A_v": _A_in["v"],
        "A_shift_k": _A_in["shift_k"],
        "A_ssm_k": _A_in["ssm_k"],
        "A_ssm_D": _A_in["ssm_D"],
        "B_x": _B_in["x"],
        "B_w_gate": _B_in["w_gate"],
        "B_w_up": _B_in["w_up"],
        "B_w_down": _B_in["w_down"],
    }

def reference(A_q, A_k, A_v, A_shift_k, A_ssm_k, A_ssm_D, B_x, B_w_gate, B_w_up, B_w_down):
    def _A_ref(q, k, v, shift_k, ssm_k, ssm_D):
        B, S, D = q.shape
        ks = shift_k.shape[-1]
        k_pad = _pad(k, ((0, 0), (ks - 1, 0), (0, 0)))
        k_shift = torch.zeros_like(k)
        for t in range(ks):
            k_shift = k_shift + k_pad[:, t:t + S, :] * shift_k[:, ks - 1 - t][None, None, :]
        kv = k_shift * v
        L = 2 * S
        kv_pad = _pad(kv, ((0, 0), (0, S), (0, 0)))
        kf_pad = _pad(ssm_k, ((0, 0), (0, S)))
        kv_f = (lambda x, axes=(-2,-1): torch.fft.rfft2(x.float(), dim=axes))(kv_pad[:, None, :, :], axes=(1, 2))
        kf_f = (lambda x, axes=(-2,-1): torch.fft.rfft2(x.float(), dim=axes))(kf_pad[:, None, :], axes=(1, 2))
        kf_f = (lambda x, *d: x.permute(*d))(kf_f, 1, 2, 0)
        y_f = kv_f * kf_f[None, :, :, :]
        y = (lambda x, s, axes=(-2,-1): torch.fft.irfft2(x, s=s, dim=axes).to(torch.bfloat16))(y_f, (1, L), axes=(1, 2))[:, 0, :S, :]
        y = y + kv * ssm_D[None, None, :]
        return q * y

    def _B_ref(x, w_gate, w_up, w_down):
        g = torch.einsum("bsd,df->bsf", x, w_gate)
        u = torch.einsum("bsd,df->bsf", x, w_up)
        h = _sigmoid(g) * u
        return torch.einsum("bsf,fd->bsd", h, w_down)

    x = A_q
    xn = _layernorm(x, axis=-1)
    return x + _A_ref(xn, A_k, A_v, A_shift_k, A_ssm_k, A_ssm_D) + _B_ref(xn, B_w_gate, B_w_up, B_w_down)
```

```cpp
#include <hip/hip_runtime.h>
#include <hip/hip_bf16.h>

typedef float f32x16 __attribute__((ext_vector_type(16)));
typedef float f32x4 __attribute__((ext_vector_type(4)));
typedef float f32x2 __attribute__((ext_vector_type(2)));
typedef __bf16 bf16x2 __attribute__((ext_vector_type(2)));
typedef unsigned u32x16 __attribute__((ext_vector_type(16)));
typedef unsigned u32x4 __attribute__((ext_vector_type(4)));
typedef unsigned u32x2 __attribute__((ext_vector_type(2)));
typedef unsigned short u16;
typedef unsigned long long u64;
#define GAS __attribute__((address_space(1)))
#define DI __device__ __forceinline__

constexpr int S_ = 2048, D_ = 1024, F_ = 3584;
constexpr size_t WS_GEN = 0;
constexpr size_t WS_TMO = 256;
constexpr size_t WS_ACK = 4096;
constexpr size_t WS_FX = 8192;
constexpr size_t WS_FU = 12288;
constexpr size_t WS_FH = 16384;
constexpr size_t WS_FZ = 20480;
constexpr size_t WS_XT = 0x100000;
constexpr size_t WS_STATS = 0x500000;
constexpr size_t WS_URT = 0x600000;
constexpr size_t WS_HT = 0xA00000;
constexpr size_t WS_ZT = 0x1900000;
constexpr size_t WS_PART = 0x1D00000;
constexpr size_t WS_STAMP = 0x2600000;

constexpr unsigned SENTINEL = 0x7f817f81u;
constexpr int LDS_BYTES = 163840;
constexpr int L_UR = 0;
constexpr int L_CE = 18624;
constexpr int L_RING = 64512;
constexpr int L_CO = 130048;
constexpr int L_P0 = L_CO;
constexpr int G1_NST = 4, G1_STAGE = 28928, G1_MATOFF = 14464;
static_assert(G1_NST * G1_STAGE <= L_CO && L_CO + 4 * 8448 <= LDS_BYTES && L_RING + 65536 <= L_CO && L_CE + 4 * 8448 <= L_RING, "lds");

DI float bf2f(unsigned v16) { return __uint_as_float(v16 << 16); }
DI float bflo(unsigned w) { return __uint_as_float(w << 16); }
DI float bfhi(unsigned w) { return __uint_as_float(w & 0xffff0000u); }
DI unsigned pk2(float lo, float hi) { f32x2 v = {lo, hi}; bf16x2 b = __builtin_convertvector(v, bf16x2); return __builtin_bit_cast(unsigned, b); }
DI float rbf(float x) { return bflo(pk2(x, x)); }
struct KArgs { const u16* q; const u16* k; const u16* v; const u16* shk; const u16* ssk; const u16* ssd; const u16* wg; const u16* wu; const u16* wd; u16* out; char* ws; };

DI void st16_sc1(void* p, u32x4 v) { asm volatile("global_store_dwordx4 %0, %1, off sc1\n\ts_nop 1" :: "v"(p), "v"(v) : "memory"); }
DI void st8_sc1(void* p, u32x2 v) { asm volatile("global_store_dwordx2 %0, %1, off sc1\n\ts_nop 1" :: "v"(p), "v"(v) : "memory"); }
DI u64 ld_flag(const char* p) { return __hip_atomic_load((GAS const u64*)p, __ATOMIC_RELAXED, __HIP_MEMORY_SCOPE_AGENT); }
DI void st_flag(char* p, u64 e) { __hip_atomic_store((GAS u64*)p, e, __ATOMIC_RELAXED, __HIP_MEMORY_SCOPE_AGENT); }
DI int lane_id() { int l; asm volatile("v_mbcnt_lo_u32_b32 %0, -1, 0\n\tv_mbcnt_hi_u32_b32 %0, -1, %0" : "=v"(l)); return l; }
DI void wait_flags(const char* p, int n, u64 e, char* ws) {
  const int l = lane_id();
  const char* q = p + (l < n ? l : n - 1) * 8;
  unsigned spins = 0;
  while (!__all(ld_flag(q) == e)) {
    __builtin_amdgcn_s_sleep(1);
    if (++spins > (1u << 20)) { st_flag(ws + WS_TMO, e); break; }
    if ((spins & 1023u) == 0u && __all(ld_flag(ws + WS_TMO) == e)) break;
  }
}
#define LANE_VARS() const int lane = lane_id(), i16 = lane & 15, q4 = i16 >> 2, p4 = i16 & 3, g = lane >> 4; const unsigned l16 = lane * 16; \
  (void)q4; (void)p4; (void)g; (void)l16; (void)i16
#define DRAIN() asm volatile("s_waitcnt vmcnt(0)" ::: "memory")
#define BAR() asm volatile("s_waitcnt lgkmcnt(0)\n\ts_barrier" ::: "memory")
#define STAMP(i) do {} while (0)

__global__ __launch_bounds__(256, 1) void fused_main(KArgs a) {
  extern __shared__ __attribute__((aligned(16))) char lds[];
  const int w = __builtin_amdgcn_readfirstlane(threadIdx.x >> 6);
  const int b = blockIdx.x, xcd = b & 7, j = b >> 3;
  const unsigned ldsb = (unsigned)(size_t)lds;
  STAMP(0);
  const int mt = j & 7, ntile = xcd * 4 + (j >> 3);
  const int ch = (xcd * 32 + j) * 4 + w;
  const int kh = xcd & 1, kg = w >> 1, th = w & 1;
  const int tm = 8 * ((xcd >> 1) & 1) + (j & 7), dn = 4 * (xcd >> 2) + (j >> 3);
  const char* wdb = (const char*)a.wd + (size_t)(1792 * kh + 16 * w) * 2048 + dn * 256;
  const unsigned m0b = ldsb + L_RING + w * 4096;
  const char* wb = (const char*)((w >> 1) ? a.wu : a.wg) + (size_t)(32 * (w & 1)) * 7168 + ntile * 224;
  const unsigned wr0 = ldsb + (w >> 1) * G1_MATOFF + (w & 1) * 7168;
  u64 ep;
  float dsc;
  unsigned tp01, tp23;
  {
    const int tid = threadIdx.x, lane = tid & 63;
    STAMP(15);
    const int tok = lane & 7, cg = lane >> 3, row = b * 8 + tok;
    u32x4 xq[4]; u32x2 gen, tpv; unsigned dsv;
    {
      const u16* xr = a.q + (size_t)row * D_ + 256 * w + 8 * cg;
      const unsigned zero = 0;
      asm volatile("global_load_dwordx4 %0, %7, off\n\tglobal_load_dwordx4 %1, %7, off offset:128\n\t"
                   "global_load_dwordx4 %2, %7, off offset:256\n\tglobal_load_dwordx4 %3, %7, off offset:384\n\t"
                   "global_load_dwordx2 %4, %8, %9 sc1\n\tglobal_load_dwordx2 %5, %8, %10\n\tglobal_load_ushort %6, %8, %11"
                   : "=&v"(xq[0]), "=&v"(xq[1]), "=&v"(xq[2]), "=&v"(xq[3]), "=&v"(gen), "=&v"(tpv), "=&v"(dsv)
                   : "v"(xr), "v"(zero), "s"(a.ws + WS_GEN), "s"(a.ssk + (size_t)ch * S_), "s"(a.ssd + ch) : "memory");
    }
    asm volatile(
      "v_add_u32 v30, 0, %[lane]\n\t"
      "v_mul_u32_u24 v31, 0x124a, v30\n\t"
      "v_lshrrev_b32 v31, 16, v31\n\t"
      "v_mul_u32_u24 v16, 14, v31\n\t"
      "v_sub_u32 v30, v30, v16\n\t"
      "v_add_u32 v30, %[rot], v30\n\t"
      "v_subrev_u32 v16, 14, v30\n\t"
      "v_min_u32 v30, v30, v16\n\t"
      "v_mul_u32_u24 v31, 0x1c00, v31\n\t"
      "v_lshl_add_u32 v16, v30, 4, v31\n\t"
      "v_add_u32 v30, 64, %[lane]\n\t"
      "v_mul_u32_u24 v31, 0x124a, v30\n\t"
      "v_lshrrev_b32 v31, 16, v31\n\t"
      "v_mul_u32_u24 v17, 14, v31\n\t"
      "v_sub_u32 v30, v30, v17\n\t"
      "v_add_u32 v30, %[rot], v30\n\t"
      "v_subrev_u32 v17, 14, v30\n\t"
      "v_min_u32 v30, v30, v17\n\t"
      "v_mul_u32_u24 v31, 0x1c00, v31\n\t"
      "v_lshl_add_u32 v17, v30, 4, v31\n\t"
      "v_add_u32 v30, 128, %[lane]\n\t"
      "v_mul_u32_u24 v31, 0x124a, v30\n\t"
      "v_lshrrev_b32 v31, 16, v31\n\t"
      "v_mul_u32_u24 v18, 14, v31\n\t"
      "v_sub_u32 v30, v30, v18\n\t"
      "v_add_u32 v30, %[rot], v30\n\t"
      "v_subrev_u32 v18, 14, v30\n\t"
      "v_min_u32 v30, v30, v18\n\t"
      "v_mul_u32_u24 v31, 0x1c00, v31\n\t"
      "v_lshl_add_u32 v18, v30, 4, v31\n\t"
      "v_add_u32 v30, 192, %[lane]\n\t"
      "v_mul_u32_u24 v31, 0x124a, v30\n\t"
      "v_lshrrev_b32 v31, 16, v31\n\t"
      "v_mul_u32_u24 v19, 14, v31\n\t"
      "v_sub_u32 v30, v30, v19\n\t"
      "v_add_u32 v30, %[rot], v30\n\t"
      "v_subrev_u32 v19, 14, v30\n\t"
      "v_min_u32 v30, v30, v19\n\t"
      "v_mul_u32_u24 v31, 0x1c00, v31\n\t"
      "v_lshl_add_u32 v19, v30, 4, v31\n\t"
      "v_add_u32 v30, 256, %[lane]\n\t"
      "v_mul_u32_u24 v31, 0x124a, v30\n\t"
      "v_lshrrev_b32 v31, 16, v31\n\t"
      "v_mul_u32_u24 v20, 14, v31\n\t"
      "v_sub_u32 v30, v30, v20\n\t"
      "v_add_u32 v30, %[rot], v30\n\t"
      "v_subrev_u32 v20, 14, v30\n\t"
      "v_min_u32 v30, v30, v20\n\t"
      "v_mul_u32_u24 v31, 0x1c00, v31\n\t"
      "v_lshl_add_u32 v20, v30, 4, v31\n\t"
      "v_add_u32 v30, 320, %[lane]\n\t"
      "v_mul_u32_u24 v31, 0x124a, v30\n\t"
      "v_lshrrev_b32 v31, 16, v31\n\t"
      "v_mul_u32_u24 v21, 14, v31\n\t"
      "v_sub_u32 v30, v30, v21\n\t"
      "v_add_u32 v30, %[rot], v30\n\t"
      "v_subrev_u32 v21, 14, v30\n\t"
      "v_min_u32 v30, v30, v21\n\t"
      "v_mul_u32_u24 v31, 0x1c00, v31\n\t"
      "v_lshl_add_u32 v21, v30, 4, v31\n\t"
      "v_add_u32 v30, 384, %[lane]\n\t"
      "v_mul_u32_u24 v31, 0x124a, v30\n\t"
      "v_lshrrev_b32 v31, 16, v31\n\t"
      "v_mul_u32_u24 v22, 14, v31\n\t"
      "v_sub_u32 v30, v30, v22\n\t"
      "v_add_u32 v30, %[rot], v30\n\t"
      "v_subrev_u32 v22, 14, v30\n\t"
      "v_min_u32 v30, v30, v22\n\t"
      "v_mul_u32_u24 v31, 0x1c00, v31\n\t"
      "v_lshl_add_u32 v22, v30, 4, v31\n\t"
      "s_mov_b64 s[40:41], %[wb]\n\t"
      "s_mov_b32 s46, %[wr0]\n\t"
      "s_mov_b32 m0, %[wr0]\n\t"
      "s_nop 4\n\t"
      "global_load_lds_dwordx4 v16, s[40:41]\n\t"
      "s_add_u32 m0, m0, 0x400\n\t"
      "s_nop 0\n\t"
      "global_load_lds_dwordx4 v17, s[40:41]\n\t"
      "s_add_u32 m0, m0, 0x400\n\t"
      "s_nop 0\n\t"
      "global_load_lds_dwordx4 v18, s[40:41]\n\t"
      "s_add_u32 m0, m0, 0x400\n\t"
      "s_nop 0\n\t"
      "global_load_lds_dwordx4 v19, s[40:41]\n\t"
      "s_add_u32 m0, m0, 0x400\n\t"
      "s_nop 0\n\t"
      "global_load_lds_dwordx4 v20, s[40:41]\n\t"
      "s_add_u32 m0, m0, 0x400\n\t"
      "s_nop 0\n\t"
      "global_load_lds_dwordx4 v21, s[40:41]\n\t"
      "s_add_u32 m0, m0, 0x400\n\t"
      "s_nop 0\n\t"
      "global_load_lds_dwordx4 v22, s[40:41]\n\t"
      "s_add_u32 s40, s40, 0x70000\n\t"
      "s_addc_u32 s41, s41, 0\n\t"
      "s_add_u32 s46, s46, 28928\n\t"
      "s_mov_b32 m0, s46\n\t"
      "s_nop 0\n\t"
      "global_load_lds_dwordx4 v16, s[40:41]\n\t"
      "s_add_u32 m0, m0, 0x400\n\t"
      "s_nop 0\n\t"
      "global_load_lds_dwordx4 v17, s[40:41]\n\t"
      "s_add_u32 m0, m0, 0x400\n\t"
      "s_nop 0\n\t"
      "global_load_lds_dwordx4 v18, s[40:41]\n\t"
      "s_add_u32 m0, m0, 0x400\n\t"
      "s_nop 0\n\t"
      "global_load_lds_dwordx4 v19, s[40:41]\n\t"
      "s_add_u32 m0, m0, 0x400\n\t"
      "s_nop 0\n\t"
      "global_load_lds_dwordx4 v20, s[40:41]\n\t"
      "s_add_u32 m0, m0, 0x400\n\t"
      "s_nop 0\n\t"
      "global_load_lds_dwordx4 v21, s[40:41]\n\t"
      "s_add_u32 m0, m0, 0x400\n\t"
      "s_nop 0\n\t"
      "global_load_lds_dwordx4 v22, s[40:41]\n\t"
      "s_add_u32 s40, s40, 0x70000\n\t"
      "s_addc_u32 s41, s41, 0\n\t"
      "s_add_u32 s46, s46, 28928\n\t"
      "s_mov_b32 m0, s46\n\t"
      "s_nop 0\n\t"
      :: [wb] "s"(wb), [wr0] "s"(wr0), [lane] "v"(lane), [rot] "s"(2 * (ntile & 1)) : "v16", "v17", "v18", "v19", "v20", "v21", "v22", "v30", "v31", "s40", "s41", "s46", "memory", "scc");
    const int ta = b & 15, cb = b >> 4;
    const int kc = tid & 7, tg = tid >> 3, s0 = 128 * ta + 4 * tg, c0 = 64 * cb + 8 * kc;
    u32x4 kr[7], vr[4], wv4[4];
    {
      const __amdgpu_buffer_rsrc_t rk = __builtin_amdgcn_make_buffer_rsrc((void*)a.k, (short)0, S_ * D_ * 2, 0x00020000);
      const __amdgpu_buffer_rsrc_t rv = __builtin_amdgcn_make_buffer_rsrc((void*)a.v, (short)0, S_ * D_ * 2, 0x00020000);
      const unsigned ko = (unsigned)((s0 - 3) * 2048 + c0 * 2);
      unsigned kof[7];
#pragma unroll
      for (int i = 0; i < 7; ++i) kof[i] = ko + i * 2048;
      asm volatile("buffer_load_dwordx4 %0, %7, %14, 0 offen nt\n\tbuffer_load_dwordx4 %1, %8, %14, 0 offen nt\n\tbuffer_load_dwordx4 %2, %9, %14, 0 offen nt\n\t"
                   "buffer_load_dwordx4 %3, %10, %14, 0 offen nt\n\tbuffer_load_dwordx4 %4, %11, %14, 0 offen nt\n\tbuffer_load_dwordx4 %5, %12, %14, 0 offen nt\n\t"
                   "buffer_load_dwordx4 %6, %13, %14, 0 offen nt"
                   : "=&v"(kr[0]), "=&v"(kr[1]), "=&v"(kr[2]), "=&v"(kr[3]), "=&v"(kr[4]), "=&v"(kr[5]), "=&v"(kr[6])
                   : "v"(kof[0]), "v"(kof[1]), "v"(kof[2]), "v"(kof[3]), "v"(kof[4]), "v"(kof[5]), "v"(kof[6]), "s"(rk) : "memory");
      asm volatile("buffer_load_dwordx4 %0, %4, %8, 0 offen nt\n\tbuffer_load_dwordx4 %1, %5, %8, 0 offen nt\n\t"
                   "buffer_load_dwordx4 %2, %6, %8, 0 offen nt\n\tbuffer_load_dwordx4 %3, %7, %8, 0 offen nt"
                   : "=&v"(vr[0]), "=&v"(vr[1]), "=&v"(vr[2]), "=&v"(vr[3])
                   : "v"(kof[3]), "v"(kof[4]), "v"(kof[5]), "v"(kof[6]), "s"(rv) : "memory");
      const u16* sp = a.shk + c0 * 4;
      asm volatile("global_load_dwordx4 %0, %4, off\n\tglobal_load_dwordx4 %1, %4, off offset:16\n\t"
                   "global_load_dwordx4 %2, %4, off offset:32\n\tglobal_load_dwordx4 %3, %4, off offset:48"
                   : "=&v"(wv4[0]), "=&v"(wv4[1]), "=&v"(wv4[2]), "=&v"(wv4[3]) : "v"(sp) : "memory");
    }
    asm volatile("s_waitcnt vmcnt(29)" : "+v"(xq[0]), "+v"(xq[1]), "+v"(xq[2]), "+v"(xq[3]), "+v"(gen), "+v"(tpv), "+v"(dsv) :: "memory");
    STAMP(12);
    dsc = __uint_as_float((unsigned)__builtin_amdgcn_readfirstlane((int)dsv) << 16);
    tp01 = __builtin_amdgcn_readfirstlane((int)tpv[0]); tp23 = __builtin_amdgcn_readfirstlane((int)tpv[1]);
    ep = ((u64)(unsigned)__builtin_amdgcn_readfirstlane((int)gen[1]) << 32 | (unsigned)__builtin_amdgcn_readfirstlane((int)gen[0])) + 1;
    if (tid == 0) st_flag(a.ws + WS_ACK + b * 8, ep);
    float x[32];
#pragma unroll
    for (int i = 0; i < 4; ++i)
#pragma unroll
      for (int e = 0; e < 4; ++e) { x[8 * i + 2 * e] = bflo(xq[i][e]); x[8 * i + 2 * e + 1] = bfhi(xq[i][e]); }
    f32x2 s1 = {0.f, 0.f};
#pragma unroll
    for (int i = 0; i < 16; ++i) s1 += (f32x2){x[2 * i], x[2 * i + 1]};
    float mu = (s1[0] + s1[1]) * (1.0f / 32.0f);
    f32x2 s2 = {0.f, 0.f};
#pragma unroll
    for (int i = 0; i < 16; ++i) { const f32x2 d = (f32x2){x[2 * i], x[2 * i + 1]} - mu; s2 += d * d; }
    float m2 = s2[0] + s2[1];
    {
      float mo = __builtin_bit_cast(float, __builtin_amdgcn_update_dpp(0, __builtin_bit_cast(int, mu), 0x128  , 0xf, 0xf, false));
      float qo = __builtin_bit_cast(float, __builtin_amdgcn_update_dpp(0, __builtin_bit_cast(int, m2), 0x128, 0xf, 0xf, false));
      m2 = m2 + qo + (mu - mo) * (mu - mo) * 16.0f; mu = (mu + mo) * 0.5f;
      auto a16 = __builtin_amdgcn_permlane16_swap(__float_as_uint(mu), __float_as_uint(mu), false, false);
      auto b16 = __builtin_amdgcn_permlane16_swap(__float_as_uint(m2), __float_as_uint(m2), false, false);
      float ma = __uint_as_float(a16[0]), mb_ = __uint_as_float(a16[1]);
      m2 = __uint_as_float(b16[0]) + __uint_as_float(b16[1]) + (ma - mb_) * (ma - mb_) * 32.0f; mu = (ma + mb_) * 0.5f;
      auto a32 = __builtin_amdgcn_permlane32_swap(__float_as_uint(mu), __float_as_uint(mu), false, false);
      auto b32 = __builtin_amdgcn_permlane32_swap(__float_as_uint(m2), __float_as_uint(m2), false, false);
      ma = __uint_as_float(a32[0]); mb_ = __uint_as_float(a32[1]);
      m2 = __uint_as_float(b32[0]) + __uint_as_float(b32[1]) + (ma - mb_) * (ma - mb_) * 64.0f; mu = (ma + mb_) * 0.5f;
    }
    f32x2* red = (f32x2*)(lds + L_P0 + 64 * 272);
    if (lane < 8) red[w * 8 + lane] = (f32x2){mu, m2};
    STAMP(13);
    BAR();
    STAMP(14);
    float mean, var;
    {
      const f32x2 r0 = red[tok], r1 = red[8 + tok], r2 = red[16 + tok], r3 = red[24 + tok];
      mean = ((r0[0] + r1[0]) + (r2[0] + r3[0])) * 0.25f;
      const float d0 = r0[0] - mean, d1 = r1[0] - mean, d2 = r2[0] - mean, d3 = r3[0] - mean;
      var = (((r0[1] + r1[1]) + (r2[1] + r3[1])) + 256.0f * ((d0 * d0 + d1 * d1) + (d2 * d2 + d3 * d3))) * (1.0f / D_);
    }
    const float mb = rbf(mean), sd = rbf(__builtin_sqrtf(rbf(rbf(var) + 1e-5f)));
    const float rs = 1.0f / sd;
    if (tid < 8) st8_sc1(a.ws + WS_STATS + row * 8, (u32x2){__float_as_uint(mb), __float_as_uint(rs)});
    char* xt = a.ws + WS_XT + (size_t)(row >> 4) * 32768 + (row & 15) * 16;
#pragma unroll
    for (int i = 0; i < 4; ++i) {
      unsigned o[4];
#pragma unroll
      for (int e = 0; e < 4; ++e) {
        const unsigned d = pk2(x[8 * i + 2 * e] - mb, x[8 * i + 2 * e + 1] - mb);
        o[e] = pk2(bflo(d) * rs, bfhi(d) * rs);
      }
      const auto s0_ = __builtin_amdgcn_permlane16_swap(o[0], o[2], false, false);
      const auto s1_ = __builtin_amdgcn_permlane16_swap(o[1], o[3], false, false);
      const int c = 8 * i + cg, s = 8 * w + (c >> 2), g = 2 * (c & 1) + ((c >> 1) & 1);
      st16_sc1(xt + s * 1024 + g * 256, (u32x4){s0_[0], s1_[0], s0_[1], s1_[1]});
    }
    STAMP(9);
    char* sm = lds + L_P0;
    asm volatile("s_waitcnt vmcnt(4)" : "+v"(kr[0]), "+v"(kr[1]), "+v"(kr[2]), "+v"(kr[3]), "+v"(kr[4]), "+v"(kr[5]), "+v"(kr[6]), "+v"(vr[0]), "+v"(vr[1]),
                 "+v"(vr[2]), "+v"(vr[3]), "+v"(wv4[0]), "+v"(wv4[1]), "+v"(wv4[2]), "+v"(wv4[3]) :: "memory");
    const unsigned wv[16] = {wv4[0][0], wv4[0][1], wv4[0][2], wv4[0][3], wv4[1][0], wv4[1][1], wv4[1][2], wv4[1][3],
                             wv4[2][0], wv4[2][1], wv4[2][2], wv4[2][3], wv4[3][0], wv4[3][1], wv4[3][2], wv4[3][3]};
#pragma unroll
    for (int c = 0; c < 8; ++c) {
      const float w0 = bflo(wv[2 * c]), w1 = bfhi(wv[2 * c]), w2 = bflo(wv[2 * c + 1]), w3 = bfhi(wv[2 * c + 1]);
      float kk[7], u[4];
#pragma unroll
      for (int i = 0; i < 7; ++i) kk[i] = (c & 1) ? bfhi(kr[i][c >> 1]) : bflo(kr[i][c >> 1]);
#pragma unroll
      for (int i = 0; i < 4; ++i) {
        const float ks = rbf(kk[i] * w3 + kk[i + 1] * w2 + kk[i + 2] * w1 + kk[i + 3] * w0);
        u[i] = ks * ((c & 1) ? bfhi(vr[i][c >> 1]) : bflo(vr[i][c >> 1]));
      }
      *(u32x2*)(sm + (8 * kc + c) * 272 + (124 - 4 * tg) * 2) = (u32x2){pk2(u[3], u[2]), pk2(u[1], u[0])};
      if (c == 0) {
        DRAIN();
        BAR();
        if (tid == 0) st_flag(a.ws + WS_FX + b * 8, ep);
      }
    }
    STAMP(10);
    wait_flags(a.ws + WS_FX + mt * 256, 32, ep, a.ws);
    if (b == 0 && w == 3) {
#pragma unroll 1
      for (int i = 0; i < 4; ++i) wait_flags(a.ws + WS_ACK + i * 512, 64, ep, a.ws);
      if (lane == 0) st_flag(a.ws + WS_GEN, ep);
    }
    asm volatile("s_waitcnt lgkmcnt(0)" ::: "memory");
  }
  STAMP(11);
  const unsigned eplo = (unsigned)ep, ephi = (unsigned)(ep >> 32);
  f32x16 y0, y1;
  const char* hb2 = a.ws + WS_HT + ((size_t)(tm * 8 + 4 * th) * 112 + 56 * kh + kg) * 1024;
  const char* chr = a.ws + WS_FH + ((tm >> 1) * 32 + 16 * kh) * 8;
  {
    f32x16 acc[14];
    {
      LANE_VARS();
      const char* xb = a.ws + WS_XT + (size_t)(mt * 16 + 4 * w) * 32768;
      const __amdgpu_buffer_rsrc_t wsrd = __builtin_amdgcn_make_buffer_rsrc((void*)((w & 1) ? a.wu : a.wg), (short)0, D_ * F_ * 2, 0x00020000);
      const unsigned wpo = (unsigned)((4 * j + w) >> 1) * 7168u + xcd * 896u;
      const __amdgpu_buffer_rsrc_t srd = __builtin_amdgcn_make_buffer_rsrc((void*)(a.ssk + (size_t)ch * S_), (short)0, 4096, 0x00020000);
      const char* ub = a.ws + WS_URT + (size_t)ch * 4096;
      const unsigned mu = ldsb + L_UR + w * 4608, mc = ldsb + L_CO + w * 8448 + 32;
      const char* hb = a.ws + WS_HT + ((size_t)(mt * 16 + 4 * w) * 112 + ((7 * ntile) >> 1)) * 1024;
      const char* hbq = hb + ((ntile & 1) ? 1024 : 0);
      const char* hbs = hb + ((ntile & 1) ? 8 : 3072);
      const char* fu = a.ws + WS_FU + ((b >> 4) * 16 + (b & 15)) * 8;
      const unsigned kvl = ldsb + L_P0 + (4 * w + (lane >> 4)) * 272 + (lane & 15) * 16;
      const unsigned kvo = (4 * w + (lane >> 4)) * 4096 + (lane & 15) * 16;
      const char* kvb = a.ws + WS_URT + ((size_t)(64 * (b >> 4)) * S_ + (1920 - 128 * (b & 15))) * 2;
      const char* fur = a.ws + WS_FU + (ch >> 6) * 128;
      const char* tmo = a.ws + WS_TMO;
      asm volatile(
      "s_mov_b64 s[42:43], %[xb]\n\t"
      "v_lshlrev_b32 v30, 4, %[lane]\n\t"
      "v_add_u32 v26, 0x0, v30\n\t"
      "v_add_u32 v27, 0x8000, v30\n\t"
      "v_add_u32 v28, 0x10000, v30\n\t"
      "v_add_u32 v29, 0x18000, v30\n\t"
      "global_load_dwordx4 a[112:115], v26, s[42:43] offset:0 sc1\n\t"
      "global_load_dwordx4 a[116:119], v27, s[42:43] offset:0 sc1\n\t"
      "global_load_dwordx4 a[120:123], v28, s[42:43] offset:0 sc1\n\t"
      "global_load_dwordx4 a[124:127], v29, s[42:43] offset:0 sc1\n\t"
      "global_load_dwordx4 a[128:131], v26, s[42:43] offset:1024 sc1\n\t"
      "global_load_dwordx4 a[132:135], v27, s[42:43] offset:1024 sc1\n\t"
      "global_load_dwordx4 a[136:139], v28, s[42:43] offset:1024 sc1\n\t"
      "global_load_dwordx4 a[140:143], v29, s[42:43] offset:1024 sc1\n\t"
      "s_add_u32 s42, s42, 0x800\n\t"
      "s_addc_u32 s43, s43, 0\n\t"
      "s_nop 0\n\t"
      "s_mov_b64 s[40:41], %[wb]\n\t"
      "s_add_u32 s46, %[wr0], 57856\n\t"
      "s_mov_b32 s49, %[wr0]\n\t"
      "s_add_u32 s50, s49, 115712\n\t"
      "s_mov_b32 s47, 0\n\t"
      "v_add_u32 v30, 0, %[lane]\n\t"
      "v_mul_u32_u24 v31, 0x124a, v30\n\t"
      "v_lshrrev_b32 v31, 16, v31\n\t"
      "v_mul_u32_u24 v16, 14, v31\n\t"
      "v_sub_u32 v30, v30, v16\n\t"
      "v_add_u32 v30, %[rot], v30\n\t"
      "v_subrev_u32 v16, 14, v30\n\t"
      "v_min_u32 v30, v30, v16\n\t"
      "v_mul_u32_u24 v31, 0x1c00, v31\n\t"
      "v_lshl_add_u32 v16, v30, 4, v31\n\t"
      "v_add_u32 v30, 64, %[lane]\n\t"
      "v_mul_u32_u24 v31, 0x124a, v30\n\t"
      "v_lshrrev_b32 v31, 16, v31\n\t"
      "v_mul_u32_u24 v17, 14, v31\n\t"
      "v_sub_u32 v30, v30, v17\n\t"
      "v_add_u32 v30, %[rot], v30\n\t"
      "v_subrev_u32 v17, 14, v30\n\t"
      "v_min_u32 v30, v30, v17\n\t"
      "v_mul_u32_u24 v31, 0x1c00, v31\n\t"
      "v_lshl_add_u32 v17, v30, 4, v31\n\t"
      "v_add_u32 v30, 128, %[lane]\n\t"
      "v_mul_u32_u24 v31, 0x124a, v30\n\t"
      "v_lshrrev_b32 v31, 16, v31\n\t"
      "v_mul_u32_u24 v18, 14, v31\n\t"
      "v_sub_u32 v30, v30, v18\n\t"
      "v_add_u32 v30, %[rot], v30\n\t"
      "v_subrev_u32 v18, 14, v30\n\t"
      "v_min_u32 v30, v30, v18\n\t"
      "v_mul_u32_u24 v31, 0x1c00, v31\n\t"
      "v_lshl_add_u32 v18, v30, 4, v31\n\t"
      "v_add_u32 v30, 192, %[lane]\n\t"
      "v_mul_u32_u24 v31, 0x124a, v30\n\t"
      "v_lshrrev_b32 v31, 16, v31\n\t"
      "v_mul_u32_u24 v19, 14, v31\n\t"
      "v_sub_u32 v30, v30, v19\n\t"
      "v_add_u32 v30, %[rot], v30\n\t"
      "v_subrev_u32 v19, 14, v30\n\t"
      "v_min_u32 v30, v30, v19\n\t"
      "v_mul_u32_u24 v31, 0x1c00, v31\n\t"
      "v_lshl_add_u32 v19, v30, 4, v31\n\t"
      "v_add_u32 v30, 256, %[lane]\n\t"
      "v_mul_u32_u24 v31, 0x124a, v30\n\t"
      "v_lshrrev_b32 v31, 16, v31\n\t"
      "v_mul_u32_u24 v20, 14, v31\n\t"
      "v_sub_u32 v30, v30, v20\n\t"
      "v_add_u32 v30, %[rot], v30\n\t"
      "v_subrev_u32 v20, 14, v30\n\t"
      "v_min_u32 v30, v30, v20\n\t"
      "v_mul_u32_u24 v31, 0x1c00, v31\n\t"
      "v_lshl_add_u32 v20, v30, 4, v31\n\t"
      "v_add_u32 v30, 320, %[lane]\n\t"
      "v_mul_u32_u24 v31, 0x124a, v30\n\t"
      "v_lshrrev_b32 v31, 16, v31\n\t"
      "v_mul_u32_u24 v21, 14, v31\n\t"
      "v_sub_u32 v30, v30, v21\n\t"
      "v_add_u32 v30, %[rot], v30\n\t"
      "v_subrev_u32 v21, 14, v30\n\t"
      "v_min_u32 v30, v30, v21\n\t"
      "v_mul_u32_u24 v31, 0x1c00, v31\n\t"
      "v_lshl_add_u32 v21, v30, 4, v31\n\t"
      "v_add_u32 v30, 384, %[lane]\n\t"
      "v_mul_u32_u24 v31, 0x124a, v30\n\t"
      "v_lshrrev_b32 v31, 16, v31\n\t"
      "v_mul_u32_u24 v22, 14, v31\n\t"
      "v_sub_u32 v30, v30, v22\n\t"
      "v_add_u32 v30, %[rot], v30\n\t"
      "v_subrev_u32 v22, 14, v30\n\t"
      "v_min_u32 v30, v30, v22\n\t"
      "v_mul_u32_u24 v31, 0x1c00, v31\n\t"
      "v_lshl_add_u32 v22, v30, 4, v31\n\t"
      "v_lshrrev_b32 v30, 2, %[lane]\n\t"
      "v_mul_u32_u24 v30, 224, v30\n\t"
      "v_and_b32 v31, 3, %[lane]\n\t"
      "v_lshl_add_u32 v30, v31, 3, v30\n\t"
      "v_add_u32 v25, %[ldsb], v30\n\t"
      "v_min_u32 v14, 6, %[lane]\n\t"
      "v_lshlrev_b32 v14, 7, v14\n\t"
      "s_add_u32 s53, %[wpo], 2752512\n\t"
      "s_mov_b64 s[64:65], %[kvb]\n\t"
      "s_mov_b64 s[62:63], %[pma]\n\t"
      "v_mov_b32 v30, %[sent]\n\t"
      "v_accvgpr_write_b32 a240, v30\n\t"
      "v_accvgpr_write_b32 a241, v30\n\t"
      "v_accvgpr_write_b32 a242, v30\n\t"
      "v_accvgpr_write_b32 a243, v30\n\t"
      "s_nop 4\n\t"
      "s_mov_b32 m0, s46\n\t"
      "s_nop 0\n\t"
      "global_load_lds_dwordx4 v16, s[40:41]\n\t"
      "s_add_u32 m0, m0, 0x400\n\t"
      "s_nop 0\n\t"
      "global_load_lds_dwordx4 v17, s[40:41]\n\t"
      "s_add_u32 m0, m0, 0x400\n\t"
      "s_nop 0\n\t"
      "global_load_lds_dwordx4 v18, s[40:41]\n\t"
      "s_add_u32 m0, m0, 0x400\n\t"
      "s_nop 0\n\t"
      "global_load_lds_dwordx4 v19, s[40:41]\n\t"
      "s_add_u32 m0, m0, 0x400\n\t"
      "s_nop 0\n\t"
      "global_load_lds_dwordx4 v20, s[40:41]\n\t"
      "s_add_u32 m0, m0, 0x400\n\t"
      "s_nop 0\n\t"
      "global_load_lds_dwordx4 v21, s[40:41]\n\t"
      "s_add_u32 m0, m0, 0x400\n\t"
      "s_nop 0\n\t"
      "global_load_lds_dwordx4 v22, s[40:41]\n\t"
      "s_add_u32 s40, s40, 0x70000\n\t"
      "s_addc_u32 s41, s41, 0\n\t"
      "s_add_u32 s46, s46, 28928\n\t"
      "s_cmp_eq_u32 s46, s50\n\t"
      "s_cselect_b32 s46, s49, s46\n\t"
      "s_mov_b32 m0, s46\n\t"
      "s_nop 0\n\t"
      "s_add_u32 s54, %[wpo], 1376256\n\t"
      "buffer_load_dword v30, v14, %[wsrd], s54 offen\n\t"
      "s_add_u32 s54, %[wpo], 1835008\n\t"
      "buffer_load_dword v30, v14, %[wsrd], s54 offen\n\t"
      "s_add_u32 s54, %[wpo], 2293760\n\t"
      "buffer_load_dword v30, v14, %[wsrd], s54 offen\n\t"
      "v_mov_b32 v32, 0\n\t"
      "v_mov_b32 v33, 0\n\t"
      "v_mov_b32 v34, 0\n\t"
      "v_mov_b32 v35, 0\n\t"
      "v_mov_b32 v36, 0\n\t"
      "v_mov_b32 v37, 0\n\t"
      "v_mov_b32 v38, 0\n\t"
      "v_mov_b32 v39, 0\n\t"
      "v_mov_b32 v40, 0\n\t"
      "v_mov_b32 v41, 0\n\t"
      "v_mov_b32 v42, 0\n\t"
      "v_mov_b32 v43, 0\n\t"
      "v_mov_b32 v44, 0\n\t"
      "v_mov_b32 v45, 0\n\t"
      "v_mov_b32 v46, 0\n\t"
      "v_mov_b32 v47, 0\n\t"
      "v_mov_b32 v48, 0\n\t"
      "v_mov_b32 v49, 0\n\t"
      "v_mov_b32 v50, 0\n\t"
      "v_mov_b32 v51, 0\n\t"
      "v_mov_b32 v52, 0\n\t"
      "v_mov_b32 v53, 0\n\t"
      "v_mov_b32 v54, 0\n\t"
      "v_mov_b32 v55, 0\n\t"
      "v_mov_b32 v56, 0\n\t"
      "v_mov_b32 v57, 0\n\t"
      "v_mov_b32 v58, 0\n\t"
      "v_mov_b32 v59, 0\n\t"
      "v_mov_b32 v60, 0\n\t"
      "v_mov_b32 v61, 0\n\t"
      "v_mov_b32 v62, 0\n\t"
      "v_mov_b32 v63, 0\n\t"
      "v_mov_b32 v64, 0\n\t"
      "v_mov_b32 v65, 0\n\t"
      "v_mov_b32 v66, 0\n\t"
      "v_mov_b32 v67, 0\n\t"
      "v_mov_b32 v68, 0\n\t"
      "v_mov_b32 v69, 0\n\t"
      "v_mov_b32 v70, 0\n\t"
      "v_mov_b32 v71, 0\n\t"
      "v_mov_b32 v72, 0\n\t"
      "v_mov_b32 v73, 0\n\t"
      "v_mov_b32 v74, 0\n\t"
      "v_mov_b32 v75, 0\n\t"
      "v_mov_b32 v76, 0\n\t"
      "v_mov_b32 v77, 0\n\t"
      "v_mov_b32 v78, 0\n\t"
      "v_mov_b32 v79, 0\n\t"
      "v_mov_b32 v80, 0\n\t"
      "v_mov_b32 v81, 0\n\t"
      "v_mov_b32 v82, 0\n\t"
      "v_mov_b32 v83, 0\n\t"
      "v_mov_b32 v84, 0\n\t"
      "v_mov_b32 v85, 0\n\t"
      "v_mov_b32 v86, 0\n\t"
      "v_mov_b32 v87, 0\n\t"
      "v_mov_b32 v88, 0\n\t"
      "v_mov_b32 v89, 0\n\t"
      "v_mov_b32 v90, 0\n\t"
      "v_mov_b32 v91, 0\n\t"
      "v_mov_b32 v92, 0\n\t"
      "v_mov_b32 v93, 0\n\t"
      "v_mov_b32 v94, 0\n\t"
      "v_mov_b32 v95, 0\n\t"
      "v_mov_b32 v96, 0\n\t"
      "v_mov_b32 v97, 0\n\t"
      "v_mov_b32 v98, 0\n\t"
      "v_mov_b32 v99, 0\n\t"
      "v_mov_b32 v100, 0\n\t"
      "v_mov_b32 v101, 0\n\t"
      "v_mov_b32 v102, 0\n\t"
      "v_mov_b32 v103, 0\n\t"
      "v_mov_b32 v104, 0\n\t"
      "v_mov_b32 v105, 0\n\t"
      "v_mov_b32 v106, 0\n\t"
      "v_mov_b32 v107, 0\n\t"
      "v_mov_b32 v108, 0\n\t"
      "v_mov_b32 v109, 0\n\t"
      "v_mov_b32 v110, 0\n\t"
      "v_mov_b32 v111, 0\n\t"
      "v_mov_b32 v112, 0\n\t"
      "v_mov_b32 v113, 0\n\t"
      "v_mov_b32 v114, 0\n\t"
      "v_mov_b32 v115, 0\n\t"
      "v_mov_b32 v116, 0\n\t"
      "v_mov_b32 v117, 0\n\t"
      "v_mov_b32 v118, 0\n\t"
      "v_mov_b32 v119, 0\n\t"
      "v_mov_b32 v120, 0\n\t"
      "v_mov_b32 v121, 0\n\t"
      "v_mov_b32 v122, 0\n\t"
      "v_mov_b32 v123, 0\n\t"
      "v_mov_b32 v124, 0\n\t"
      "v_mov_b32 v125, 0\n\t"
      "v_mov_b32 v126, 0\n\t"
      "v_mov_b32 v127, 0\n\t"
      "v_mov_b32 v128, 0\n\t"
      "v_mov_b32 v129, 0\n\t"
      "v_mov_b32 v130, 0\n\t"
      "v_mov_b32 v131, 0\n\t"
      "v_mov_b32 v132, 0\n\t"
      "v_mov_b32 v133, 0\n\t"
      "v_mov_b32 v134, 0\n\t"
      "v_mov_b32 v135, 0\n\t"
      "v_mov_b32 v136, 0\n\t"
      "v_mov_b32 v137, 0\n\t"
      "v_mov_b32 v138, 0\n\t"
      "v_mov_b32 v139, 0\n\t"
      "v_mov_b32 v140, 0\n\t"
      "v_mov_b32 v141, 0\n\t"
      "v_mov_b32 v142, 0\n\t"
      "v_mov_b32 v143, 0\n\t"
      "v_mov_b32 v144, 0\n\t"
      "v_mov_b32 v145, 0\n\t"
      "v_mov_b32 v146, 0\n\t"
      "v_mov_b32 v147, 0\n\t"
      "v_mov_b32 v148, 0\n\t"
      "v_mov_b32 v149, 0\n\t"
      "v_mov_b32 v150, 0\n\t"
      "v_mov_b32 v151, 0\n\t"
      "v_mov_b32 v152, 0\n\t"
      "v_mov_b32 v153, 0\n\t"
      "v_mov_b32 v154, 0\n\t"
      "v_mov_b32 v155, 0\n\t"
      "v_mov_b32 v156, 0\n\t"
      "v_mov_b32 v157, 0\n\t"
      "v_mov_b32 v158, 0\n\t"
      "v_mov_b32 v159, 0\n\t"
      "v_mov_b32 v160, 0\n\t"
      "v_mov_b32 v161, 0\n\t"
      "v_mov_b32 v162, 0\n\t"
      "v_mov_b32 v163, 0\n\t"
      "v_mov_b32 v164, 0\n\t"
      "v_mov_b32 v165, 0\n\t"
      "v_mov_b32 v166, 0\n\t"
      "v_mov_b32 v167, 0\n\t"
      "v_mov_b32 v168, 0\n\t"
      "v_mov_b32 v169, 0\n\t"
      "v_mov_b32 v170, 0\n\t"
      "v_mov_b32 v171, 0\n\t"
      "v_mov_b32 v172, 0\n\t"
      "v_mov_b32 v173, 0\n\t"
      "v_mov_b32 v174, 0\n\t"
      "v_mov_b32 v175, 0\n\t"
      "v_mov_b32 v176, 0\n\t"
      "v_mov_b32 v177, 0\n\t"
      "v_mov_b32 v178, 0\n\t"
      "v_mov_b32 v179, 0\n\t"
      "v_mov_b32 v180, 0\n\t"
      "v_mov_b32 v181, 0\n\t"
      "v_mov_b32 v182, 0\n\t"
      "v_mov_b32 v183, 0\n\t"
      "v_mov_b32 v184, 0\n\t"
      "v_mov_b32 v185, 0\n\t"
      "v_mov_b32 v186, 0\n\t"
      "v_mov_b32 v187, 0\n\t"
      "v_mov_b32 v188, 0\n\t"
      "v_mov_b32 v189, 0\n\t"
      "v_mov_b32 v190, 0\n\t"
      "v_mov_b32 v191, 0\n\t"
      "v_mov_b32 v192, 0\n\t"
      "v_mov_b32 v193, 0\n\t"
      "v_mov_b32 v194, 0\n\t"
      "v_mov_b32 v195, 0\n\t"
      "v_mov_b32 v196, 0\n\t"
      "v_mov_b32 v197, 0\n\t"
      "v_mov_b32 v198, 0\n\t"
      "v_mov_b32 v199, 0\n\t"
      "v_mov_b32 v200, 0\n\t"
      "v_mov_b32 v201, 0\n\t"
      "v_mov_b32 v202, 0\n\t"
      "v_mov_b32 v203, 0\n\t"
      "v_mov_b32 v204, 0\n\t"
      "v_mov_b32 v205, 0\n\t"
      "v_mov_b32 v206, 0\n\t"
      "v_mov_b32 v207, 0\n\t"
      "v_mov_b32 v208, 0\n\t"
      "v_mov_b32 v209, 0\n\t"
      "v_mov_b32 v210, 0\n\t"
      "v_mov_b32 v211, 0\n\t"
      "v_mov_b32 v212, 0\n\t"
      "v_mov_b32 v213, 0\n\t"
      "v_mov_b32 v214, 0\n\t"
      "v_mov_b32 v215, 0\n\t"
      "v_mov_b32 v216, 0\n\t"
      "v_mov_b32 v217, 0\n\t"
      "v_mov_b32 v218, 0\n\t"
      "v_mov_b32 v219, 0\n\t"
      "v_mov_b32 v220, 0\n\t"
      "v_mov_b32 v221, 0\n\t"
      "v_mov_b32 v222, 0\n\t"
      "v_mov_b32 v223, 0\n\t"
      "v_mov_b32 v224, 0\n\t"
      "v_mov_b32 v225, 0\n\t"
      "v_mov_b32 v226, 0\n\t"
      "v_mov_b32 v227, 0\n\t"
      "v_mov_b32 v228, 0\n\t"
      "v_mov_b32 v229, 0\n\t"
      "v_mov_b32 v230, 0\n\t"
      "v_mov_b32 v231, 0\n\t"
      "v_mov_b32 v232, 0\n\t"
      "v_mov_b32 v233, 0\n\t"
      "v_mov_b32 v234, 0\n\t"
      "v_mov_b32 v235, 0\n\t"
      "v_mov_b32 v236, 0\n\t"
      "v_mov_b32 v237, 0\n\t"
      "v_mov_b32 v238, 0\n\t"
      "v_mov_b32 v239, 0\n\t"
      "v_mov_b32 v240, 0\n\t"
      "v_mov_b32 v241, 0\n\t"
      "v_mov_b32 v242, 0\n\t"
      "v_mov_b32 v243, 0\n\t"
      "v_mov_b32 v244, 0\n\t"
      "v_mov_b32 v245, 0\n\t"
      "v_mov_b32 v246, 0\n\t"
      "v_mov_b32 v247, 0\n\t"
      "v_mov_b32 v248, 0\n\t"
      "v_mov_b32 v249, 0\n\t"
      "v_mov_b32 v250, 0\n\t"
      "v_mov_b32 v251, 0\n\t"
      "v_mov_b32 v252, 0\n\t"
      "v_mov_b32 v253, 0\n\t"
      "v_mov_b32 v254, 0\n\t"
      "v_mov_b32 v255, 0\n\t"
      "s_barrier\n\t"
      "v_add_u32 v23, s47, v25\n\t"
      "ds_read_b64_tr_b16 a[0:1], v23 offset:0\n\t"
      "ds_read_b64_tr_b16 a[2:3], v23 offset:3584\n\t"
      "ds_read_b64_tr_b16 a[4:5], v23 offset:32\n\t"
      "ds_read_b64_tr_b16 a[6:7], v23 offset:3616\n\t"
      "ds_read_b64_tr_b16 a[8:9], v23 offset:64\n\t"
      "ds_read_b64_tr_b16 a[10:11], v23 offset:3648\n\t"
      "ds_read_b64_tr_b16 a[12:13], v23 offset:96\n\t"
      "ds_read_b64_tr_b16 a[14:15], v23 offset:3680\n\t"
      "ds_read_b64_tr_b16 a[16:17], v23 offset:128\n\t"
      "ds_read_b64_tr_b16 a[18:19], v23 offset:3712\n\t"
      "ds_read_b64_tr_b16 a[20:21], v23 offset:160\n\t"
      "ds_read_b64_tr_b16 a[22:23], v23 offset:3744\n\t"
      "ds_read_b64_tr_b16 a[24:25], v23 offset:192\n\t"
      "ds_read_b64_tr_b16 a[26:27], v23 offset:3776\n\t"
      "ds_read_b64_tr_b16 a[28:29], v23 offset:14464\n\t"
      "ds_read_b64_tr_b16 a[30:31], v23 offset:18048\n\t"
      "ds_read_b64_tr_b16 a[32:33], v23 offset:14496\n\t"
      "ds_read_b64_tr_b16 a[34:35], v23 offset:18080\n\t"
      "ds_read_b64_tr_b16 a[36:37], v23 offset:14528\n\t"
      "ds_read_b64_tr_b16 a[38:39], v23 offset:18112\n\t"
      "ds_read_b64_tr_b16 a[40:41], v23 offset:14560\n\t"
      "ds_read_b64_tr_b16 a[42:43], v23 offset:18144\n\t"
      "ds_read_b64_tr_b16 a[44:45], v23 offset:14592\n\t"
      "ds_read_b64_tr_b16 a[46:47], v23 offset:18176\n\t"
      "ds_read_b64_tr_b16 a[48:49], v23 offset:14624\n\t"
      "ds_read_b64_tr_b16 a[50:51], v23 offset:18208\n\t"
      "ds_read_b64_tr_b16 a[52:53], v23 offset:14656\n\t"
      "ds_read_b64_tr_b16 a[54:55], v23 offset:18240\n\t"
      "s_waitcnt vmcnt(14)\n\t"
      "s_waitcnt lgkmcnt(0)\n\t"
      "v_mfma_f32_16x16x32_bf16 v[32:35], a[0:3], a[112:115], v[32:35]\n\t"
      "global_load_dwordx4 a[144:147], v26, s[42:43] offset:0 sc1\n\t"
      "v_mfma_f32_16x16x32_bf16 v[36:39], a[0:3], a[116:119], v[36:39]\n\t"
      "ds_read_b64_tr_b16 a[56:57], v23 offset:7168\n\t"
      "v_mfma_f32_16x16x32_bf16 v[40:43], a[0:3], a[120:123], v[40:43]\n\t"
      "ds_read_b64_tr_b16 a[58:59], v23 offset:10752\n\t"
      "v_mfma_f32_16x16x32_bf16 v[44:47], a[0:3], a[124:127], v[44:47]\n\t"
      "v_mfma_f32_16x16x32_bf16 v[48:51], a[4:7], a[112:115], v[48:51]\n\t"
      "ds_read_b64_tr_b16 a[60:61], v23 offset:7200\n\t"
      "v_mfma_f32_16x16x32_bf16 v[52:55], a[4:7], a[116:119], v[52:55]\n\t"
      "v_mfma_f32_16x16x32_bf16 v[56:59], a[4:7], a[120:123], v[56:59]\n\t"
      "ds_read_b64_tr_b16 a[62:63], v23 offset:10784\n\t"
      "global_load_dwordx4 a[148:151], v27, s[42:43] offset:0 sc1\n\t"
      "v_mfma_f32_16x16x32_bf16 v[60:63], a[4:7], a[124:127], v[60:63]\n\t"
      "v_mfma_f32_16x16x32_bf16 v[64:67], a[8:11], a[112:115], v[64:67]\n\t"
      "ds_read_b64_tr_b16 a[64:65], v23 offset:7232\n\t"
      "v_mfma_f32_16x16x32_bf16 v[68:71], a[8:11], a[116:119], v[68:71]\n\t"
      "v_mfma_f32_16x16x32_bf16 v[72:75], a[8:11], a[120:123], v[72:75]\n\t"
      "ds_read_b64_tr_b16 a[66:67], v23 offset:10816\n\t"
      "v_mfma_f32_16x16x32_bf16 v[76:79], a[8:11], a[124:127], v[76:79]\n\t"
      "v_mfma_f32_16x16x32_bf16 v[80:83], a[12:15], a[112:115], v[80:83]\n\t"
      "ds_read_b64_tr_b16 a[68:69], v23 offset:7264\n\t"
      "global_load_dwordx4 a[152:155], v28, s[42:43] offset:0 sc1\n\t"
      "v_mfma_f32_16x16x32_bf16 v[84:87], a[12:15], a[116:119], v[84:87]\n\t"
      "v_mfma_f32_16x16x32_bf16 v[88:91], a[12:15], a[120:123], v[88:91]\n\t"
      "ds_read_b64_tr_b16 a[70:71], v23 offset:10848\n\t"
      "v_mfma_f32_16x16x32_bf16 v[92:95], a[12:15], a[124:127], v[92:95]\n\t"
      "ds_read_b64_tr_b16 a[72:73], v23 offset:7296\n\t"
      "v_mfma_f32_16x16x32_bf16 v[96:99], a[16:19], a[112:115], v[96:99]\n\t"
      "v_mfma_f32_16x16x32_bf16 v[100:103], a[16:19], a[116:119], v[100:103]\n\t"
      "ds_read_b64_tr_b16 a[74:75], v23 offset:10880\n\t"
      "v_mfma_f32_16x16x32_bf16 v[104:107], a[16:19], a[120:123], v[104:107]\n\t"
      "global_load_dwordx4 a[156:159], v29, s[42:43] offset:0 sc1\n\t"
      "v_mfma_f32_16x16x32_bf16 v[108:111], a[16:19], a[124:127], v[108:111]\n\t"
      "ds_read_b64_tr_b16 a[76:77], v23 offset:7328\n\t"
      "v_mfma_f32_16x16x32_bf16 v[112:115], a[20:23], a[112:115], v[112:115]\n\t"
      "v_mfma_f32_16x16x32_bf16 v[116:119], a[20:23], a[116:119], v[116:119]\n\t"
      "ds_read_b64_tr_b16 a[78:79], v23 offset:10912\n\t"
      "v_mfma_f32_16x16x32_bf16 v[120:123], a[20:23], a[120:123], v[120:123]\n\t"
      "v_mfma_f32_16x16x32_bf16 v[124:127], a[20:23], a[124:127], v[124:127]\n\t"
      "ds_read_b64_tr_b16 a[80:81], v23 offset:7360\n\t"
      "v_mfma_f32_16x16x32_bf16 v[128:131], a[24:27], a[112:115], v[128:131]\n\t"
      "global_load_dwordx4 a[160:163], v26, s[42:43] offset:1024 sc1\n\t"
      "v_mfma_f32_16x16x32_bf16 v[132:135], a[24:27], a[116:119], v[132:135]\n\t"
      "ds_read_b64_tr_b16 a[82:83], v23 offset:10944\n\t"
      "v_mfma_f32_16x16x32_bf16 v[136:139], a[24:27], a[120:123], v[136:139]\n\t"
      "v_mfma_f32_16x16x32_bf16 v[140:143], a[24:27], a[124:127], v[140:143]\n\t"
      "ds_read_b64_tr_b16 a[84:85], v23 offset:21632\n\t"
      "v_mfma_f32_16x16x32_bf16 v[144:147], a[28:31], a[112:115], v[144:147]\n\t"
      "ds_read_b64_tr_b16 a[86:87], v23 offset:25216\n\t"
      "v_mfma_f32_16x16x32_bf16 v[148:151], a[28:31], a[116:119], v[148:151]\n\t"
      "v_mfma_f32_16x16x32_bf16 v[152:155], a[28:31], a[120:123], v[152:155]\n\t"
      "ds_read_b64_tr_b16 a[88:89], v23 offset:21664\n\t"
      "global_load_dwordx4 a[164:167], v27, s[42:43] offset:1024 sc1\n\t"
      "v_mfma_f32_16x16x32_bf16 v[156:159], a[28:31], a[124:127], v[156:159]\n\t"
      "v_mfma_f32_16x16x32_bf16 v[160:163], a[32:35], a[112:115], v[160:163]\n\t"
      "ds_read_b64_tr_b16 a[90:91], v23 offset:25248\n\t"
      "v_mfma_f32_16x16x32_bf16 v[164:167], a[32:35], a[116:119], v[164:167]\n\t"
      "v_mfma_f32_16x16x32_bf16 v[168:171], a[32:35], a[120:123], v[168:171]\n\t"
      "ds_read_b64_tr_b16 a[92:93], v23 offset:21696\n\t"
      "v_mfma_f32_16x16x32_bf16 v[172:175], a[32:35], a[124:127], v[172:175]\n\t"
      "v_mfma_f32_16x16x32_bf16 v[176:179], a[36:39], a[112:115], v[176:179]\n\t"
      "ds_read_b64_tr_b16 a[94:95], v23 offset:25280\n\t"
      "global_load_dwordx4 a[168:171], v28, s[42:43] offset:1024 sc1\n\t"
      "v_mfma_f32_16x16x32_bf16 v[180:183], a[36:39], a[116:119], v[180:183]\n\t"
      "v_mfma_f32_16x16x32_bf16 v[184:187], a[36:39], a[120:123], v[184:187]\n\t"
      "ds_read_b64_tr_b16 a[96:97], v23 offset:21728\n\t"
      "v_mfma_f32_16x16x32_bf16 v[188:191], a[36:39], a[124:127], v[188:191]\n\t"
      "v_mfma_f32_16x16x32_bf16 v[192:195], a[40:43], a[112:115], v[192:195]\n\t"
      "ds_read_b64_tr_b16 a[98:99], v23 offset:25312\n\t"
      "v_mfma_f32_16x16x32_bf16 v[196:199], a[40:43], a[116:119], v[196:199]\n\t"
      "ds_read_b64_tr_b16 a[100:101], v23 offset:21760\n\t"
      "v_mfma_f32_16x16x32_bf16 v[200:203], a[40:43], a[120:123], v[200:203]\n\t"
      "global_load_dwordx4 a[172:175], v29, s[42:43] offset:1024 sc1\n\t"
      "v_mfma_f32_16x16x32_bf16 v[204:207], a[40:43], a[124:127], v[204:207]\n\t"
      "ds_read_b64_tr_b16 a[102:103], v23 offset:25344\n\t"
      "v_mfma_f32_16x16x32_bf16 v[208:211], a[44:47], a[112:115], v[208:211]\n\t"
      "s_add_u32 s47, s47, 28928\n\t"
      "v_mfma_f32_16x16x32_bf16 v[212:215], a[44:47], a[116:119], v[212:215]\n\t"
      "ds_read_b64_tr_b16 a[104:105], v23 offset:21792\n\t"
      "v_mfma_f32_16x16x32_bf16 v[216:219], a[44:47], a[120:123], v[216:219]\n\t"
      "s_cmp_eq_u32 s47, 115712\n\t"
      "s_cselect_b32 s47, 0, s47\n\t"
      "v_mfma_f32_16x16x32_bf16 v[220:223], a[44:47], a[124:127], v[220:223]\n\t"
      "ds_read_b64_tr_b16 a[106:107], v23 offset:25376\n\t"
      "v_mfma_f32_16x16x32_bf16 v[224:227], a[48:51], a[112:115], v[224:227]\n\t"
      "v_add_u32 v24, s47, v25\n\t"
      "s_add_u32 s42, s42, 0x800\n\t"
      "s_addc_u32 s43, s43, 0\n\t"
      "global_load_dwordx4 a[176:179], v26, s[42:43] offset:0 sc1\n\t"
      "v_mfma_f32_16x16x32_bf16 v[228:231], a[48:51], a[116:119], v[228:231]\n\t"
      "ds_read_b64_tr_b16 a[108:109], v23 offset:21824\n\t"
      "v_mfma_f32_16x16x32_bf16 v[232:235], a[48:51], a[120:123], v[232:235]\n\t"
      "v_mfma_f32_16x16x32_bf16 v[236:239], a[48:51], a[124:127], v[236:239]\n\t"
      "ds_read_b64_tr_b16 a[110:111], v23 offset:25408\n\t"
      "v_mfma_f32_16x16x32_bf16 v[240:243], a[52:55], a[112:115], v[240:243]\n\t"
      "buffer_load_dword v30, v14, %[wsrd], s53 offen\n\t"
      "v_mfma_f32_16x16x32_bf16 v[244:247], a[52:55], a[116:119], v[244:247]\n\t"
      "v_mfma_f32_16x16x32_bf16 v[248:251], a[52:55], a[120:123], v[248:251]\n\t"
      "global_load_dwordx4 a[180:183], v27, s[42:43] offset:0 sc1\n\t"
      "v_mfma_f32_16x16x32_bf16 v[252:255], a[52:55], a[124:127], v[252:255]\n\t"
      "s_waitcnt vmcnt(21)\n\t"
      "s_waitcnt lgkmcnt(0)\n\t"
      "v_mfma_f32_16x16x32_bf16 v[32:35], a[56:59], a[128:131], v[32:35]\n\t"
      "s_add_u32 s53, s53, 0x70000\n\t"
      "v_mfma_f32_16x16x32_bf16 v[36:39], a[56:59], a[132:135], v[36:39]\n\t"
      "v_mfma_f32_16x16x32_bf16 v[40:43], a[56:59], a[136:139], v[40:43]\n\t"
      "v_mfma_f32_16x16x32_bf16 v[44:47], a[56:59], a[140:143], v[44:47]\n\t"
      "v_mfma_f32_16x16x32_bf16 v[48:51], a[60:63], a[128:131], v[48:51]\n\t"
      "global_load_dwordx4 a[184:187], v28, s[42:43] offset:0 sc1\n\t"
      "v_mfma_f32_16x16x32_bf16 v[52:55], a[60:63], a[132:135], v[52:55]\n\t"
      "v_mfma_f32_16x16x32_bf16 v[56:59], a[60:63], a[136:139], v[56:59]\n\t"
      "v_mfma_f32_16x16x32_bf16 v[60:63], a[60:63], a[140:143], v[60:63]\n\t"
      "v_mfma_f32_16x16x32_bf16 v[64:67], a[64:67], a[128:131], v[64:67]\n\t"
      "s_waitcnt lgkmcnt(0)\n\t"
      "s_waitcnt vmcnt(30)\n\t"
      "s_barrier\n\t"
      "v_mfma_f32_16x16x32_bf16 v[68:71], a[64:67], a[132:135], v[68:71]\n\t"
      "ds_read_b64_tr_b16 a[0:1], v24 offset:0\n\t"
      "v_mfma_f32_16x16x32_bf16 v[72:75], a[64:67], a[136:139], v[72:75]\n\t"
      "ds_read_b64_tr_b16 a[2:3], v24 offset:3584\n\t"
      "global_load_dwordx4 a[188:191], v29, s[42:43] offset:0 sc1\n\t"
      "v_mfma_f32_16x16x32_bf16 v[76:79], a[64:67], a[140:143], v[76:79]\n\t"
      "ds_read_b64_tr_b16 a[4:5], v24 offset:32\n\t"
      "v_mfma_f32_16x16x32_bf16 v[80:83], a[68:71], a[128:131], v[80:83]\n\t"
      "ds_read_b64_tr_b16 a[6:7], v24 offset:3616\n\t"
      "v_mfma_f32_16x16x32_bf16 v[84:87], a[68:71], a[132:135], v[84:87]\n\t"
      "ds_read_b64_tr_b16 a[8:9], v24 offset:64\n\t"
      "v_mfma_f32_16x16x32_bf16 v[88:91], a[68:71], a[136:139], v[88:91]\n\t"
      "ds_read_b64_tr_b16 a[10:11], v24 offset:3648\n\t"
      "v_mfma_f32_16x16x32_bf16 v[92:95], a[68:71], a[140:143], v[92:95]\n\t"
      "ds_read_b64_tr_b16 a[12:13], v24 offset:96\n\t"
      "v_mfma_f32_16x16x32_bf16 v[96:99], a[72:75], a[128:131], v[96:99]\n\t"
      "ds_read_b64_tr_b16 a[14:15], v24 offset:3680\n\t"
      "global_load_dwordx4 a[192:195], v26, s[42:43] offset:1024 sc1\n\t"
      "v_mfma_f32_16x16x32_bf16 v[100:103], a[72:75], a[132:135], v[100:103]\n\t"
      "ds_read_b64_tr_b16 a[16:17], v24 offset:128\n\t"
      "v_mfma_f32_16x16x32_bf16 v[104:107], a[72:75], a[136:139], v[104:107]\n\t"
      "ds_read_b64_tr_b16 a[18:19], v24 offset:3712\n\t"
      "v_mfma_f32_16x16x32_bf16 v[108:111], a[72:75], a[140:143], v[108:111]\n\t"
      "ds_read_b64_tr_b16 a[20:21], v24 offset:160\n\t"
      "v_mfma_f32_16x16x32_bf16 v[112:115], a[76:79], a[128:131], v[112:115]\n\t"
      "v_mfma_f32_16x16x32_bf16 v[116:119], a[76:79], a[132:135], v[116:119]\n\t"
      "ds_read_b64_tr_b16 a[22:23], v24 offset:3744\n\t"
      "v_mfma_f32_16x16x32_bf16 v[120:123], a[76:79], a[136:139], v[120:123]\n\t"
      "global_load_dwordx4 a[196:199], v27, s[42:43] offset:1024 sc1\n\t"
      "v_mfma_f32_16x16x32_bf16 v[124:127], a[76:79], a[140:143], v[124:127]\n\t"
      "ds_read_b64_tr_b16 a[24:25], v24 offset:192\n\t"
      "v_mfma_f32_16x16x32_bf16 v[128:131], a[80:83], a[128:131], v[128:131]\n\t"
      "v_mfma_f32_16x16x32_bf16 v[132:135], a[80:83], a[132:135], v[132:135]\n\t"
      "ds_read_b64_tr_b16 a[26:27], v24 offset:3776\n\t"
      "v_mfma_f32_16x16x32_bf16 v[136:139], a[80:83], a[136:139], v[136:139]\n\t"
      "v_mfma_f32_16x16x32_bf16 v[140:143], a[80:83], a[140:143], v[140:143]\n\t"
      "ds_read_b64_tr_b16 a[28:29], v24 offset:14464\n\t"
      "v_mfma_f32_16x16x32_bf16 v[144:147], a[84:87], a[128:131], v[144:147]\n\t"
      "ds_read_b64_tr_b16 a[30:31], v24 offset:18048\n\t"
      "global_load_dwordx4 a[200:203], v28, s[42:43] offset:1024 sc1\n\t"
      "v_mfma_f32_16x16x32_bf16 v[148:151], a[84:87], a[132:135], v[148:151]\n\t"
      "v_mfma_f32_16x16x32_bf16 v[152:155], a[84:87], a[136:139], v[152:155]\n\t"
      "ds_read_b64_tr_b16 a[32:33], v24 offset:14496\n\t"
      "v_mfma_f32_16x16x32_bf16 v[156:159], a[84:87], a[140:143], v[156:159]\n\t"
      "v_mfma_f32_16x16x32_bf16 v[160:163], a[88:91], a[128:131], v[160:163]\n\t"
      "ds_read_b64_tr_b16 a[34:35], v24 offset:18080\n\t"
      "v_mfma_f32_16x16x32_bf16 v[164:167], a[88:91], a[132:135], v[164:167]\n\t"
      "v_mfma_f32_16x16x32_bf16 v[168:171], a[88:91], a[136:139], v[168:171]\n\t"
      "ds_read_b64_tr_b16 a[36:37], v24 offset:14528\n\t"
      "global_load_dwordx4 a[204:207], v29, s[42:43] offset:1024 sc1\n\t"
      "v_mfma_f32_16x16x32_bf16 v[172:175], a[88:91], a[140:143], v[172:175]\n\t"
      "s_add_u32 s42, s42, 0x800\n\t"
      "s_addc_u32 s43, s43, 0\n\t"
      "v_mfma_f32_16x16x32_bf16 v[176:179], a[92:95], a[128:131], v[176:179]\n\t"
      "ds_read_b64_tr_b16 a[38:39], v24 offset:18112\n\t"
      "v_mfma_f32_16x16x32_bf16 v[180:183], a[92:95], a[132:135], v[180:183]\n\t"
      "v_mfma_f32_16x16x32_bf16 v[184:187], a[92:95], a[136:139], v[184:187]\n\t"
      "ds_read_b64_tr_b16 a[40:41], v24 offset:14560\n\t"
      "v_mfma_f32_16x16x32_bf16 v[188:191], a[92:95], a[140:143], v[188:191]\n\t"
      "v_mfma_f32_16x16x32_bf16 v[192:195], a[96:99], a[128:131], v[192:195]\n\t"
      "ds_read_b64_tr_b16 a[42:43], v24 offset:18144\n\t"
      "global_load_lds_dwordx4 v16, s[40:41]\n\t"
      "s_add_u32 m0, m0, 0x400\n\t"
      "v_mfma_f32_16x16x32_bf16 v[196:199], a[96:99], a[132:135], v[196:199]\n\t"
      "ds_read_b64_tr_b16 a[44:45], v24 offset:14592\n\t"
      "v_mfma_f32_16x16x32_bf16 v[200:203], a[96:99], a[136:139], v[200:203]\n\t"
      "v_mfma_f32_16x16x32_bf16 v[204:207], a[96:99], a[140:143], v[204:207]\n\t"
      "ds_read_b64_tr_b16 a[46:47], v24 offset:18176\n\t"
      "v_mfma_f32_16x16x32_bf16 v[208:211], a[100:103], a[128:131], v[208:211]\n\t"
      "v_mfma_f32_16x16x32_bf16 v[212:215], a[100:103], a[132:135], v[212:215]\n\t"
      "ds_read_b64_tr_b16 a[48:49], v24 offset:14624\n\t"
      "v_mfma_f32_16x16x32_bf16 v[216:219], a[100:103], a[136:139], v[216:219]\n\t"
      "global_load_lds_dwordx4 v17, s[40:41]\n\t"
      "s_add_u32 m0, m0, 0x400\n\t"
      "v_mfma_f32_16x16x32_bf16 v[220:223], a[100:103], a[140:143], v[220:223]\n\t"
      "ds_read_b64_tr_b16 a[50:51], v24 offset:18208\n\t"
      "v_mfma_f32_16x16x32_bf16 v[224:227], a[104:107], a[128:131], v[224:227]\n\t"
      "v_mfma_f32_16x16x32_bf16 v[228:231], a[104:107], a[132:135], v[228:231]\n\t"
      "ds_read_b64_tr_b16 a[52:53], v24 offset:14656\n\t"
      "v_mfma_f32_16x16x32_bf16 v[232:235], a[104:107], a[136:139], v[232:235]\n\t"
      "v_mfma_f32_16x16x32_bf16 v[236:239], a[104:107], a[140:143], v[236:239]\n\t"
      "ds_read_b64_tr_b16 a[54:55], v24 offset:18240\n\t"
      "v_mfma_f32_16x16x32_bf16 v[240:243], a[108:111], a[128:131], v[240:243]\n\t"
      "global_load_lds_dwordx4 v18, s[40:41]\n\t"
      "s_add_u32 m0, m0, 0x400\n\t"
      "v_mfma_f32_16x16x32_bf16 v[244:247], a[108:111], a[132:135], v[244:247]\n\t"
      "v_mfma_f32_16x16x32_bf16 v[248:251], a[108:111], a[136:139], v[248:251]\n\t"
      "v_mfma_f32_16x16x32_bf16 v[252:255], a[108:111], a[140:143], v[252:255]\n\t"
      "s_waitcnt vmcnt(16)\n\t"
      "s_waitcnt lgkmcnt(0)\n\t"
      "v_mfma_f32_16x16x32_bf16 v[32:35], a[0:3], a[144:147], v[32:35]\n\t"
      "v_mfma_f32_16x16x32_bf16 v[36:39], a[0:3], a[148:151], v[36:39]\n\t"
      "ds_read_b64_tr_b16 a[56:57], v24 offset:7168\n\t"
      "v_mfma_f32_16x16x32_bf16 v[40:43], a[0:3], a[152:155], v[40:43]\n\t"
      "ds_read_b64_tr_b16 a[58:59], v24 offset:10752\n\t"
      "global_load_lds_dwordx4 v19, s[40:41]\n\t"
      "s_add_u32 m0, m0, 0x400\n\t"
      "v_mfma_f32_16x16x32_bf16 v[44:47], a[0:3], a[156:159], v[44:47]\n\t"
      "v_mfma_f32_16x16x32_bf16 v[48:51], a[4:7], a[144:147], v[48:51]\n\t"
      "ds_read_b64_tr_b16 a[60:61], v24 offset:7200\n\t"
      "ds_read_b128 a[244:247], %[kvl] offset:0\n\t"
      "v_mfma_f32_16x16x32_bf16 v[52:55], a[4:7], a[148:151], v[52:55]\n\t"
      "v_mfma_f32_16x16x32_bf16 v[56:59], a[4:7], a[152:155], v[56:59]\n\t"
      "ds_read_b64_tr_b16 a[62:63], v24 offset:10784\n\t"
      "v_mfma_f32_16x16x32_bf16 v[60:63], a[4:7], a[156:159], v[60:63]\n\t"
      "v_mfma_f32_16x16x32_bf16 v[64:67], a[8:11], a[144:147], v[64:67]\n\t"
      "ds_read_b64_tr_b16 a[64:65], v24 offset:7232\n\t"
      "global_load_lds_dwordx4 v20, s[40:41]\n\t"
      "s_add_u32 m0, m0, 0x400\n\t"
      "v_mfma_f32_16x16x32_bf16 v[68:71], a[8:11], a[148:151], v[68:71]\n\t"
      "v_mfma_f32_16x16x32_bf16 v[72:75], a[8:11], a[152:155], v[72:75]\n\t"
      "ds_read_b64_tr_b16 a[66:67], v24 offset:10816\n\t"
      "v_mfma_f32_16x16x32_bf16 v[76:79], a[8:11], a[156:159], v[76:79]\n\t"
      "v_mfma_f32_16x16x32_bf16 v[80:83], a[12:15], a[144:147], v[80:83]\n\t"
      "ds_read_b64_tr_b16 a[68:69], v24 offset:7264\n\t"
      "v_mfma_f32_16x16x32_bf16 v[84:87], a[12:15], a[148:151], v[84:87]\n\t"
      "v_mfma_f32_16x16x32_bf16 v[88:91], a[12:15], a[152:155], v[88:91]\n\t"
      "ds_read_b64_tr_b16 a[70:71], v24 offset:10848\n\t"
      "global_load_lds_dwordx4 v21, s[40:41]\n\t"
      "s_add_u32 m0, m0, 0x400\n\t"
      "v_mfma_f32_16x16x32_bf16 v[92:95], a[12:15], a[156:159], v[92:95]\n\t"
      "ds_read_b64_tr_b16 a[72:73], v24 offset:7296\n\t"
      "v_mfma_f32_16x16x32_bf16 v[96:99], a[16:19], a[144:147], v[96:99]\n\t"
      "v_mfma_f32_16x16x32_bf16 v[100:103], a[16:19], a[148:151], v[100:103]\n\t"
      "ds_read_b64_tr_b16 a[74:75], v24 offset:10880\n\t"
      "v_mfma_f32_16x16x32_bf16 v[104:107], a[16:19], a[152:155], v[104:107]\n\t"
      "v_mfma_f32_16x16x32_bf16 v[108:111], a[16:19], a[156:159], v[108:111]\n\t"
      "ds_read_b64_tr_b16 a[76:77], v24 offset:7328\n\t"
      "v_mfma_f32_16x16x32_bf16 v[112:115], a[20:23], a[144:147], v[112:115]\n\t"
      "global_load_lds_dwordx4 v22, s[40:41]\n\t"
      "v_mfma_f32_16x16x32_bf16 v[116:119], a[20:23], a[148:151], v[116:119]\n\t"
      "ds_read_b64_tr_b16 a[78:79], v24 offset:10912\n\t"
      "v_mfma_f32_16x16x32_bf16 v[120:123], a[20:23], a[152:155], v[120:123]\n\t"
      "s_add_u32 s40, s40, 0x70000\n\t"
      "s_addc_u32 s41, s41, 0\n\t"
      "v_mfma_f32_16x16x32_bf16 v[124:127], a[20:23], a[156:159], v[124:127]\n\t"
      "ds_read_b64_tr_b16 a[80:81], v24 offset:7360\n\t"
      "v_mfma_f32_16x16x32_bf16 v[128:131], a[24:27], a[144:147], v[128:131]\n\t"
      "s_add_u32 s46, s46, 28928\n\t"
      "v_mfma_f32_16x16x32_bf16 v[132:135], a[24:27], a[148:151], v[132:135]\n\t"
      "ds_read_b64_tr_b16 a[82:83], v24 offset:10944\n\t"
      "v_mfma_f32_16x16x32_bf16 v[136:139], a[24:27], a[152:155], v[136:139]\n\t"
      "global_load_dwordx4 a[208:211], v26, s[42:43] offset:0 sc1\n\t"
      "v_mfma_f32_16x16x32_bf16 v[140:143], a[24:27], a[156:159], v[140:143]\n\t"
      "ds_read_b64_tr_b16 a[84:85], v24 offset:21632\n\t"
      "v_mfma_f32_16x16x32_bf16 v[144:147], a[28:31], a[144:147], v[144:147]\n\t"
      "ds_read_b64_tr_b16 a[86:87], v24 offset:25216\n\t"
      "ds_read_b128 a[248:251], %[kvl] offset:4352\n\t"
      "v_mfma_f32_16x16x32_bf16 v[148:151], a[28:31], a[148:151], v[148:151]\n\t"
      "s_cmp_eq_u32 s46, s50\n\t"
      "s_cselect_b32 s46, s49, s46\n\t"
      "v_mfma_f32_16x16x32_bf16 v[152:155], a[28:31], a[152:155], v[152:155]\n\t"
      "ds_read_b64_tr_b16 a[88:89], v24 offset:21664\n\t"
      "v_mfma_f32_16x16x32_bf16 v[156:159], a[28:31], a[156:159], v[156:159]\n\t"
      "s_mov_b32 m0, s46\n\t"
      "v_mfma_f32_16x16x32_bf16 v[160:163], a[32:35], a[144:147], v[160:163]\n\t"
      "ds_read_b64_tr_b16 a[90:91], v24 offset:25248\n\t"
      "global_load_dwordx4 a[212:215], v27, s[42:43] offset:0 sc1\n\t"
      "v_mfma_f32_16x16x32_bf16 v[164:167], a[32:35], a[148:151], v[164:167]\n\t"
      "v_mfma_f32_16x16x32_bf16 v[168:171], a[32:35], a[152:155], v[168:171]\n\t"
      "ds_read_b64_tr_b16 a[92:93], v24 offset:21696\n\t"
      "v_mfma_f32_16x16x32_bf16 v[172:175], a[32:35], a[156:159], v[172:175]\n\t"
      "v_mfma_f32_16x16x32_bf16 v[176:179], a[36:39], a[144:147], v[176:179]\n\t"
      "ds_read_b64_tr_b16 a[94:95], v24 offset:25280\n\t"
      "v_mfma_f32_16x16x32_bf16 v[180:183], a[36:39], a[148:151], v[180:183]\n\t"
      "v_mfma_f32_16x16x32_bf16 v[184:187], a[36:39], a[152:155], v[184:187]\n\t"
      "ds_read_b64_tr_b16 a[96:97], v24 offset:21728\n\t"
      "global_load_dwordx4 a[216:219], v28, s[42:43] offset:0 sc1\n\t"
      "v_mfma_f32_16x16x32_bf16 v[188:191], a[36:39], a[156:159], v[188:191]\n\t"
      "v_mfma_f32_16x16x32_bf16 v[192:195], a[40:43], a[144:147], v[192:195]\n\t"
      "ds_read_b64_tr_b16 a[98:99], v24 offset:25312\n\t"
      "v_mfma_f32_16x16x32_bf16 v[196:199], a[40:43], a[148:151], v[196:199]\n\t"
      "ds_read_b64_tr_b16 a[100:101], v24 offset:21760\n\t"
      "v_mfma_f32_16x16x32_bf16 v[200:203], a[40:43], a[152:155], v[200:203]\n\t"
      "s_add_u32 s47, s47, 28928\n\t"
      "v_mfma_f32_16x16x32_bf16 v[204:207], a[40:43], a[156:159], v[204:207]\n\t"
      "ds_read_b64_tr_b16 a[102:103], v24 offset:25344\n\t"
      "v_mfma_f32_16x16x32_bf16 v[208:211], a[44:47], a[144:147], v[208:211]\n\t"
      "global_load_dwordx4 a[220:223], v29, s[42:43] offset:0 sc1\n\t"
      "v_mfma_f32_16x16x32_bf16 v[212:215], a[44:47], a[148:151], v[212:215]\n\t"
      "ds_read_b64_tr_b16 a[104:105], v24 offset:21792\n\t"
      "v_mfma_f32_16x16x32_bf16 v[216:219], a[44:47], a[152:155], v[216:219]\n\t"
      "s_cmp_eq_u32 s47, 115712\n\t"
      "s_cselect_b32 s47, 0, s47\n\t"
      "v_mfma_f32_16x16x32_bf16 v[220:223], a[44:47], a[156:159], v[220:223]\n\t"
      "ds_read_b64_tr_b16 a[106:107], v24 offset:25376\n\t"
      "v_mfma_f32_16x16x32_bf16 v[224:227], a[48:51], a[144:147], v[224:227]\n\t"
      "v_add_u32 v23, s47, v25\n\t"
      "v_mfma_f32_16x16x32_bf16 v[228:231], a[48:51], a[148:151], v[228:231]\n\t"
      "ds_read_b64_tr_b16 a[108:109], v24 offset:21824\n\t"
      "v_mfma_f32_16x16x32_bf16 v[232:235], a[48:51], a[152:155], v[232:235]\n\t"
      "global_load_dwordx4 a[224:227], v26, s[42:43] offset:1024 sc1\n\t"
      "v_mfma_f32_16x16x32_bf16 v[236:239], a[48:51], a[156:159], v[236:239]\n\t"
      "ds_read_b64_tr_b16 a[110:111], v24 offset:25408\n\t"
      "v_mfma_f32_16x16x32_bf16 v[240:243], a[52:55], a[144:147], v[240:243]\n\t"
      "buffer_load_dword v30, v14, %[wsrd], s53 offen\n\t"
      "ds_read_b128 a[252:255], %[kvl] offset:8704\n\t"
      "v_mfma_f32_16x16x32_bf16 v[244:247], a[52:55], a[148:151], v[244:247]\n\t"
      "v_mfma_f32_16x16x32_bf16 v[248:251], a[52:55], a[152:155], v[248:251]\n\t"
      "v_mfma_f32_16x16x32_bf16 v[252:255], a[52:55], a[156:159], v[252:255]\n\t"
      "s_waitcnt vmcnt(22)\n\t"
      "s_waitcnt lgkmcnt(0)\n\t"
      "v_mfma_f32_16x16x32_bf16 v[32:35], a[56:59], a[160:163], v[32:35]\n\t"
      "global_load_dwordx4 a[228:231], v27, s[42:43] offset:1024 sc1\n\t"
      "s_add_u32 s53, s53, 0x70000\n\t"
      "v_mfma_f32_16x16x32_bf16 v[36:39], a[56:59], a[164:167], v[36:39]\n\t"
      "v_mfma_f32_16x16x32_bf16 v[40:43], a[56:59], a[168:171], v[40:43]\n\t"
      "v_mfma_f32_16x16x32_bf16 v[44:47], a[56:59], a[172:175], v[44:47]\n\t"
      "v_mfma_f32_16x16x32_bf16 v[48:51], a[60:63], a[160:163], v[48:51]\n\t"
      "global_store_dwordx4 %[kvo], a[244:247], s[64:65] sc1\n\t"
      "s_add_u32 s64, s64, 0x10000\n\t"
      "s_addc_u32 s65, s65, 0\n\t"
      "v_mfma_f32_16x16x32_bf16 v[52:55], a[60:63], a[164:167], v[52:55]\n\t"
      "v_mfma_f32_16x16x32_bf16 v[56:59], a[60:63], a[168:171], v[56:59]\n\t"
      "global_load_dwordx4 a[232:235], v28, s[42:43] offset:1024 sc1\n\t"
      "v_mfma_f32_16x16x32_bf16 v[60:63], a[60:63], a[172:175], v[60:63]\n\t"
      "v_mfma_f32_16x16x32_bf16 v[64:67], a[64:67], a[160:163], v[64:67]\n\t"
      "s_waitcnt lgkmcnt(0)\n\t"
      "s_waitcnt vmcnt(36)\n\t"
      "s_barrier\n\t"
      "v_mfma_f32_16x16x32_bf16 v[68:71], a[64:67], a[164:167], v[68:71]\n\t"
      "ds_read_b64_tr_b16 a[0:1], v23 offset:0\n\t"
      "v_mfma_f32_16x16x32_bf16 v[72:75], a[64:67], a[168:171], v[72:75]\n\t"
      "ds_read_b64_tr_b16 a[2:3], v23 offset:3584\n\t"
      "v_mfma_f32_16x16x32_bf16 v[76:79], a[64:67], a[172:175], v[76:79]\n\t"
      "ds_read_b64_tr_b16 a[4:5], v23 offset:32\n\t"
      "v_mfma_f32_16x16x32_bf16 v[80:83], a[68:71], a[160:163], v[80:83]\n\t"
      "ds_read_b64_tr_b16 a[6:7], v23 offset:3616\n\t"
      "global_load_dwordx4 a[236:239], v29, s[42:43] offset:1024 sc1\n\t"
      "v_mfma_f32_16x16x32_bf16 v[84:87], a[68:71], a[164:167], v[84:87]\n\t"
      "ds_read_b64_tr_b16 a[8:9], v23 offset:64\n\t"
      "v_mfma_f32_16x16x32_bf16 v[88:91], a[68:71], a[168:171], v[88:91]\n\t"
      "ds_read_b64_tr_b16 a[10:11], v23 offset:3648\n\t"
      "global_store_dwordx4 %[kvo], a[248:251], s[64:65] sc1\n\t"
      "s_add_u32 s64, s64, 0x10000\n\t"
      "s_addc_u32 s65, s65, 0\n\t"
      "v_mfma_f32_16x16x32_bf16 v[92:95], a[68:71], a[172:175], v[92:95]\n\t"
      "ds_read_b64_tr_b16 a[12:13], v23 offset:96\n\t"
      "v_mfma_f32_16x16x32_bf16 v[96:99], a[72:75], a[160:163], v[96:99]\n\t"
      "ds_read_b64_tr_b16 a[14:15], v23 offset:3680\n\t"
      "v_mfma_f32_16x16x32_bf16 v[100:103], a[72:75], a[164:167], v[100:103]\n\t"
      "ds_read_b64_tr_b16 a[16:17], v23 offset:128\n\t"
      "v_mfma_f32_16x16x32_bf16 v[104:107], a[72:75], a[168:171], v[104:107]\n\t"
      "ds_read_b64_tr_b16 a[18:19], v23 offset:3712\n\t"
      "global_load_lds_dwordx4 v16, s[40:41]\n\t"
      "s_add_u32 m0, m0, 0x400\n\t"
      "v_mfma_f32_16x16x32_bf16 v[108:111], a[72:75], a[172:175], v[108:111]\n\t"
      "ds_read_b64_tr_b16 a[20:21], v23 offset:160\n\t"
      "v_mfma_f32_16x16x32_bf16 v[112:115], a[76:79], a[160:163], v[112:115]\n\t"
      "ds_read_b128 a[244:247], %[kvl] offset:13056\n\t"
      "v_mfma_f32_16x16x32_bf16 v[116:119], a[76:79], a[164:167], v[116:119]\n\t"
      "ds_read_b64_tr_b16 a[22:23], v23 offset:3744\n\t"
      "v_mfma_f32_16x16x32_bf16 v[120:123], a[76:79], a[168:171], v[120:123]\n\t"
      "s_add_u32 s42, s42, 0x800\n\t"
      "s_addc_u32 s43, s43, 0\n\t"
      "v_mfma_f32_16x16x32_bf16 v[124:127], a[76:79], a[172:175], v[124:127]\n\t"
      "ds_read_b64_tr_b16 a[24:25], v23 offset:192\n\t"
      "v_mfma_f32_16x16x32_bf16 v[128:131], a[80:83], a[160:163], v[128:131]\n\t"
      "global_load_lds_dwordx4 v17, s[40:41]\n\t"
      "s_add_u32 m0, m0, 0x400\n\t"
      "v_mfma_f32_16x16x32_bf16 v[132:135], a[80:83], a[164:167], v[132:135]\n\t"
      "ds_read_b64_tr_b16 a[26:27], v23 offset:3776\n\t"
      "v_mfma_f32_16x16x32_bf16 v[136:139], a[80:83], a[168:171], v[136:139]\n\t"
      "v_mfma_f32_16x16x32_bf16 v[140:143], a[80:83], a[172:175], v[140:143]\n\t"
      "ds_read_b64_tr_b16 a[28:29], v23 offset:14464\n\t"
      "v_mfma_f32_16x16x32_bf16 v[144:147], a[84:87], a[160:163], v[144:147]\n\t"
      "ds_read_b64_tr_b16 a[30:31], v23 offset:18048\n\t"
      "v_mfma_f32_16x16x32_bf16 v[148:151], a[84:87], a[164:167], v[148:151]\n\t"
      "v_mfma_f32_16x16x32_bf16 v[152:155], a[84:87], a[168:171], v[152:155]\n\t"
      "ds_read_b64_tr_b16 a[32:33], v23 offset:14496\n\t"
      "global_load_lds_dwordx4 v18, s[40:41]\n\t"
      "s_add_u32 m0, m0, 0x400\n\t"
      "v_mfma_f32_16x16x32_bf16 v[156:159], a[84:87], a[172:175], v[156:159]\n\t"
      "v_mfma_f32_16x16x32_bf16 v[160:163], a[88:91], a[160:163], v[160:163]\n\t"
      "ds_read_b64_tr_b16 a[34:35], v23 offset:18080\n\t"
      "v_mfma_f32_16x16x32_bf16 v[164:167], a[88:91], a[164:167], v[164:167]\n\t"
      "v_mfma_f32_16x16x32_bf16 v[168:171], a[88:91], a[168:171], v[168:171]\n\t"
      "ds_read_b64_tr_b16 a[36:37], v23 offset:14528\n\t"
      "v_mfma_f32_16x16x32_bf16 v[172:175], a[88:91], a[172:175], v[172:175]\n\t"
      "v_mfma_f32_16x16x32_bf16 v[176:179], a[92:95], a[160:163], v[176:179]\n\t"
      "ds_read_b64_tr_b16 a[38:39], v23 offset:18112\n\t"
      "global_load_lds_dwordx4 v19, s[40:41]\n\t"
      "s_add_u32 m0, m0, 0x400\n\t"
      "v_mfma_f32_16x16x32_bf16 v[180:183], a[92:95], a[164:167], v[180:183]\n\t"
      "v_mfma_f32_16x16x32_bf16 v[184:187], a[92:95], a[168:171], v[184:187]\n\t"
      "ds_read_b64_tr_b16 a[40:41], v23 offset:14560\n\t"
      "v_mfma_f32_16x16x32_bf16 v[188:191], a[92:95], a[172:175], v[188:191]\n\t"
      "v_mfma_f32_16x16x32_bf16 v[192:195], a[96:99], a[160:163], v[192:195]\n\t"
      "ds_read_b64_tr_b16 a[42:43], v23 offset:18144\n\t"
      "v_mfma_f32_16x16x32_bf16 v[196:199], a[96:99], a[164:167], v[196:199]\n\t"
      "ds_read_b64_tr_b16 a[44:45], v23 offset:14592\n\t"
      "v_mfma_f32_16x16x32_bf16 v[200:203], a[96:99], a[168:171], v[200:203]\n\t"
      "global_load_lds_dwordx4 v20, s[40:41]\n\t"
      "s_add_u32 m0, m0, 0x400\n\t"
      "v_mfma_f32_16x16x32_bf16 v[204:207], a[96:99], a[172:175], v[204:207]\n\t"
      "ds_read_b64_tr_b16 a[46:47], v23 offset:18176\n\t"
      "v_mfma_f32_16x16x32_bf16 v[208:211], a[100:103], a[160:163], v[208:211]\n\t"
      "v_mfma_f32_16x16x32_bf16 v[212:215], a[100:103], a[164:167], v[212:215]\n\t"
      "ds_read_b64_tr_b16 a[48:49], v23 offset:14624\n\t"
      "v_mfma_f32_16x16x32_bf16 v[216:219], a[100:103], a[168:171], v[216:219]\n\t"
      "v_mfma_f32_16x16x32_bf16 v[220:223], a[100:103], a[172:175], v[220:223]\n\t"
      "ds_read_b64_tr_b16 a[50:51], v23 offset:18208\n\t"
      "v_mfma_f32_16x16x32_bf16 v[224:227], a[104:107], a[160:163], v[224:227]\n\t"
      "global_load_lds_dwordx4 v21, s[40:41]\n\t"
      "s_add_u32 m0, m0, 0x400\n\t"
      "v_mfma_f32_16x16x32_bf16 v[228:231], a[104:107], a[164:167], v[228:231]\n\t"
      "ds_read_b64_tr_b16 a[52:53], v23 offset:14656\n\t"
      "v_mfma_f32_16x16x32_bf16 v[232:235], a[104:107], a[168:171], v[232:235]\n\t"
      "v_mfma_f32_16x16x32_bf16 v[236:239], a[104:107], a[172:175], v[236:239]\n\t"
      "ds_read_b64_tr_b16 a[54:55], v23 offset:18240\n\t"
      "v_mfma_f32_16x16x32_bf16 v[240:243], a[108:111], a[160:163], v[240:243]\n\t"
      "v_mfma_f32_16x16x32_bf16 v[244:247], a[108:111], a[164:167], v[244:247]\n\t"
      "v_mfma_f32_16x16x32_bf16 v[248:251], a[108:111], a[168:171], v[248:251]\n\t"
      "global_load_lds_dwordx4 v22, s[40:41]\n\t"
      "v_mfma_f32_16x16x32_bf16 v[252:255], a[108:111], a[172:175], v[252:255]\n\t"
      "s_add_u32 s40, s40, 0x70000\n\t"
      "s_addc_u32 s41, s41, 0\n\t"
      "s_add_u32 s46, s46, 28928\n\t"
      "s_cmp_eq_u32 s46, s50\n\t"
      "s_cselect_b32 s46, s49, s46\n\t"
      "s_mov_b32 m0, s46\n\t"
      "s_waitcnt vmcnt(29)\n\t"
      "s_waitcnt lgkmcnt(0)\n\t"
      "v_mfma_f32_16x16x32_bf16 v[32:35], a[0:3], a[176:179], v[32:35]\n\t"
      "v_mfma_f32_16x16x32_bf16 v[36:39], a[0:3], a[180:183], v[36:39]\n\t"
      "ds_read_b64_tr_b16 a[56:57], v23 offset:7168\n\t"
      "v_mfma_f32_16x16x32_bf16 v[40:43], a[0:3], a[184:187], v[40:43]\n\t"
      "ds_read_b64_tr_b16 a[58:59], v23 offset:10752\n\t"
      "v_mfma_f32_16x16x32_bf16 v[44:47], a[0:3], a[188:191], v[44:47]\n\t"
      "v_mfma_f32_16x16x32_bf16 v[48:51], a[4:7], a[176:179], v[48:51]\n\t"
      "ds_read_b64_tr_b16 a[60:61], v23 offset:7200\n\t"
      "global_load_dwordx4 a[112:115], v26, s[42:43] offset:0 sc1\n\t"
      "v_mfma_f32_16x16x32_bf16 v[52:55], a[4:7], a[180:183], v[52:55]\n\t"
      "v_mfma_f32_16x16x32_bf16 v[56:59], a[4:7], a[184:187], v[56:59]\n\t"
      "ds_read_b64_tr_b16 a[62:63], v23 offset:10784\n\t"
      "global_store_dwordx4 %[kvo], a[252:255], s[64:65] sc1\n\t"
      "s_add_u32 s64, s64, 0x10000\n\t"
      "s_addc_u32 s65, s65, 0\n\t"
      "v_mfma_f32_16x16x32_bf16 v[60:63], a[4:7], a[188:191], v[60:63]\n\t"
      "v_mfma_f32_16x16x32_bf16 v[64:67], a[8:11], a[176:179], v[64:67]\n\t"
      "ds_read_b64_tr_b16 a[64:65], v23 offset:7232\n\t"
      "v_mfma_f32_16x16x32_bf16 v[68:71], a[8:11], a[180:183], v[68:71]\n\t"
      "v_mfma_f32_16x16x32_bf16 v[72:75], a[8:11], a[184:187], v[72:75]\n\t"
      "ds_read_b64_tr_b16 a[66:67], v23 offset:10816\n\t"
      "global_load_dwordx4 a[116:119], v27, s[42:43] offset:0 sc1\n\t"
      "v_mfma_f32_16x16x32_bf16 v[76:79], a[8:11], a[188:191], v[76:79]\n\t"
      "v_mfma_f32_16x16x32_bf16 v[80:83], a[12:15], a[176:179], v[80:83]\n\t"
      "ds_read_b64_tr_b16 a[68:69], v23 offset:7264\n\t"
      "v_mfma_f32_16x16x32_bf16 v[84:87], a[12:15], a[180:183], v[84:87]\n\t"
      "v_mfma_f32_16x16x32_bf16 v[88:91], a[12:15], a[184:187], v[88:91]\n\t"
      "ds_read_b64_tr_b16 a[70:71], v23 offset:10848\n\t"
      "v_mfma_f32_16x16x32_bf16 v[92:95], a[12:15], a[188:191], v[92:95]\n\t"
      "ds_read_b64_tr_b16 a[72:73], v23 offset:7296\n\t"
      "v_mfma_f32_16x16x32_bf16 v[96:99], a[16:19], a[176:179], v[96:99]\n\t"
      "global_load_dwordx4 a[120:123], v28, s[42:43] offset:0 sc1\n\t"
      "global_store_dwordx4 %[kvo], a[244:247], s[64:65] sc1\n\t"
      "s_add_u32 s64, s64, 0x10000\n\t"
      "s_addc_u32 s65, s65, 0\n\t"
      "v_mfma_f32_16x16x32_bf16 v[100:103], a[16:19], a[180:183], v[100:103]\n\t"
      "ds_read_b64_tr_b16 a[74:75], v23 offset:10880\n\t"
      "v_mfma_f32_16x16x32_bf16 v[104:107], a[16:19], a[184:187], v[104:107]\n\t"
      "v_mfma_f32_16x16x32_bf16 v[108:111], a[16:19], a[188:191], v[108:111]\n\t"
      "ds_read_b64_tr_b16 a[76:77], v23 offset:7328\n\t"
      "v_mfma_f32_16x16x32_bf16 v[112:115], a[20:23], a[176:179], v[112:115]\n\t"
      "v_mfma_f32_16x16x32_bf16 v[116:119], a[20:23], a[180:183], v[116:119]\n\t"
      "ds_read_b64_tr_b16 a[78:79], v23 offset:10912\n\t"
      "v_mfma_f32_16x16x32_bf16 v[120:123], a[20:23], a[184:187], v[120:123]\n\t"
      "global_load_dwordx4 a[124:127], v29, s[42:43] offset:0 sc1\n\t"
      "v_mfma_f32_16x16x32_bf16 v[124:127], a[20:23], a[188:191], v[124:127]\n\t"
      "ds_read_b64_tr_b16 a[80:81], v23 offset:7360\n\t"
      "v_mfma_f32_16x16x32_bf16 v[128:131], a[24:27], a[176:179], v[128:131]\n\t"
      "v_mfma_f32_16x16x32_bf16 v[132:135], a[24:27], a[180:183], v[132:135]\n\t"
      "ds_read_b64_tr_b16 a[82:83], v23 offset:10944\n\t"
      "v_mfma_f32_16x16x32_bf16 v[136:139], a[24:27], a[184:187], v[136:139]\n\t"
      "v_mfma_f32_16x16x32_bf16 v[140:143], a[24:27], a[188:191], v[140:143]\n\t"
      "ds_read_b64_tr_b16 a[84:85], v23 offset:21632\n\t"
      "v_mfma_f32_16x16x32_bf16 v[144:147], a[28:31], a[176:179], v[144:147]\n\t"
      "ds_read_b64_tr_b16 a[86:87], v23 offset:25216\n\t"
      "global_load_dwordx4 a[128:131], v26, s[42:43] offset:1024 sc1\n\t"
      "v_mfma_f32_16x16x32_bf16 v[148:151], a[28:31], a[180:183], v[148:151]\n\t"
      "v_mfma_f32_16x16x32_bf16 v[152:155], a[28:31], a[184:187], v[152:155]\n\t"
      "ds_read_b64_tr_b16 a[88:89], v23 offset:21664\n\t"
      "v_mfma_f32_16x16x32_bf16 v[156:159], a[28:31], a[188:191], v[156:159]\n\t"
      "v_mfma_f32_16x16x32_bf16 v[160:163], a[32:35], a[176:179], v[160:163]\n\t"
      "ds_read_b64_tr_b16 a[90:91], v23 offset:25248\n\t"
      "v_mfma_f32_16x16x32_bf16 v[164:167], a[32:35], a[180:183], v[164:167]\n\t"
      "v_mfma_f32_16x16x32_bf16 v[168:171], a[32:35], a[184:187], v[168:171]\n\t"
      "ds_read_b64_tr_b16 a[92:93], v23 offset:21696\n\t"
      "global_load_dwordx4 a[132:135], v27, s[42:43] offset:1024 sc1\n\t"
      "v_mfma_f32_16x16x32_bf16 v[172:175], a[32:35], a[188:191], v[172:175]\n\t"
      "v_mfma_f32_16x16x32_bf16 v[176:179], a[36:39], a[176:179], v[176:179]\n\t"
      "ds_read_b64_tr_b16 a[94:95], v23 offset:25280\n\t"
      "v_mfma_f32_16x16x32_bf16 v[180:183], a[36:39], a[180:183], v[180:183]\n\t"
      "v_mfma_f32_16x16x32_bf16 v[184:187], a[36:39], a[184:187], v[184:187]\n\t"
      "ds_read_b64_tr_b16 a[96:97], v23 offset:21728\n\t"
      "v_mfma_f32_16x16x32_bf16 v[188:191], a[36:39], a[188:191], v[188:191]\n\t"
      "v_mfma_f32_16x16x32_bf16 v[192:195], a[40:43], a[176:179], v[192:195]\n\t"
      "ds_read_b64_tr_b16 a[98:99], v23 offset:25312\n\t"
      "global_load_dwordx4 a[136:139], v28, s[42:43] offset:1024 sc1\n\t"
      "v_mfma_f32_16x16x32_bf16 v[196:199], a[40:43], a[180:183], v[196:199]\n\t"
      "ds_read_b64_tr_b16 a[100:101], v23 offset:21760\n\t"
      "v_mfma_f32_16x16x32_bf16 v[200:203], a[40:43], a[184:187], v[200:203]\n\t"
      "s_add_u32 s47, s47, 28928\n\t"
      "v_mfma_f32_16x16x32_bf16 v[204:207], a[40:43], a[188:191], v[204:207]\n\t"
      "ds_read_b64_tr_b16 a[102:103], v23 offset:25344\n\t"
      "v_mfma_f32_16x16x32_bf16 v[208:211], a[44:47], a[176:179], v[208:211]\n\t"
      "s_cmp_eq_u32 s47, 115712\n\t"
      "s_cselect_b32 s47, 0, s47\n\t"
      "v_mfma_f32_16x16x32_bf16 v[212:215], a[44:47], a[180:183], v[212:215]\n\t"
      "ds_read_b64_tr_b16 a[104:105], v23 offset:21792\n\t"
      "v_mfma_f32_16x16x32_bf16 v[216:219], a[44:47], a[184:187], v[216:219]\n\t"
      "global_load_dwordx4 a[140:143], v29, s[42:43] offset:1024 sc1\n\t"
      "v_mfma_f32_16x16x32_bf16 v[220:223], a[44:47], a[188:191], v[220:223]\n\t"
      "ds_read_b64_tr_b16 a[106:107], v23 offset:25376\n\t"
      "v_mfma_f32_16x16x32_bf16 v[224:227], a[48:51], a[176:179], v[224:227]\n\t"
      "v_add_u32 v24, s47, v25\n\t"
      "v_mfma_f32_16x16x32_bf16 v[228:231], a[48:51], a[180:183], v[228:231]\n\t"
      "ds_read_b64_tr_b16 a[108:109], v23 offset:21824\n\t"
      "v_mfma_f32_16x16x32_bf16 v[232:235], a[48:51], a[184:187], v[232:235]\n\t"
      "s_add_u32 s42, s42, 0x800\n\t"
      "s_addc_u32 s43, s43, 0\n\t"
      "v_mfma_f32_16x16x32_bf16 v[236:239], a[48:51], a[188:191], v[236:239]\n\t"
      "ds_read_b64_tr_b16 a[110:111], v23 offset:25408\n\t"
      "v_mfma_f32_16x16x32_bf16 v[240:243], a[52:55], a[176:179], v[240:243]\n\t"
      "global_load_lds_dwordx4 v16, s[40:41]\n\t"
      "buffer_load_dword v30, v14, %[wsrd], s53 offen\n\t"
      "s_add_u32 m0, m0, 0x400\n\t"
      "v_mfma_f32_16x16x32_bf16 v[244:247], a[52:55], a[180:183], v[244:247]\n\t"
      "v_mfma_f32_16x16x32_bf16 v[248:251], a[52:55], a[184:187], v[248:251]\n\t"
      "v_mfma_f32_16x16x32_bf16 v[252:255], a[52:55], a[188:191], v[252:255]\n\t"
      "s_waitcnt vmcnt(37)\n\t"
      "s_waitcnt lgkmcnt(0)\n\t"
      "v_mfma_f32_16x16x32_bf16 v[32:35], a[56:59], a[192:195], v[32:35]\n\t"
      "s_add_u32 s53, s53, 0x70000\n\t"
      "v_mfma_f32_16x16x32_bf16 v[36:39], a[56:59], a[196:199], v[36:39]\n\t"
      "v_mfma_f32_16x16x32_bf16 v[40:43], a[56:59], a[200:203], v[40:43]\n\t"
      "global_load_lds_dwordx4 v17, s[40:41]\n\t"
      "s_add_u32 m0, m0, 0x400\n\t"
      "v_mfma_f32_16x16x32_bf16 v[44:47], a[56:59], a[204:207], v[44:47]\n\t"
      "v_mfma_f32_16x16x32_bf16 v[48:51], a[60:63], a[192:195], v[48:51]\n\t"
      "v_mfma_f32_16x16x32_bf16 v[52:55], a[60:63], a[196:199], v[52:55]\n\t"
      "v_mfma_f32_16x16x32_bf16 v[56:59], a[60:63], a[200:203], v[56:59]\n\t"
      "v_mfma_f32_16x16x32_bf16 v[60:63], a[60:63], a[204:207], v[60:63]\n\t"
      "v_mfma_f32_16x16x32_bf16 v[64:67], a[64:67], a[192:195], v[64:67]\n\t"
      "s_waitcnt lgkmcnt(0)\n\t"
      "s_waitcnt vmcnt(8)\n\t"
      "s_barrier\n\t"
      "s_mov_b64 exec, 1\n\t"
      "v_mov_b32 v12, %[eplo]\n\t"
      "v_mov_b32 v13, %[ephi]\n\t"
      "v_mov_b32 v15, 0\n\t"
      "global_store_dwordx2 v15, v[12:13], %[fu] sc1\n\t"
      "s_mov_b64 exec, -1\n\t"
      "global_load_lds_dwordx4 v18, s[40:41]\n\t"
      "s_add_u32 m0, m0, 0x400\n\t"
      "v_mfma_f32_16x16x32_bf16 v[68:71], a[64:67], a[196:199], v[68:71]\n\t"
      "ds_read_b64_tr_b16 a[0:1], v24 offset:0\n\t"
      "v_mfma_f32_16x16x32_bf16 v[72:75], a[64:67], a[200:203], v[72:75]\n\t"
      "ds_read_b64_tr_b16 a[2:3], v24 offset:3584\n\t"
      "v_mfma_f32_16x16x32_bf16 v[76:79], a[64:67], a[204:207], v[76:79]\n\t"
      "ds_read_b64_tr_b16 a[4:5], v24 offset:32\n\t"
      "v_mfma_f32_16x16x32_bf16 v[80:83], a[68:71], a[192:195], v[80:83]\n\t"
      "ds_read_b64_tr_b16 a[6:7], v24 offset:3616\n\t"
      "v_mfma_f32_16x16x32_bf16 v[84:87], a[68:71], a[196:199], v[84:87]\n\t"
      "ds_read_b64_tr_b16 a[8:9], v24 offset:64\n\t"
      "v_mfma_f32_16x16x32_bf16 v[88:91], a[68:71], a[200:203], v[88:91]\n\t"
      "ds_read_b64_tr_b16 a[10:11], v24 offset:3648\n\t"
      "global_load_lds_dwordx4 v19, s[40:41]\n\t"
      "s_add_u32 m0, m0, 0x400\n\t"
      "v_mfma_f32_16x16x32_bf16 v[92:95], a[68:71], a[204:207], v[92:95]\n\t"
      "ds_read_b64_tr_b16 a[12:13], v24 offset:96\n\t"
      "v_mfma_f32_16x16x32_bf16 v[96:99], a[72:75], a[192:195], v[96:99]\n\t"
      "ds_read_b64_tr_b16 a[14:15], v24 offset:3680\n\t"
      "v_mfma_f32_16x16x32_bf16 v[100:103], a[72:75], a[196:199], v[100:103]\n\t"
      "ds_read_b64_tr_b16 a[16:17], v24 offset:128\n\t"
      "v_mfma_f32_16x16x32_bf16 v[104:107], a[72:75], a[200:203], v[104:107]\n\t"
      "ds_read_b64_tr_b16 a[18:19], v24 offset:3712\n\t"
      "v_mfma_f32_16x16x32_bf16 v[108:111], a[72:75], a[204:207], v[108:111]\n\t"
      "ds_read_b64_tr_b16 a[20:21], v24 offset:160\n\t"
      "v_mfma_f32_16x16x32_bf16 v[112:115], a[76:79], a[192:195], v[112:115]\n\t"
      "global_load_lds_dwordx4 v20, s[40:41]\n\t"
      "s_add_u32 m0, m0, 0x400\n\t"
      "v_mfma_f32_16x16x32_bf16 v[116:119], a[76:79], a[196:199], v[116:119]\n\t"
      "ds_read_b64_tr_b16 a[22:23], v24 offset:3744\n\t"
      "v_mfma_f32_16x16x32_bf16 v[120:123], a[76:79], a[200:203], v[120:123]\n\t"
      "v_mfma_f32_16x16x32_bf16 v[124:127], a[76:79], a[204:207], v[124:127]\n\t"
      "ds_read_b64_tr_b16 a[24:25], v24 offset:192\n\t"
      "v_mfma_f32_16x16x32_bf16 v[128:131], a[80:83], a[192:195], v[128:131]\n\t"
      "v_mfma_f32_16x16x32_bf16 v[132:135], a[80:83], a[196:199], v[132:135]\n\t"
      "ds_read_b64_tr_b16 a[26:27], v24 offset:3776\n\t"
      "v_mfma_f32_16x16x32_bf16 v[136:139], a[80:83], a[200:203], v[136:139]\n\t"
      "global_load_lds_dwordx4 v21, s[40:41]\n\t"
      "s_add_u32 m0, m0, 0x400\n\t"
      "v_mfma_f32_16x16x32_bf16 v[140:143], a[80:83], a[204:207], v[140:143]\n\t"
      "ds_read_b64_tr_b16 a[28:29], v24 offset:14464\n\t"
      "v_mfma_f32_16x16x32_bf16 v[144:147], a[84:87], a[192:195], v[144:147]\n\t"
      "ds_read_b64_tr_b16 a[30:31], v24 offset:18048\n\t"
      "v_mfma_f32_16x16x32_bf16 v[148:151], a[84:87], a[196:199], v[148:151]\n\t"
      "v_mfma_f32_16x16x32_bf16 v[152:155], a[84:87], a[200:203], v[152:155]\n\t"
      "ds_read_b64_tr_b16 a[32:33], v24 offset:14496\n\t"
      "v_mfma_f32_16x16x32_bf16 v[156:159], a[84:87], a[204:207], v[156:159]\n\t"
      "v_mfma_f32_16x16x32_bf16 v[160:163], a[88:91], a[192:195], v[160:163]\n\t"
      "ds_read_b64_tr_b16 a[34:35], v24 offset:18080\n\t"
      "global_load_lds_dwordx4 v22, s[40:41]\n\t"
      "v_mfma_f32_16x16x32_bf16 v[164:167], a[88:91], a[196:199], v[164:167]\n\t"
      "s_add_u32 s40, s40, 0x70000\n\t"
      "s_addc_u32 s41, s41, 0\n\t"
      "v_mfma_f32_16x16x32_bf16 v[168:171], a[88:91], a[200:203], v[168:171]\n\t"
      "ds_read_b64_tr_b16 a[36:37], v24 offset:14528\n\t"
      "v_mfma_f32_16x16x32_bf16 v[172:175], a[88:91], a[204:207], v[172:175]\n\t"
      "s_add_u32 s46, s46, 28928\n\t"
      "v_mfma_f32_16x16x32_bf16 v[176:179], a[92:95], a[192:195], v[176:179]\n\t"
      "ds_read_b64_tr_b16 a[38:39], v24 offset:18112\n\t"
      "v_mfma_f32_16x16x32_bf16 v[180:183], a[92:95], a[196:199], v[180:183]\n\t"
      "s_cmp_eq_u32 s46, s50\n\t"
      "s_cselect_b32 s46, s49, s46\n\t"
      "v_mfma_f32_16x16x32_bf16 v[184:187], a[92:95], a[200:203], v[184:187]\n\t"
      "ds_read_b64_tr_b16 a[40:41], v24 offset:14560\n\t"
      "global_load_dwordx4 a[144:147], v26, s[42:43] offset:0 sc1\n\t"
      "v_mfma_f32_16x16x32_bf16 v[188:191], a[92:95], a[204:207], v[188:191]\n\t"
      "s_mov_b32 m0, s46\n\t"
      "v_mfma_f32_16x16x32_bf16 v[192:195], a[96:99], a[192:195], v[192:195]\n\t"
      "ds_read_b64_tr_b16 a[42:43], v24 offset:18144\n\t"
      "v_mfma_f32_16x16x32_bf16 v[196:199], a[96:99], a[196:199], v[196:199]\n\t"
      "ds_read_b64_tr_b16 a[44:45], v24 offset:14592\n\t"
      "v_mfma_f32_16x16x32_bf16 v[200:203], a[96:99], a[200:203], v[200:203]\n\t"
      "v_mfma_f32_16x16x32_bf16 v[204:207], a[96:99], a[204:207], v[204:207]\n\t"
      "ds_read_b64_tr_b16 a[46:47], v24 offset:18176\n\t"
      "v_mfma_f32_16x16x32_bf16 v[208:211], a[100:103], a[192:195], v[208:211]\n\t"
      "global_load_dwordx4 a[148:151], v27, s[42:43] offset:0 sc1\n\t"
      "v_mfma_f32_16x16x32_bf16 v[212:215], a[100:103], a[196:199], v[212:215]\n\t"
      "ds_read_b64_tr_b16 a[48:49], v24 offset:14624\n\t"
      "v_mfma_f32_16x16x32_bf16 v[216:219], a[100:103], a[200:203], v[216:219]\n\t"
      "v_mfma_f32_16x16x32_bf16 v[220:223], a[100:103], a[204:207], v[220:223]\n\t"
      "ds_read_b64_tr_b16 a[50:51], v24 offset:18208\n\t"
      "v_mfma_f32_16x16x32_bf16 v[224:227], a[104:107], a[192:195], v[224:227]\n\t"
      "v_mfma_f32_16x16x32_bf16 v[228:231], a[104:107], a[196:199], v[228:231]\n\t"
      "ds_read_b64_tr_b16 a[52:53], v24 offset:14656\n\t"
      "v_mfma_f32_16x16x32_bf16 v[232:235], a[104:107], a[200:203], v[232:235]\n\t"
      "global_load_dwordx4 a[152:155], v28, s[42:43] offset:0 sc1\n\t"
      "v_mfma_f32_16x16x32_bf16 v[236:239], a[104:107], a[204:207], v[236:239]\n\t"
      "ds_read_b64_tr_b16 a[54:55], v24 offset:18240\n\t"
      "v_mfma_f32_16x16x32_bf16 v[240:243], a[108:111], a[192:195], v[240:243]\n\t"
      "v_mfma_f32_16x16x32_bf16 v[244:247], a[108:111], a[196:199], v[244:247]\n\t"
      "v_mfma_f32_16x16x32_bf16 v[248:251], a[108:111], a[200:203], v[248:251]\n\t"
      "v_mfma_f32_16x16x32_bf16 v[252:255], a[108:111], a[204:207], v[252:255]\n\t"
      "s_waitcnt vmcnt(36)\n\t"
      "s_waitcnt lgkmcnt(0)\n\t"
      "v_mfma_f32_16x16x32_bf16 v[32:35], a[0:3], a[208:211], v[32:35]\n\t"
      "global_load_dwordx4 a[156:159], v29, s[42:43] offset:0 sc1\n\t"
      "v_mfma_f32_16x16x32_bf16 v[36:39], a[0:3], a[212:215], v[36:39]\n\t"
      "ds_read_b64_tr_b16 a[56:57], v24 offset:7168\n\t"
      "v_mfma_f32_16x16x32_bf16 v[40:43], a[0:3], a[216:219], v[40:43]\n\t"
      "ds_read_b64_tr_b16 a[58:59], v24 offset:10752\n\t"
      "v_mfma_f32_16x16x32_bf16 v[44:47], a[0:3], a[220:223], v[44:47]\n\t"
      "v_mfma_f32_16x16x32_bf16 v[48:51], a[4:7], a[208:211], v[48:51]\n\t"
      "ds_read_b64_tr_b16 a[60:61], v24 offset:7200\n\t"
      "v_mfma_f32_16x16x32_bf16 v[52:55], a[4:7], a[212:215], v[52:55]\n\t"
      "v_mfma_f32_16x16x32_bf16 v[56:59], a[4:7], a[216:219], v[56:59]\n\t"
      "ds_read_b64_tr_b16 a[62:63], v24 offset:10784\n\t"
      "global_load_dwordx4 a[160:163], v26, s[42:43] offset:1024 sc1\n\t"
      "v_mfma_f32_16x16x32_bf16 v[60:63], a[4:7], a[220:223], v[60:63]\n\t"
      "v_mfma_f32_16x16x32_bf16 v[64:67], a[8:11], a[208:211], v[64:67]\n\t"
      "ds_read_b64_tr_b16 a[64:65], v24 offset:7232\n\t"
      "v_mfma_f32_16x16x32_bf16 v[68:71], a[8:11], a[212:215], v[68:71]\n\t"
      "v_mfma_f32_16x16x32_bf16 v[72:75], a[8:11], a[216:219], v[72:75]\n\t"
      "ds_read_b64_tr_b16 a[66:67], v24 offset:10816\n\t"
      "v_mfma_f32_16x16x32_bf16 v[76:79], a[8:11], a[220:223], v[76:79]\n\t"
      "v_mfma_f32_16x16x32_bf16 v[80:83], a[12:15], a[208:211], v[80:83]\n\t"
      "ds_read_b64_tr_b16 a[68:69], v24 offset:7264\n\t"
      "global_load_dwordx4 a[164:167], v27, s[42:43] offset:1024 sc1\n\t"
      "v_mfma_f32_16x16x32_bf16 v[84:87], a[12:15], a[212:215], v[84:87]\n\t"
      "v_mfma_f32_16x16x32_bf16 v[88:91], a[12:15], a[216:219], v[88:91]\n\t"
      "ds_read_b64_tr_b16 a[70:71], v24 offset:10848\n\t"
      "v_mfma_f32_16x16x32_bf16 v[92:95], a[12:15], a[220:223], v[92:95]\n\t"
      "ds_read_b64_tr_b16 a[72:73], v24 offset:7296\n\t"
      "v_mfma_f32_16x16x32_bf16 v[96:99], a[16:19], a[208:211], v[96:99]\n\t"
      "v_mfma_f32_16x16x32_bf16 v[100:103], a[16:19], a[212:215], v[100:103]\n\t"
      "ds_read_b64_tr_b16 a[74:75], v24 offset:10880\n\t"
      "v_mfma_f32_16x16x32_bf16 v[104:107], a[16:19], a[216:219], v[104:107]\n\t"
      "global_load_dwordx4 a[168:171], v28, s[42:43] offset:1024 sc1\n\t"
      "v_mfma_f32_16x16x32_bf16 v[108:111], a[16:19], a[220:223], v[108:111]\n\t"
      "ds_read_b64_tr_b16 a[76:77], v24 offset:7328\n\t"
      "v_mfma_f32_16x16x32_bf16 v[112:115], a[20:23], a[208:211], v[112:115]\n\t"
      "v_mfma_f32_16x16x32_bf16 v[116:119], a[20:23], a[212:215], v[116:119]\n\t"
      "ds_read_b64_tr_b16 a[78:79], v24 offset:10912\n\t"
      "v_mfma_f32_16x16x32_bf16 v[120:123], a[20:23], a[216:219], v[120:123]\n\t"
      "v_mfma_f32_16x16x32_bf16 v[124:127], a[20:23], a[220:223], v[124:127]\n\t"
      "ds_read_b64_tr_b16 a[80:81], v24 offset:7360\n\t"
      "v_mfma_f32_16x16x32_bf16 v[128:131], a[24:27], a[208:211], v[128:131]\n\t"
      "global_load_dwordx4 a[172:175], v29, s[42:43] offset:1024 sc1\n\t"
      "v_mfma_f32_16x16x32_bf16 v[132:135], a[24:27], a[212:215], v[132:135]\n\t"
      "ds_read_b64_tr_b16 a[82:83], v24 offset:10944\n\t"
      "v_mfma_f32_16x16x32_bf16 v[136:139], a[24:27], a[216:219], v[136:139]\n\t"
      "s_add_u32 s42, s42, 0x800\n\t"
      "s_addc_u32 s43, s43, 0\n\t"
      "v_mfma_f32_16x16x32_bf16 v[140:143], a[24:27], a[220:223], v[140:143]\n\t"
      "ds_read_b64_tr_b16 a[84:85], v24 offset:21632\n\t"
      "v_mfma_f32_16x16x32_bf16 v[144:147], a[28:31], a[208:211], v[144:147]\n\t"
      "ds_read_b64_tr_b16 a[86:87], v24 offset:25216\n\t"
      "v_mfma_f32_16x16x32_bf16 v[148:151], a[28:31], a[212:215], v[148:151]\n\t"
      "v_mfma_f32_16x16x32_bf16 v[152:155], a[28:31], a[216:219], v[152:155]\n\t"
      "ds_read_b64_tr_b16 a[88:89], v24 offset:21664\n\t"
      "global_load_lds_dwordx4 v16, s[40:41]\n\t"
      "s_add_u32 m0, m0, 0x400\n\t"
      "v_mfma_f32_16x16x32_bf16 v[156:159], a[28:31], a[220:223], v[156:159]\n\t"
      "v_mfma_f32_16x16x32_bf16 v[160:163], a[32:35], a[208:211], v[160:163]\n\t"
      "ds_read_b64_tr_b16 a[90:91], v24 offset:25248\n\t"
      "v_mfma_f32_16x16x32_bf16 v[164:167], a[32:35], a[212:215], v[164:167]\n\t"
      "v_mfma_f32_16x16x32_bf16 v[168:171], a[32:35], a[216:219], v[168:171]\n\t"
      "ds_read_b64_tr_b16 a[92:93], v24 offset:21696\n\t"
      "v_mfma_f32_16x16x32_bf16 v[172:175], a[32:35], a[220:223], v[172:175]\n\t"
      "v_mfma_f32_16x16x32_bf16 v[176:179], a[36:39], a[208:211], v[176:179]\n\t"
      "ds_read_b64_tr_b16 a[94:95], v24 offset:25280\n\t"
      "global_load_lds_dwordx4 v17, s[40:41]\n\t"
      "s_add_u32 m0, m0, 0x400\n\t"
      "v_mfma_f32_16x16x32_bf16 v[180:183], a[36:39], a[212:215], v[180:183]\n\t"
      "v_mfma_f32_16x16x32_bf16 v[184:187], a[36:39], a[216:219], v[184:187]\n\t"
      "ds_read_b64_tr_b16 a[96:97], v24 offset:21728\n\t"
      "v_mfma_f32_16x16x32_bf16 v[188:191], a[36:39], a[220:223], v[188:191]\n\t"
      "v_mfma_f32_16x16x32_bf16 v[192:195], a[40:43], a[208:211], v[192:195]\n\t"
      "ds_read_b64_tr_b16 a[98:99], v24 offset:25312\n\t"
      "v_mfma_f32_16x16x32_bf16 v[196:199], a[40:43], a[212:215], v[196:199]\n\t"
      "ds_read_b64_tr_b16 a[100:101], v24 offset:21760\n\t"
      "v_mfma_f32_16x16x32_bf16 v[200:203], a[40:43], a[216:219], v[200:203]\n\t"
      "global_load_lds_dwordx4 v18, s[40:41]\n\t"
      "s_add_u32 m0, m0, 0x400\n\t"
      "v_mfma_f32_16x16x32_bf16 v[204:207], a[40:43], a[220:223], v[204:207]\n\t"
      "ds_read_b64_tr_b16 a[102:103], v24 offset:25344\n\t"
      "v_mfma_f32_16x16x32_bf16 v[208:211], a[44:47], a[208:211], v[208:211]\n\t"
      "s_add_u32 s47, s47, 28928\n\t"
      "v_mfma_f32_16x16x32_bf16 v[212:215], a[44:47], a[212:215], v[212:215]\n\t"
      "ds_read_b64_tr_b16 a[104:105], v24 offset:21792\n\t"
      "v_mfma_f32_16x16x32_bf16 v[216:219], a[44:47], a[216:219], v[216:219]\n\t"
      "s_cmp_eq_u32 s47, 115712\n\t"
      "s_cselect_b32 s47, 0, s47\n\t"
      "v_mfma_f32_16x16x32_bf16 v[220:223], a[44:47], a[220:223], v[220:223]\n\t"
      "ds_read_b64_tr_b16 a[106:107], v24 offset:25376\n\t"
      "v_mfma_f32_16x16x32_bf16 v[224:227], a[48:51], a[208:211], v[224:227]\n\t"
      "global_load_lds_dwordx4 v19, s[40:41]\n\t"
      "s_add_u32 m0, m0, 0x400\n\t"
      "v_mfma_f32_16x16x32_bf16 v[228:231], a[48:51], a[212:215], v[228:231]\n\t"
      "ds_read_b64_tr_b16 a[108:109], v24 offset:21824\n\t"
      "v_mfma_f32_16x16x32_bf16 v[232:235], a[48:51], a[216:219], v[232:235]\n\t"
      "v_add_u32 v23, s47, v25\n\t"
      "v_mfma_f32_16x16x32_bf16 v[236:239], a[48:51], a[220:223], v[236:239]\n\t"
      "ds_read_b64_tr_b16 a[110:111], v24 offset:25408\n\t"
      "v_mfma_f32_16x16x32_bf16 v[240:243], a[52:55], a[208:211], v[240:243]\n\t"
      "buffer_load_dword v30, v14, %[wsrd], s53 offen\n\t"
      "v_mfma_f32_16x16x32_bf16 v[244:247], a[52:55], a[212:215], v[244:247]\n\t"
      "v_mfma_f32_16x16x32_bf16 v[248:251], a[52:55], a[216:219], v[248:251]\n\t"
      "global_load_lds_dwordx4 v20, s[40:41]\n\t"
      "s_add_u32 m0, m0, 0x400\n\t"
      "v_mfma_f32_16x16x32_bf16 v[252:255], a[52:55], a[220:223], v[252:255]\n\t"
      "s_waitcnt vmcnt(41)\n\t"
      "s_waitcnt lgkmcnt(0)\n\t"
      "v_mfma_f32_16x16x32_bf16 v[32:35], a[56:59], a[224:227], v[32:35]\n\t"
      "s_add_u32 s53, s53, 0x70000\n\t"
      "v_mfma_f32_16x16x32_bf16 v[36:39], a[56:59], a[228:231], v[36:39]\n\t"
      "v_mfma_f32_16x16x32_bf16 v[40:43], a[56:59], a[232:235], v[40:43]\n\t"
      "v_mfma_f32_16x16x32_bf16 v[44:47], a[56:59], a[236:239], v[44:47]\n\t"
      "v_mfma_f32_16x16x32_bf16 v[48:51], a[60:63], a[224:227], v[48:51]\n\t"
      "global_load_lds_dwordx4 v21, s[40:41]\n\t"
      "s_add_u32 m0, m0, 0x400\n\t"
      "v_mfma_f32_16x16x32_bf16 v[52:55], a[60:63], a[228:231], v[52:55]\n\t"
      "v_mfma_f32_16x16x32_bf16 v[56:59], a[60:63], a[232:235], v[56:59]\n\t"
      "v_mfma_f32_16x16x32_bf16 v[60:63], a[60:63], a[236:239], v[60:63]\n\t"
      "v_mfma_f32_16x16x32_bf16 v[64:67], a[64:67], a[224:227], v[64:67]\n\t"
      "s_waitcnt lgkmcnt(0)\n\t"
      "s_waitcnt vmcnt(34)\n\t"
      "s_barrier\n\t"
      "v_mfma_f32_16x16x32_bf16 v[68:71], a[64:67], a[228:231], v[68:71]\n\t"
      "ds_read_b64_tr_b16 a[0:1], v23 offset:0\n\t"
      "v_mfma_f32_16x16x32_bf16 v[72:75], a[64:67], a[232:235], v[72:75]\n\t"
      "ds_read_b64_tr_b16 a[2:3], v23 offset:3584\n\t"
      "global_load_lds_dwordx4 v22, s[40:41]\n\t"
      "v_mfma_f32_16x16x32_bf16 v[76:79], a[64:67], a[236:239], v[76:79]\n\t"
      "ds_read_b64_tr_b16 a[4:5], v23 offset:32\n\t"
      "v_mfma_f32_16x16x32_bf16 v[80:83], a[68:71], a[224:227], v[80:83]\n\t"
      "ds_read_b64_tr_b16 a[6:7], v23 offset:3616\n\t"
      "v_mfma_f32_16x16x32_bf16 v[84:87], a[68:71], a[228:231], v[84:87]\n\t"
      "ds_read_b64_tr_b16 a[8:9], v23 offset:64\n\t"
      "v_mfma_f32_16x16x32_bf16 v[88:91], a[68:71], a[232:235], v[88:91]\n\t"
      "ds_read_b64_tr_b16 a[10:11], v23 offset:3648\n\t"
      "v_mfma_f32_16x16x32_bf16 v[92:95], a[68:71], a[236:239], v[92:95]\n\t"
      "ds_read_b64_tr_b16 a[12:13], v23 offset:96\n\t"
      "v_mfma_f32_16x16x32_bf16 v[96:99], a[72:75], a[224:227], v[96:99]\n\t"
      "ds_read_b64_tr_b16 a[14:15], v23 offset:3680\n\t"
      "global_load_dwordx4 a[176:179], v26, s[42:43] offset:0 sc1\n\t"
      "v_mfma_f32_16x16x32_bf16 v[100:103], a[72:75], a[228:231], v[100:103]\n\t"
      "ds_read_b64_tr_b16 a[16:17], v23 offset:128\n\t"
      "v_mfma_f32_16x16x32_bf16 v[104:107], a[72:75], a[232:235], v[104:107]\n\t"
      "ds_read_b64_tr_b16 a[18:19], v23 offset:3712\n\t"
      "s_add_u32 s40, s40, 0x70000\n\t"
      "s_addc_u32 s41, s41, 0\n\t"
      "s_add_u32 s46, s46, 28928\n\t"
      "s_cmp_eq_u32 s46, s50\n\t"
      "s_cselect_b32 s46, s49, s46\n\t"
      "s_mov_b32 m0, s46\n\t"
      "s_nop 0\n\t"
      "global_load_lds_dwordx4 v16, s[40:41]\n\t"
      "s_add_u32 m0, m0, 0x400\n\t"
      "v_mfma_f32_16x16x32_bf16 v[108:111], a[72:75], a[236:239], v[108:111]\n\t"
      "ds_read_b64_tr_b16 a[20:21], v23 offset:160\n\t"
      "v_mfma_f32_16x16x32_bf16 v[112:115], a[76:79], a[224:227], v[112:115]\n\t"
      "v_mfma_f32_16x16x32_bf16 v[116:119], a[76:79], a[228:231], v[116:119]\n\t"
      "ds_read_b64_tr_b16 a[22:23], v23 offset:3744\n\t"
      "v_mfma_f32_16x16x32_bf16 v[120:123], a[76:79], a[232:235], v[120:123]\n\t"
      "v_mfma_f32_16x16x32_bf16 v[124:127], a[76:79], a[236:239], v[124:127]\n\t"
      "ds_read_b64_tr_b16 a[24:25], v23 offset:192\n\t"
      "v_mfma_f32_16x16x32_bf16 v[128:131], a[80:83], a[224:227], v[128:131]\n\t"
      "global_load_dwordx4 a[180:183], v27, s[42:43] offset:0 sc1\n\t"
      "v_mfma_f32_16x16x32_bf16 v[132:135], a[80:83], a[228:231], v[132:135]\n\t"
      "ds_read_b64_tr_b16 a[26:27], v23 offset:3776\n\t"
      "v_mfma_f32_16x16x32_bf16 v[136:139], a[80:83], a[232:235], v[136:139]\n\t"
      "v_mfma_f32_16x16x32_bf16 v[140:143], a[80:83], a[236:239], v[140:143]\n\t"
      "ds_read_b64_tr_b16 a[28:29], v23 offset:14464\n\t"
      "v_mfma_f32_16x16x32_bf16 v[144:147], a[84:87], a[224:227], v[144:147]\n\t"
      "ds_read_b64_tr_b16 a[30:31], v23 offset:18048\n\t"
      "v_mfma_f32_16x16x32_bf16 v[148:151], a[84:87], a[228:231], v[148:151]\n\t"
      "v_mfma_f32_16x16x32_bf16 v[152:155], a[84:87], a[232:235], v[152:155]\n\t"
      "ds_read_b64_tr_b16 a[32:33], v23 offset:14496\n\t"
      "global_load_lds_dwordx4 v17, s[40:41]\n\t"
      "s_add_u32 m0, m0, 0x400\n\t"
      "v_mfma_f32_16x16x32_bf16 v[156:159], a[84:87], a[236:239], v[156:159]\n\t"
      "v_mfma_f32_16x16x32_bf16 v[160:163], a[88:91], a[224:227], v[160:163]\n\t"
      "ds_read_b64_tr_b16 a[34:35], v23 offset:18080\n\t"
      "v_mfma_f32_16x16x32_bf16 v[164:167], a[88:91], a[228:231], v[164:167]\n\t"
      "v_mfma_f32_16x16x32_bf16 v[168:171], a[88:91], a[232:235], v[168:171]\n\t"
      "ds_read_b64_tr_b16 a[36:37], v23 offset:14528\n\t"
      "v_mfma_f32_16x16x32_bf16 v[172:175], a[88:91], a[236:239], v[172:175]\n\t"
      "v_mfma_f32_16x16x32_bf16 v[176:179], a[92:95], a[224:227], v[176:179]\n\t"
      "ds_read_b64_tr_b16 a[38:39], v23 offset:18112\n\t"
      "global_load_dwordx4 a[184:187], v28, s[42:43] offset:0 sc1\n\t"
      "v_mfma_f32_16x16x32_bf16 v[180:183], a[92:95], a[228:231], v[180:183]\n\t"
      "v_mfma_f32_16x16x32_bf16 v[184:187], a[92:95], a[232:235], v[184:187]\n\t"
      "ds_read_b64_tr_b16 a[40:41], v23 offset:14560\n\t"
      "v_mfma_f32_16x16x32_bf16 v[188:191], a[92:95], a[236:239], v[188:191]\n\t"
      "v_mfma_f32_16x16x32_bf16 v[192:195], a[96:99], a[224:227], v[192:195]\n\t"
      "ds_read_b64_tr_b16 a[42:43], v23 offset:18144\n\t"
      "v_mfma_f32_16x16x32_bf16 v[196:199], a[96:99], a[228:231], v[196:199]\n\t"
      "ds_read_b64_tr_b16 a[44:45], v23 offset:14592\n\t"
      "v_mfma_f32_16x16x32_bf16 v[200:203], a[96:99], a[232:235], v[200:203]\n\t"
      "global_load_lds_dwordx4 v18, s[40:41]\n\t"
      "s_add_u32 m0, m0, 0x400\n\t"
      "v_mfma_f32_16x16x32_bf16 v[204:207], a[96:99], a[236:239], v[204:207]\n\t"
      "ds_read_b64_tr_b16 a[46:47], v23 offset:18176\n\t"
      "v_mfma_f32_16x16x32_bf16 v[208:211], a[100:103], a[224:227], v[208:211]\n\t"
      "v_mfma_f32_16x16x32_bf16 v[212:215], a[100:103], a[228:231], v[212:215]\n\t"
      "ds_read_b64_tr_b16 a[48:49], v23 offset:14624\n\t"
      "v_mfma_f32_16x16x32_bf16 v[216:219], a[100:103], a[232:235], v[216:219]\n\t"
      "v_mfma_f32_16x16x32_bf16 v[220:223], a[100:103], a[236:239], v[220:223]\n\t"
      "ds_read_b64_tr_b16 a[50:51], v23 offset:18208\n\t"
      "v_mfma_f32_16x16x32_bf16 v[224:227], a[104:107], a[224:227], v[224:227]\n\t"
      "global_load_dwordx4 a[188:191], v29, s[42:43] offset:0 sc1\n\t"
      "v_mfma_f32_16x16x32_bf16 v[228:231], a[104:107], a[228:231], v[228:231]\n\t"
      "ds_read_b64_tr_b16 a[52:53], v23 offset:14656\n\t"
      "v_mfma_f32_16x16x32_bf16 v[232:235], a[104:107], a[232:235], v[232:235]\n\t"
      "v_mfma_f32_16x16x32_bf16 v[236:239], a[104:107], a[236:239], v[236:239]\n\t"
      "ds_read_b64_tr_b16 a[54:55], v23 offset:18240\n\t"
      "v_mfma_f32_16x16x32_bf16 v[240:243], a[108:111], a[224:227], v[240:243]\n\t"
      "v_mfma_f32_16x16x32_bf16 v[244:247], a[108:111], a[228:231], v[244:247]\n\t"
      "v_mfma_f32_16x16x32_bf16 v[248:251], a[108:111], a[232:235], v[248:251]\n\t"
      "global_load_lds_dwordx4 v19, s[40:41]\n\t"
      "s_add_u32 m0, m0, 0x400\n\t"
      "v_mfma_f32_16x16x32_bf16 v[252:255], a[108:111], a[236:239], v[252:255]\n\t"
      "s_waitcnt vmcnt(37)\n\t"
      "s_waitcnt lgkmcnt(0)\n\t"
      "v_mfma_f32_16x16x32_bf16 v[32:35], a[0:3], a[112:115], v[32:35]\n\t"
      "v_mfma_f32_16x16x32_bf16 v[36:39], a[0:3], a[116:119], v[36:39]\n\t"
      "ds_read_b64_tr_b16 a[56:57], v23 offset:7168\n\t"
      "v_mfma_f32_16x16x32_bf16 v[40:43], a[0:3], a[120:123], v[40:43]\n\t"
      "ds_read_b64_tr_b16 a[58:59], v23 offset:10752\n\t"
      "v_mfma_f32_16x16x32_bf16 v[44:47], a[0:3], a[124:127], v[44:47]\n\t"
      "v_mfma_f32_16x16x32_bf16 v[48:51], a[4:7], a[112:115], v[48:51]\n\t"
      "ds_read_b64_tr_b16 a[60:61], v23 offset:7200\n\t"
      "global_load_dwordx4 a[192:195], v26, s[42:43] offset:1024 sc1\n\t"
      "v_mfma_f32_16x16x32_bf16 v[52:55], a[4:7], a[116:119], v[52:55]\n\t"
      "v_mfma_f32_16x16x32_bf16 v[56:59], a[4:7], a[120:123], v[56:59]\n\t"
      "ds_read_b64_tr_b16 a[62:63], v23 offset:10784\n\t"
      "v_mfma_f32_16x16x32_bf16 v[60:63], a[4:7], a[124:127], v[60:63]\n\t"
      "v_mfma_f32_16x16x32_bf16 v[64:67], a[8:11], a[112:115], v[64:67]\n\t"
      "ds_read_b64_tr_b16 a[64:65], v23 offset:7232\n\t"
      "v_mfma_f32_16x16x32_bf16 v[68:71], a[8:11], a[116:119], v[68:71]\n\t"
      "v_mfma_f32_16x16x32_bf16 v[72:75], a[8:11], a[120:123], v[72:75]\n\t"
      "ds_read_b64_tr_b16 a[66:67], v23 offset:10816\n\t"
      "global_load_lds_dwordx4 v20, s[40:41]\n\t"
      "s_add_u32 m0, m0, 0x400\n\t"
      "v_mfma_f32_16x16x32_bf16 v[76:79], a[8:11], a[124:127], v[76:79]\n\t"
      "v_mfma_f32_16x16x32_bf16 v[80:83], a[12:15], a[112:115], v[80:83]\n\t"
      "ds_read_b64_tr_b16 a[68:69], v23 offset:7264\n\t"
      "v_mfma_f32_16x16x32_bf16 v[84:87], a[12:15], a[116:119], v[84:87]\n\t"
      "v_mfma_f32_16x16x32_bf16 v[88:91], a[12:15], a[120:123], v[88:91]\n\t"
      "ds_read_b64_tr_b16 a[70:71], v23 offset:10848\n\t"
      "v_mfma_f32_16x16x32_bf16 v[92:95], a[12:15], a[124:127], v[92:95]\n\t"
      "ds_read_b64_tr_b16 a[72:73], v23 offset:7296\n\t"
      "v_mfma_f32_16x16x32_bf16 v[96:99], a[16:19], a[112:115], v[96:99]\n\t"
      "global_load_dwordx4 a[196:199], v27, s[42:43] offset:1024 sc1\n\t"
      "v_mfma_f32_16x16x32_bf16 v[100:103], a[16:19], a[116:119], v[100:103]\n\t"
      "ds_read_b64_tr_b16 a[74:75], v23 offset:10880\n\t"
      "v_mfma_f32_16x16x32_bf16 v[104:107], a[16:19], a[120:123], v[104:107]\n\t"
      "v_mfma_f32_16x16x32_bf16 v[108:111], a[16:19], a[124:127], v[108:111]\n\t"
      "ds_read_b64_tr_b16 a[76:77], v23 offset:7328\n\t"
      "v_mfma_f32_16x16x32_bf16 v[112:115], a[20:23], a[112:115], v[112:115]\n\t"
      "v_mfma_f32_16x16x32_bf16 v[116:119], a[20:23], a[116:119], v[116:119]\n\t"
      "ds_read_b64_tr_b16 a[78:79], v23 offset:10912\n\t"
      "v_mfma_f32_16x16x32_bf16 v[120:123], a[20:23], a[120:123], v[120:123]\n\t"
      "global_load_dwordx4 a[200:203], v28, s[42:43] offset:1024 sc1\n\t"
      "v_mfma_f32_16x16x32_bf16 v[124:127], a[20:23], a[124:127], v[124:127]\n\t"
      "ds_read_b64_tr_b16 a[80:81], v23 offset:7360\n\t"
      "v_mfma_f32_16x16x32_bf16 v[128:131], a[24:27], a[112:115], v[128:131]\n\t"
      "v_mfma_f32_16x16x32_bf16 v[132:135], a[24:27], a[116:119], v[132:135]\n\t"
      "ds_read_b64_tr_b16 a[82:83], v23 offset:10944\n\t"
      "v_mfma_f32_16x16x32_bf16 v[136:139], a[24:27], a[120:123], v[136:139]\n\t"
      "global_load_lds_dwordx4 v21, s[40:41]\n\t"
      "s_add_u32 m0, m0, 0x400\n\t"
      "v_mfma_f32_16x16x32_bf16 v[140:143], a[24:27], a[124:127], v[140:143]\n\t"
      "ds_read_b64_tr_b16 a[84:85], v23 offset:21632\n\t"
      "v_mfma_f32_16x16x32_bf16 v[144:147], a[28:31], a[112:115], v[144:147]\n\t"
      "ds_read_b64_tr_b16 a[86:87], v23 offset:25216\n\t"
      "v_mfma_f32_16x16x32_bf16 v[148:151], a[28:31], a[116:119], v[148:151]\n\t"
      "v_mfma_f32_16x16x32_bf16 v[152:155], a[28:31], a[120:123], v[152:155]\n\t"
      "ds_read_b64_tr_b16 a[88:89], v23 offset:21664\n\t"
      "v_mfma_f32_16x16x32_bf16 v[156:159], a[28:31], a[124:127], v[156:159]\n\t"
      "v_mfma_f32_16x16x32_bf16 v[160:163], a[32:35], a[112:115], v[160:163]\n\t"
      "ds_read_b64_tr_b16 a[90:91], v23 offset:25248\n\t"
      "global_load_dwordx4 a[204:207], v29, s[42:43] offset:1024 sc1\n\t"
      "v_mfma_f32_16x16x32_bf16 v[164:167], a[32:35], a[116:119], v[164:167]\n\t"
      "s_add_u32 s42, s42, 0x800\n\t"
      "s_addc_u32 s43, s43, 0\n\t"
      "v_mfma_f32_16x16x32_bf16 v[168:171], a[32:35], a[120:123], v[168:171]\n\t"
      "ds_read_b64_tr_b16 a[92:93], v23 offset:21696\n\t"
      "v_mfma_f32_16x16x32_bf16 v[172:175], a[32:35], a[124:127], v[172:175]\n\t"
      "v_mfma_f32_16x16x32_bf16 v[176:179], a[36:39], a[112:115], v[176:179]\n\t"
      "ds_read_b64_tr_b16 a[94:95], v23 offset:25280\n\t"
      "v_mfma_f32_16x16x32_bf16 v[180:183], a[36:39], a[116:119], v[180:183]\n\t"
      "v_mfma_f32_16x16x32_bf16 v[184:187], a[36:39], a[120:123], v[184:187]\n\t"
      "ds_read_b64_tr_b16 a[96:97], v23 offset:21728\n\t"
      "global_load_dwordx4 a[208:211], v26, s[42:43] offset:0 sc1\n\t"
      "v_mfma_f32_16x16x32_bf16 v[188:191], a[36:39], a[124:127], v[188:191]\n\t"
      "v_mfma_f32_16x16x32_bf16 v[192:195], a[40:43], a[112:115], v[192:195]\n\t"
      "ds_read_b64_tr_b16 a[98:99], v23 offset:25312\n\t"
      "v_mfma_f32_16x16x32_bf16 v[196:199], a[40:43], a[116:119], v[196:199]\n\t"
      "ds_read_b64_tr_b16 a[100:101], v23 offset:21760\n\t"
      "v_mfma_f32_16x16x32_bf16 v[200:203], a[40:43], a[120:123], v[200:203]\n\t"
      "global_load_lds_dwordx4 v22, s[40:41]\n\t"
      "s_add_u32 s47, s47, 28928\n\t"
      "v_mfma_f32_16x16x32_bf16 v[204:207], a[40:43], a[124:127], v[204:207]\n\t"
      "ds_read_b64_tr_b16 a[102:103], v23 offset:25344\n\t"
      "s_cmp_eq_u32 s47, 115712\n\t"
      "s_cselect_b32 s47, 0, s47\n\t"
      "v_mfma_f32_16x16x32_bf16 v[208:211], a[44:47], a[112:115], v[208:211]\n\t"
      "v_add_u32 v24, s47, v25\n\t"
      "v_mfma_f32_16x16x32_bf16 v[212:215], a[44:47], a[116:119], v[212:215]\n\t"
      "ds_read_b64_tr_b16 a[104:105], v23 offset:21792\n\t"
      "v_mfma_f32_16x16x32_bf16 v[216:219], a[44:47], a[120:123], v[216:219]\n\t"
      "s_add_u32 s40, s40, 0x70000\n\t"
      "s_addc_u32 s41, s41, 0\n\t"
      "v_mfma_f32_16x16x32_bf16 v[220:223], a[44:47], a[124:127], v[220:223]\n\t"
      "ds_read_b64_tr_b16 a[106:107], v23 offset:25376\n\t"
      "v_mfma_f32_16x16x32_bf16 v[224:227], a[48:51], a[112:115], v[224:227]\n\t"
      "global_load_dwordx4 a[212:215], v27, s[42:43] offset:0 sc1\n\t"
      "v_mfma_f32_16x16x32_bf16 v[228:231], a[48:51], a[116:119], v[228:231]\n\t"
      "ds_read_b64_tr_b16 a[108:109], v23 offset:21824\n\t"
      "v_mfma_f32_16x16x32_bf16 v[232:235], a[48:51], a[120:123], v[232:235]\n\t"
      "s_add_u32 s46, s46, 28928\n\t"
      "v_mfma_f32_16x16x32_bf16 v[236:239], a[48:51], a[124:127], v[236:239]\n\t"
      "ds_read_b64_tr_b16 a[110:111], v23 offset:25408\n\t"
      "v_mfma_f32_16x16x32_bf16 v[240:243], a[52:55], a[112:115], v[240:243]\n\t"
      "buffer_load_dword v30, v14, %[wsrd], s53 offen\n\t"
      "v_mfma_f32_16x16x32_bf16 v[244:247], a[52:55], a[116:119], v[244:247]\n\t"
      "s_cmp_eq_u32 s46, s50\n\t"
      "s_cselect_b32 s46, s49, s46\n\t"
      "v_mfma_f32_16x16x32_bf16 v[248:251], a[52:55], a[120:123], v[248:251]\n\t"
      "global_load_dwordx4 a[216:219], v28, s[42:43] offset:0 sc1\n\t"
      "v_mfma_f32_16x16x32_bf16 v[252:255], a[52:55], a[124:127], v[252:255]\n\t"
      "s_mov_b32 m0, s46\n\t"
      "s_waitcnt vmcnt(44)\n\t"
      "s_waitcnt lgkmcnt(0)\n\t"
      "v_mfma_f32_16x16x32_bf16 v[32:35], a[56:59], a[128:131], v[32:35]\n\t"
      "s_add_u32 s53, s53, 0x70000\n\t"
      "v_mfma_f32_16x16x32_bf16 v[36:39], a[56:59], a[132:135], v[36:39]\n\t"
      "v_mfma_f32_16x16x32_bf16 v[40:43], a[56:59], a[136:139], v[40:43]\n\t"
      "v_mfma_f32_16x16x32_bf16 v[44:47], a[56:59], a[140:143], v[44:47]\n\t"
      "v_mfma_f32_16x16x32_bf16 v[48:51], a[60:63], a[128:131], v[48:51]\n\t"
      "global_load_dwordx4 a[220:223], v29, s[42:43] offset:0 sc1\n\t"
      "v_mfma_f32_16x16x32_bf16 v[52:55], a[60:63], a[132:135], v[52:55]\n\t"
      "v_mfma_f32_16x16x32_bf16 v[56:59], a[60:63], a[136:139], v[56:59]\n\t"
      "v_mfma_f32_16x16x32_bf16 v[60:63], a[60:63], a[140:143], v[60:63]\n\t"
      "v_mfma_f32_16x16x32_bf16 v[64:67], a[64:67], a[128:131], v[64:67]\n\t"
      "s_waitcnt lgkmcnt(0)\n\t"
      "s_waitcnt vmcnt(36)\n\t"
      "s_barrier\n\t"
      "v_mfma_f32_16x16x32_bf16 v[68:71], a[64:67], a[132:135], v[68:71]\n\t"
      "ds_read_b64_tr_b16 a[0:1], v24 offset:0\n\t"
      "v_mfma_f32_16x16x32_bf16 v[72:75], a[64:67], a[136:139], v[72:75]\n\t"
      "ds_read_b64_tr_b16 a[2:3], v24 offset:3584\n\t"
      "global_load_dwordx4 a[224:227], v26, s[42:43] offset:1024 sc1\n\t"
      "v_mfma_f32_16x16x32_bf16 v[76:79], a[64:67], a[140:143], v[76:79]\n\t"
      "ds_read_b64_tr_b16 a[4:5], v24 offset:32\n\t"
      "v_mfma_f32_16x16x32_bf16 v[80:83], a[68:71], a[128:131], v[80:83]\n\t"
      "ds_read_b64_tr_b16 a[6:7], v24 offset:3616\n\t"
      "v_mfma_f32_16x16x32_bf16 v[84:87], a[68:71], a[132:135], v[84:87]\n\t"
      "ds_read_b64_tr_b16 a[8:9], v24 offset:64\n\t"
      "v_mfma_f32_16x16x32_bf16 v[88:91], a[68:71], a[136:139], v[88:91]\n\t"
      "ds_read_b64_tr_b16 a[10:11], v24 offset:3648\n\t"
      "v_mfma_f32_16x16x32_bf16 v[92:95], a[68:71], a[140:143], v[92:95]\n\t"
      "ds_read_b64_tr_b16 a[12:13], v24 offset:96\n\t"
      "v_mfma_f32_16x16x32_bf16 v[96:99], a[72:75], a[128:131], v[96:99]\n\t"
      "ds_read_b64_tr_b16 a[14:15], v24 offset:3680\n\t"
      "global_load_dwordx4 a[228:231], v27, s[42:43] offset:1024 sc1\n\t"
      "v_mfma_f32_16x16x32_bf16 v[100:103], a[72:75], a[132:135], v[100:103]\n\t"
      "ds_read_b64_tr_b16 a[16:17], v24 offset:128\n\t"
      "v_mfma_f32_16x16x32_bf16 v[104:107], a[72:75], a[136:139], v[104:107]\n\t"
      "ds_read_b64_tr_b16 a[18:19], v24 offset:3712\n\t"
      "global_load_lds_dwordx4 v16, s[40:41]\n\t"
      "s_add_u32 m0, m0, 0x400\n\t"
      "v_mfma_f32_16x16x32_bf16 v[108:111], a[72:75], a[140:143], v[108:111]\n\t"
      "ds_read_b64_tr_b16 a[20:21], v24 offset:160\n\t"
      "v_mfma_f32_16x16x32_bf16 v[112:115], a[76:79], a[128:131], v[112:115]\n\t"
      "v_mfma_f32_16x16x32_bf16 v[116:119], a[76:79], a[132:135], v[116:119]\n\t"
      "ds_read_b64_tr_b16 a[22:23], v24 offset:3744\n\t"
      "v_mfma_f32_16x16x32_bf16 v[120:123], a[76:79], a[136:139], v[120:123]\n\t"
      "v_mfma_f32_16x16x32_bf16 v[124:127], a[76:79], a[140:143], v[124:127]\n\t"
      "ds_read_b64_tr_b16 a[24:25], v24 offset:192\n\t"
      "v_mfma_f32_16x16x32_bf16 v[128:131], a[80:83], a[128:131], v[128:131]\n\t"
      "global_load_dwordx4 a[232:235], v28, s[42:43] offset:1024 sc1\n\t"
      "v_mfma_f32_16x16x32_bf16 v[132:135], a[80:83], a[132:135], v[132:135]\n\t"
      "ds_read_b64_tr_b16 a[26:27], v24 offset:3776\n\t"
      "v_mfma_f32_16x16x32_bf16 v[136:139], a[80:83], a[136:139], v[136:139]\n\t"
      "v_mfma_f32_16x16x32_bf16 v[140:143], a[80:83], a[140:143], v[140:143]\n\t"
      "ds_read_b64_tr_b16 a[28:29], v24 offset:14464\n\t"
      "v_mfma_f32_16x16x32_bf16 v[144:147], a[84:87], a[128:131], v[144:147]\n\t"
      "ds_read_b64_tr_b16 a[30:31], v24 offset:18048\n\t"
      "v_mfma_f32_16x16x32_bf16 v[148:151], a[84:87], a[132:135], v[148:151]\n\t"
      "v_mfma_f32_16x16x32_bf16 v[152:155], a[84:87], a[136:139], v[152:155]\n\t"
      "ds_read_b64_tr_b16 a[32:33], v24 offset:14496\n\t"
      "global_load_lds_dwordx4 v17, s[40:41]\n\t"
      "s_add_u32 m0, m0, 0x400\n\t"
      "v_mfma_f32_16x16x32_bf16 v[156:159], a[84:87], a[140:143], v[156:159]\n\t"
      "v_mfma_f32_16x16x32_bf16 v[160:163], a[88:91], a[128:131], v[160:163]\n\t"
      "ds_read_b64_tr_b16 a[34:35], v24 offset:18080\n\t"
      "v_mfma_f32_16x16x32_bf16 v[164:167], a[88:91], a[132:135], v[164:167]\n\t"
      "v_mfma_f32_16x16x32_bf16 v[168:171], a[88:91], a[136:139], v[168:171]\n\t"
      "ds_read_b64_tr_b16 a[36:37], v24 offset:14528\n\t"
      "v_mfma_f32_16x16x32_bf16 v[172:175], a[88:91], a[140:143], v[172:175]\n\t"
      "v_mfma_f32_16x16x32_bf16 v[176:179], a[92:95], a[128:131], v[176:179]\n\t"
      "ds_read_b64_tr_b16 a[38:39], v24 offset:18112\n\t"
      "global_load_dwordx4 a[236:239], v29, s[42:43] offset:1024 sc1\n\t"
      "v_mfma_f32_16x16x32_bf16 v[180:183], a[92:95], a[132:135], v[180:183]\n\t"
      "s_add_u32 s42, s42, 0x800\n\t"
      "s_addc_u32 s43, s43, 0\n\t"
      "v_mfma_f32_16x16x32_bf16 v[184:187], a[92:95], a[136:139], v[184:187]\n\t"
      "ds_read_b64_tr_b16 a[40:41], v24 offset:14560\n\t"
      "v_mfma_f32_16x16x32_bf16 v[188:191], a[92:95], a[140:143], v[188:191]\n\t"
      "v_mfma_f32_16x16x32_bf16 v[192:195], a[96:99], a[128:131], v[192:195]\n\t"
      "ds_read_b64_tr_b16 a[42:43], v24 offset:18144\n\t"
      "v_mfma_f32_16x16x32_bf16 v[196:199], a[96:99], a[132:135], v[196:199]\n\t"
      "ds_read_b64_tr_b16 a[44:45], v24 offset:14592\n\t"
      "v_mfma_f32_16x16x32_bf16 v[200:203], a[96:99], a[136:139], v[200:203]\n\t"
      "global_load_lds_dwordx4 v18, s[40:41]\n\t"
      "s_add_u32 m0, m0, 0x400\n\t"
      "v_mfma_f32_16x16x32_bf16 v[204:207], a[96:99], a[140:143], v[204:207]\n\t"
      "ds_read_b64_tr_b16 a[46:47], v24 offset:18176\n\t"
      "v_mfma_f32_16x16x32_bf16 v[208:211], a[100:103], a[128:131], v[208:211]\n\t"
      "v_mfma_f32_16x16x32_bf16 v[212:215], a[100:103], a[132:135], v[212:215]\n\t"
      "ds_read_b64_tr_b16 a[48:49], v24 offset:14624\n\t"
      "v_mfma_f32_16x16x32_bf16 v[216:219], a[100:103], a[136:139], v[216:219]\n\t"
      "v_mfma_f32_16x16x32_bf16 v[220:223], a[100:103], a[140:143], v[220:223]\n\t"
      "ds_read_b64_tr_b16 a[50:51], v24 offset:18208\n\t"
      "v_mfma_f32_16x16x32_bf16 v[224:227], a[104:107], a[128:131], v[224:227]\n\t"
      "global_load_dwordx4 a[112:115], v26, s[42:43] offset:0 sc1\n\t"
      "v_mfma_f32_16x16x32_bf16 v[228:231], a[104:107], a[132:135], v[228:231]\n\t"
      "ds_read_b64_tr_b16 a[52:53], v24 offset:14656\n\t"
      "v_mfma_f32_16x16x32_bf16 v[232:235], a[104:107], a[136:139], v[232:235]\n\t"
      "v_mfma_f32_16x16x32_bf16 v[236:239], a[104:107], a[140:143], v[236:239]\n\t"
      "ds_read_b64_tr_b16 a[54:55], v24 offset:18240\n\t"
      "v_mfma_f32_16x16x32_bf16 v[240:243], a[108:111], a[128:131], v[240:243]\n\t"
      "v_mfma_f32_16x16x32_bf16 v[244:247], a[108:111], a[132:135], v[244:247]\n\t"
      "v_mfma_f32_16x16x32_bf16 v[248:251], a[108:111], a[136:139], v[248:251]\n\t"
      "global_load_lds_dwordx4 v19, s[40:41]\n\t"
      "s_add_u32 m0, m0, 0x400\n\t"
      "v_mfma_f32_16x16x32_bf16 v[252:255], a[108:111], a[140:143], v[252:255]\n\t"
      "s_waitcnt vmcnt(41)\n\t"
      "s_waitcnt lgkmcnt(0)\n\t"
      "v_mfma_f32_16x16x32_bf16 v[32:35], a[0:3], a[144:147], v[32:35]\n\t"
      "v_mfma_f32_16x16x32_bf16 v[36:39], a[0:3], a[148:151], v[36:39]\n\t"
      "ds_read_b64_tr_b16 a[56:57], v24 offset:7168\n\t"
      "v_mfma_f32_16x16x32_bf16 v[40:43], a[0:3], a[152:155], v[40:43]\n\t"
      "ds_read_b64_tr_b16 a[58:59], v24 offset:10752\n\t"
      "v_mfma_f32_16x16x32_bf16 v[44:47], a[0:3], a[156:159], v[44:47]\n\t"
      "v_mfma_f32_16x16x32_bf16 v[48:51], a[4:7], a[144:147], v[48:51]\n\t"
      "ds_read_b64_tr_b16 a[60:61], v24 offset:7200\n\t"
      "global_load_dwordx4 a[116:119], v27, s[42:43] offset:0 sc1\n\t"
      "v_mfma_f32_16x16x32_bf16 v[52:55], a[4:7], a[148:151], v[52:55]\n\t"
      "v_mfma_f32_16x16x32_bf16 v[56:59], a[4:7], a[152:155], v[56:59]\n\t"
      "ds_read_b64_tr_b16 a[62:63], v24 offset:10784\n\t"
      "v_mfma_f32_16x16x32_bf16 v[60:63], a[4:7], a[156:159], v[60:63]\n\t"
      "v_mfma_f32_16x16x32_bf16 v[64:67], a[8:11], a[144:147], v[64:67]\n\t"
      "ds_read_b64_tr_b16 a[64:65], v24 offset:7232\n\t"
      "v_mfma_f32_16x16x32_bf16 v[68:71], a[8:11], a[148:151], v[68:71]\n\t"
      "v_mfma_f32_16x16x32_bf16 v[72:75], a[8:11], a[152:155], v[72:75]\n\t"
      "ds_read_b64_tr_b16 a[66:67], v24 offset:10816\n\t"
      "global_load_lds_dwordx4 v20, s[40:41]\n\t"
      "s_add_u32 m0, m0, 0x400\n\t"
      "v_mfma_f32_16x16x32_bf16 v[76:79], a[8:11], a[156:159], v[76:79]\n\t"
      "v_mfma_f32_16x16x32_bf16 v[80:83], a[12:15], a[144:147], v[80:83]\n\t"
      "ds_read_b64_tr_b16 a[68:69], v24 offset:7264\n\t"
      "v_mfma_f32_16x16x32_bf16 v[84:87], a[12:15], a[148:151], v[84:87]\n\t"
      "v_mfma_f32_16x16x32_bf16 v[88:91], a[12:15], a[152:155], v[88:91]\n\t"
      "ds_read_b64_tr_b16 a[70:71], v24 offset:10848\n\t"
      "v_mfma_f32_16x16x32_bf16 v[92:95], a[12:15], a[156:159], v[92:95]\n\t"
      "ds_read_b64_tr_b16 a[72:73], v24 offset:7296\n\t"
      "v_mfma_f32_16x16x32_bf16 v[96:99], a[16:19], a[144:147], v[96:99]\n\t"
      "global_load_dwordx4 a[120:123], v28, s[42:43] offset:0 sc1\n\t"
      "v_mfma_f32_16x16x32_bf16 v[100:103], a[16:19], a[148:151], v[100:103]\n\t"
      "ds_read_b64_tr_b16 a[74:75], v24 offset:10880\n\t"
      "v_mfma_f32_16x16x32_bf16 v[104:107], a[16:19], a[152:155], v[104:107]\n\t"
      "v_mfma_f32_16x16x32_bf16 v[108:111], a[16:19], a[156:159], v[108:111]\n\t"
      "ds_read_b64_tr_b16 a[76:77], v24 offset:7328\n\t"
      "v_mfma_f32_16x16x32_bf16 v[112:115], a[20:23], a[144:147], v[112:115]\n\t"
      "v_mfma_f32_16x16x32_bf16 v[116:119], a[20:23], a[148:151], v[116:119]\n\t"
      "ds_read_b64_tr_b16 a[78:79], v24 offset:10912\n\t"
      "v_mfma_f32_16x16x32_bf16 v[120:123], a[20:23], a[152:155], v[120:123]\n\t"
      "global_load_dwordx4 a[124:127], v29, s[42:43] offset:0 sc1\n\t"
      "v_mfma_f32_16x16x32_bf16 v[124:127], a[20:23], a[156:159], v[124:127]\n\t"
      "ds_read_b64_tr_b16 a[80:81], v24 offset:7360\n\t"
      "v_mfma_f32_16x16x32_bf16 v[128:131], a[24:27], a[144:147], v[128:131]\n\t"
      "v_mfma_f32_16x16x32_bf16 v[132:135], a[24:27], a[148:151], v[132:135]\n\t"
      "ds_read_b64_tr_b16 a[82:83], v24 offset:10944\n\t"
      "v_mfma_f32_16x16x32_bf16 v[136:139], a[24:27], a[152:155], v[136:139]\n\t"
      "global_load_lds_dwordx4 v21, s[40:41]\n\t"
      "s_add_u32 m0, m0, 0x400\n\t"
      "v_mfma_f32_16x16x32_bf16 v[140:143], a[24:27], a[156:159], v[140:143]\n\t"
      "ds_read_b64_tr_b16 a[84:85], v24 offset:21632\n\t"
      "v_mfma_f32_16x16x32_bf16 v[144:147], a[28:31], a[144:147], v[144:147]\n\t"
      "ds_read_b64_tr_b16 a[86:87], v24 offset:25216\n\t"
      "v_mfma_f32_16x16x32_bf16 v[148:151], a[28:31], a[148:151], v[148:151]\n\t"
      "v_mfma_f32_16x16x32_bf16 v[152:155], a[28:31], a[152:155], v[152:155]\n\t"
      "ds_read_b64_tr_b16 a[88:89], v24 offset:21664\n\t"
      "v_mfma_f32_16x16x32_bf16 v[156:159], a[28:31], a[156:159], v[156:159]\n\t"
      "v_mfma_f32_16x16x32_bf16 v[160:163], a[32:35], a[144:147], v[160:163]\n\t"
      "ds_read_b64_tr_b16 a[90:91], v24 offset:25248\n\t"
      "global_load_dwordx4 a[128:131], v26, s[42:43] offset:1024 sc1\n\t"
      "v_mfma_f32_16x16x32_bf16 v[164:167], a[32:35], a[148:151], v[164:167]\n\t"
      "v_mfma_f32_16x16x32_bf16 v[168:171], a[32:35], a[152:155], v[168:171]\n\t"
      "ds_read_b64_tr_b16 a[92:93], v24 offset:21696\n\t"
      "v_mfma_f32_16x16x32_bf16 v[172:175], a[32:35], a[156:159], v[172:175]\n\t"
      "v_mfma_f32_16x16x32_bf16 v[176:179], a[36:39], a[144:147], v[176:179]\n\t"
      "ds_read_b64_tr_b16 a[94:95], v24 offset:25280\n\t"
      "v_mfma_f32_16x16x32_bf16 v[180:183], a[36:39], a[148:151], v[180:183]\n\t"
      "v_mfma_f32_16x16x32_bf16 v[184:187], a[36:39], a[152:155], v[184:187]\n\t"
      "ds_read_b64_tr_b16 a[96:97], v24 offset:21728\n\t"
      "global_load_dwordx4 a[132:135], v27, s[42:43] offset:1024 sc1\n\t"
      "v_mfma_f32_16x16x32_bf16 v[188:191], a[36:39], a[156:159], v[188:191]\n\t"
      "v_mfma_f32_16x16x32_bf16 v[192:195], a[40:43], a[144:147], v[192:195]\n\t"
      "ds_read_b64_tr_b16 a[98:99], v24 offset:25312\n\t"
      "v_mfma_f32_16x16x32_bf16 v[196:199], a[40:43], a[148:151], v[196:199]\n\t"
      "ds_read_b64_tr_b16 a[100:101], v24 offset:21760\n\t"
      "v_mfma_f32_16x16x32_bf16 v[200:203], a[40:43], a[152:155], v[200:203]\n\t"
      "global_load_lds_dwordx4 v22, s[40:41]\n\t"
      "s_add_u32 s47, s47, 28928\n\t"
      "v_mfma_f32_16x16x32_bf16 v[204:207], a[40:43], a[156:159], v[204:207]\n\t"
      "ds_read_b64_tr_b16 a[102:103], v24 offset:25344\n\t"
      "s_cmp_eq_u32 s47, 115712\n\t"
      "s_cselect_b32 s47, 0, s47\n\t"
      "v_mfma_f32_16x16x32_bf16 v[208:211], a[44:47], a[144:147], v[208:211]\n\t"
      "v_add_u32 v23, s47, v25\n\t"
      "v_mfma_f32_16x16x32_bf16 v[212:215], a[44:47], a[148:151], v[212:215]\n\t"
      "ds_read_b64_tr_b16 a[104:105], v24 offset:21792\n\t"
      "v_mfma_f32_16x16x32_bf16 v[216:219], a[44:47], a[152:155], v[216:219]\n\t"
      "s_add_u32 s40, s40, 0x70000\n\t"
      "s_addc_u32 s41, s41, 0\n\t"
      "v_mfma_f32_16x16x32_bf16 v[220:223], a[44:47], a[156:159], v[220:223]\n\t"
      "ds_read_b64_tr_b16 a[106:107], v24 offset:25376\n\t"
      "v_mfma_f32_16x16x32_bf16 v[224:227], a[48:51], a[144:147], v[224:227]\n\t"
      "global_load_dwordx4 a[136:139], v28, s[42:43] offset:1024 sc1\n\t"
      "global_load_dwordx4 a[140:143], v29, s[42:43] offset:1024 sc1\n\t"
      "v_mfma_f32_16x16x32_bf16 v[228:231], a[48:51], a[148:151], v[228:231]\n\t"
      "ds_read_b64_tr_b16 a[108:109], v24 offset:21824\n\t"
      "v_mfma_f32_16x16x32_bf16 v[232:235], a[48:51], a[152:155], v[232:235]\n\t"
      "s_add_u32 s46, s46, 28928\n\t"
      "v_mfma_f32_16x16x32_bf16 v[236:239], a[48:51], a[156:159], v[236:239]\n\t"
      "ds_read_b64_tr_b16 a[110:111], v24 offset:25408\n\t"
      "v_mfma_f32_16x16x32_bf16 v[240:243], a[52:55], a[144:147], v[240:243]\n\t"
      "buffer_load_dword v30, v14, %[wsrd], s53 offen\n\t"
      "v_mfma_f32_16x16x32_bf16 v[244:247], a[52:55], a[148:151], v[244:247]\n\t"
      "s_cmp_eq_u32 s46, s50\n\t"
      "s_cselect_b32 s46, s49, s46\n\t"
      "v_mfma_f32_16x16x32_bf16 v[248:251], a[52:55], a[152:155], v[248:251]\n\t"
      "s_mov_b32 m0, s46\n\t"
      "v_mfma_f32_16x16x32_bf16 v[252:255], a[52:55], a[156:159], v[252:255]\n\t"
      "s_add_u32 s42, s42, 0x800\n\t"
      "s_addc_u32 s43, s43, 0\n\t"
      "s_waitcnt vmcnt(48)\n\t"
      "s_waitcnt lgkmcnt(0)\n\t"
      "v_mfma_f32_16x16x32_bf16 v[32:35], a[56:59], a[160:163], v[32:35]\n\t"
      "s_add_u32 s53, s53, 0x70000\n\t"
      "v_mfma_f32_16x16x32_bf16 v[36:39], a[56:59], a[164:167], v[36:39]\n\t"
      "v_mfma_f32_16x16x32_bf16 v[40:43], a[56:59], a[168:171], v[40:43]\n\t"
      "v_mfma_f32_16x16x32_bf16 v[44:47], a[56:59], a[172:175], v[44:47]\n\t"
      "v_mfma_f32_16x16x32_bf16 v[48:51], a[60:63], a[160:163], v[48:51]\n\t"
      "v_mfma_f32_16x16x32_bf16 v[52:55], a[60:63], a[164:167], v[52:55]\n\t"
      "v_mfma_f32_16x16x32_bf16 v[56:59], a[60:63], a[168:171], v[56:59]\n\t"
      "global_load_dwordx4 a[144:147], v26, s[42:43] offset:0 sc1\n\t"
      "v_mfma_f32_16x16x32_bf16 v[60:63], a[60:63], a[172:175], v[60:63]\n\t"
      "v_mfma_f32_16x16x32_bf16 v[64:67], a[64:67], a[160:163], v[64:67]\n\t"
      "s_waitcnt lgkmcnt(0)\n\t"
      "s_waitcnt vmcnt(41)\n\t"
      "s_barrier\n\t"
      "v_mfma_f32_16x16x32_bf16 v[68:71], a[64:67], a[164:167], v[68:71]\n\t"
      "ds_read_b64_tr_b16 a[0:1], v23 offset:0\n\t"
      "v_mfma_f32_16x16x32_bf16 v[72:75], a[64:67], a[168:171], v[72:75]\n\t"
      "ds_read_b64_tr_b16 a[2:3], v23 offset:3584\n\t"
      "v_mfma_f32_16x16x32_bf16 v[76:79], a[64:67], a[172:175], v[76:79]\n\t"
      "ds_read_b64_tr_b16 a[4:5], v23 offset:32\n\t"
      "v_mfma_f32_16x16x32_bf16 v[80:83], a[68:71], a[160:163], v[80:83]\n\t"
      "ds_read_b64_tr_b16 a[6:7], v23 offset:3616\n\t"
      "v_mfma_f32_16x16x32_bf16 v[84:87], a[68:71], a[164:167], v[84:87]\n\t"
      "ds_read_b64_tr_b16 a[8:9], v23 offset:64\n\t"
      "v_mfma_f32_16x16x32_bf16 v[88:91], a[68:71], a[168:171], v[88:91]\n\t"
      "ds_read_b64_tr_b16 a[10:11], v23 offset:3648\n\t"
      "v_mfma_f32_16x16x32_bf16 v[92:95], a[68:71], a[172:175], v[92:95]\n\t"
      "ds_read_b64_tr_b16 a[12:13], v23 offset:96\n\t"
      "v_mfma_f32_16x16x32_bf16 v[96:99], a[72:75], a[160:163], v[96:99]\n\t"
      "ds_read_b64_tr_b16 a[14:15], v23 offset:3680\n\t"
      "v_mfma_f32_16x16x32_bf16 v[100:103], a[72:75], a[164:167], v[100:103]\n\t"
      "ds_read_b64_tr_b16 a[16:17], v23 offset:128\n\t"
      "v_mfma_f32_16x16x32_bf16 v[104:107], a[72:75], a[168:171], v[104:107]\n\t"
      "ds_read_b64_tr_b16 a[18:19], v23 offset:3712\n\t"
      "global_load_lds_dwordx4 v16, s[40:41]\n\t"
      "s_add_u32 m0, m0, 0x400\n\t"
      "v_mfma_f32_16x16x32_bf16 v[108:111], a[72:75], a[172:175], v[108:111]\n\t"
      "ds_read_b64_tr_b16 a[20:21], v23 offset:160\n\t"
      "v_mfma_f32_16x16x32_bf16 v[112:115], a[76:79], a[160:163], v[112:115]\n\t"
      "global_load_dwordx4 a[148:151], v27, s[42:43] offset:0 sc1\n\t"
      "v_mfma_f32_16x16x32_bf16 v[116:119], a[76:79], a[164:167], v[116:119]\n\t"
      "ds_read_b64_tr_b16 a[22:23], v23 offset:3744\n\t"
      "v_mfma_f32_16x16x32_bf16 v[120:123], a[76:79], a[168:171], v[120:123]\n\t"
      "v_mfma_f32_16x16x32_bf16 v[124:127], a[76:79], a[172:175], v[124:127]\n\t"
      "ds_read_b64_tr_b16 a[24:25], v23 offset:192\n\t"
      "v_mfma_f32_16x16x32_bf16 v[128:131], a[80:83], a[160:163], v[128:131]\n\t"
      "v_mfma_f32_16x16x32_bf16 v[132:135], a[80:83], a[164:167], v[132:135]\n\t"
      "ds_read_b64_tr_b16 a[26:27], v23 offset:3776\n\t"
      "v_mfma_f32_16x16x32_bf16 v[136:139], a[80:83], a[168:171], v[136:139]\n\t"
      "v_mfma_f32_16x16x32_bf16 v[140:143], a[80:83], a[172:175], v[140:143]\n\t"
      "ds_read_b64_tr_b16 a[28:29], v23 offset:14464\n\t"
      "v_mfma_f32_16x16x32_bf16 v[144:147], a[84:87], a[160:163], v[144:147]\n\t"
      "ds_read_b64_tr_b16 a[30:31], v23 offset:18048\n\t"
      "v_mfma_f32_16x16x32_bf16 v[148:151], a[84:87], a[164:167], v[148:151]\n\t"
      "v_mfma_f32_16x16x32_bf16 v[152:155], a[84:87], a[168:171], v[152:155]\n\t"
      "ds_read_b64_tr_b16 a[32:33], v23 offset:14496\n\t"
      "global_load_lds_dwordx4 v17, s[40:41]\n\t"
      "s_add_u32 m0, m0, 0x400\n\t"
      "v_mfma_f32_16x16x32_bf16 v[156:159], a[84:87], a[172:175], v[156:159]\n\t"
      "v_mfma_f32_16x16x32_bf16 v[160:163], a[88:91], a[160:163], v[160:163]\n\t"
      "ds_read_b64_tr_b16 a[34:35], v23 offset:18080\n\t"
      "v_mfma_f32_16x16x32_bf16 v[164:167], a[88:91], a[164:167], v[164:167]\n\t"
      "v_mfma_f32_16x16x32_bf16 v[168:171], a[88:91], a[168:171], v[168:171]\n\t"
      "ds_read_b64_tr_b16 a[36:37], v23 offset:14528\n\t"
      "global_load_dwordx4 a[152:155], v28, s[42:43] offset:0 sc1\n\t"
      "v_mfma_f32_16x16x32_bf16 v[172:175], a[88:91], a[172:175], v[172:175]\n\t"
      "v_mfma_f32_16x16x32_bf16 v[176:179], a[92:95], a[160:163], v[176:179]\n\t"
      "ds_read_b64_tr_b16 a[38:39], v23 offset:18112\n\t"
      "v_mfma_f32_16x16x32_bf16 v[180:183], a[92:95], a[164:167], v[180:183]\n\t"
      "v_mfma_f32_16x16x32_bf16 v[184:187], a[92:95], a[168:171], v[184:187]\n\t"
      "ds_read_b64_tr_b16 a[40:41], v23 offset:14560\n\t"
      "v_mfma_f32_16x16x32_bf16 v[188:191], a[92:95], a[172:175], v[188:191]\n\t"
      "v_mfma_f32_16x16x32_bf16 v[192:195], a[96:99], a[160:163], v[192:195]\n\t"
      "ds_read_b64_tr_b16 a[42:43], v23 offset:18144\n\t"
      "v_mfma_f32_16x16x32_bf16 v[196:199], a[96:99], a[164:167], v[196:199]\n\t"
      "ds_read_b64_tr_b16 a[44:45], v23 offset:14592\n\t"
      "v_mfma_f32_16x16x32_bf16 v[200:203], a[96:99], a[168:171], v[200:203]\n\t"
      "global_load_lds_dwordx4 v18, s[40:41]\n\t"
      "s_add_u32 m0, m0, 0x400\n\t"
      "v_mfma_f32_16x16x32_bf16 v[204:207], a[96:99], a[172:175], v[204:207]\n\t"
      "ds_read_b64_tr_b16 a[46:47], v23 offset:18176\n\t"
      "v_mfma_f32_16x16x32_bf16 v[208:211], a[100:103], a[160:163], v[208:211]\n\t"
      "v_mfma_f32_16x16x32_bf16 v[212:215], a[100:103], a[164:167], v[212:215]\n\t"
      "ds_read_b64_tr_b16 a[48:49], v23 offset:14624\n\t"
      "v_mfma_f32_16x16x32_bf16 v[216:219], a[100:103], a[168:171], v[216:219]\n\t"
      "v_mfma_f32_16x16x32_bf16 v[220:223], a[100:103], a[172:175], v[220:223]\n\t"
      "ds_read_b64_tr_b16 a[50:51], v23 offset:18208\n\t"
      "v_mfma_f32_16x16x32_bf16 v[224:227], a[104:107], a[160:163], v[224:227]\n\t"
      "global_load_dwordx4 a[156:159], v29, s[42:43] offset:0 sc1\n\t"
      "v_mfma_f32_16x16x32_bf16 v[228:231], a[104:107], a[164:167], v[228:231]\n\t"
      "ds_read_b64_tr_b16 a[52:53], v23 offset:14656\n\t"
      "v_mfma_f32_16x16x32_bf16 v[232:235], a[104:107], a[168:171], v[232:235]\n\t"
      "v_mfma_f32_16x16x32_bf16 v[236:239], a[104:107], a[172:175], v[236:239]\n\t"
      "ds_read_b64_tr_b16 a[54:55], v23 offset:18240\n\t"
      "v_mfma_f32_16x16x32_bf16 v[240:243], a[108:111], a[160:163], v[240:243]\n\t"
      "v_mfma_f32_16x16x32_bf16 v[244:247], a[108:111], a[164:167], v[244:247]\n\t"
      "v_mfma_f32_16x16x32_bf16 v[248:251], a[108:111], a[168:171], v[248:251]\n\t"
      "global_load_lds_dwordx4 v19, s[40:41]\n\t"
      "s_add_u32 m0, m0, 0x400\n\t"
      "v_mfma_f32_16x16x32_bf16 v[252:255], a[108:111], a[172:175], v[252:255]\n\t"
      "s_mov_b32 s48, 1\n\t"
      "G1LOOP_%=:\n\t"
      "s_waitcnt vmcnt(41)\n\t"
      "s_waitcnt lgkmcnt(0)\n\t"
      "v_mfma_f32_16x16x32_bf16 v[32:35], a[0:3], a[176:179], v[32:35]\n\t"
      "v_mfma_f32_16x16x32_bf16 v[36:39], a[0:3], a[180:183], v[36:39]\n\t"
      "ds_read_b64_tr_b16 a[56:57], v23 offset:7168\n\t"
      "v_mfma_f32_16x16x32_bf16 v[40:43], a[0:3], a[184:187], v[40:43]\n\t"
      "ds_read_b64_tr_b16 a[58:59], v23 offset:10752\n\t"
      "v_mfma_f32_16x16x32_bf16 v[44:47], a[0:3], a[188:191], v[44:47]\n\t"
      "v_mfma_f32_16x16x32_bf16 v[48:51], a[4:7], a[176:179], v[48:51]\n\t"
      "ds_read_b64_tr_b16 a[60:61], v23 offset:7200\n\t"
      "v_mfma_f32_16x16x32_bf16 v[52:55], a[4:7], a[180:183], v[52:55]\n\t"
      "v_mfma_f32_16x16x32_bf16 v[56:59], a[4:7], a[184:187], v[56:59]\n\t"
      "ds_read_b64_tr_b16 a[62:63], v23 offset:10784\n\t"
      "global_load_dwordx4 a[160:163], v26, s[42:43] offset:1024 sc1\n\t"
      "v_mfma_f32_16x16x32_bf16 v[60:63], a[4:7], a[188:191], v[60:63]\n\t"
      "v_mfma_f32_16x16x32_bf16 v[64:67], a[8:11], a[176:179], v[64:67]\n\t"
      "ds_read_b64_tr_b16 a[64:65], v23 offset:7232\n\t"
      "v_mfma_f32_16x16x32_bf16 v[68:71], a[8:11], a[180:183], v[68:71]\n\t"
      "v_mfma_f32_16x16x32_bf16 v[72:75], a[8:11], a[184:187], v[72:75]\n\t"
      "ds_read_b64_tr_b16 a[66:67], v23 offset:10816\n\t"
      "global_load_lds_dwordx4 v20, s[40:41]\n\t"
      "s_add_u32 m0, m0, 0x400\n\t"
      "v_mfma_f32_16x16x32_bf16 v[76:79], a[8:11], a[188:191], v[76:79]\n\t"
      "v_mfma_f32_16x16x32_bf16 v[80:83], a[12:15], a[176:179], v[80:83]\n\t"
      "ds_read_b64_tr_b16 a[68:69], v23 offset:7264\n\t"
      "v_mfma_f32_16x16x32_bf16 v[84:87], a[12:15], a[180:183], v[84:87]\n\t"
      "v_mfma_f32_16x16x32_bf16 v[88:91], a[12:15], a[184:187], v[88:91]\n\t"
      "ds_read_b64_tr_b16 a[70:71], v23 offset:10848\n\t"
      "v_mfma_f32_16x16x32_bf16 v[92:95], a[12:15], a[188:191], v[92:95]\n\t"
      "ds_read_b64_tr_b16 a[72:73], v23 offset:7296\n\t"
      "v_mfma_f32_16x16x32_bf16 v[96:99], a[16:19], a[176:179], v[96:99]\n\t"
      "v_mfma_f32_16x16x32_bf16 v[100:103], a[16:19], a[180:183], v[100:103]\n\t"
      "ds_read_b64_tr_b16 a[74:75], v23 offset:10880\n\t"
      "v_mfma_f32_16x16x32_bf16 v[104:107], a[16:19], a[184:187], v[104:107]\n\t"
      "v_mfma_f32_16x16x32_bf16 v[108:111], a[16:19], a[188:191], v[108:111]\n\t"
      "ds_read_b64_tr_b16 a[76:77], v23 offset:7328\n\t"
      "v_mfma_f32_16x16x32_bf16 v[112:115], a[20:23], a[176:179], v[112:115]\n\t"
      "global_load_dwordx4 a[164:167], v27, s[42:43] offset:1024 sc1\n\t"
      "v_mfma_f32_16x16x32_bf16 v[116:119], a[20:23], a[180:183], v[116:119]\n\t"
      "ds_read_b64_tr_b16 a[78:79], v23 offset:10912\n\t"
      "v_mfma_f32_16x16x32_bf16 v[120:123], a[20:23], a[184:187], v[120:123]\n\t"
      "v_mfma_f32_16x16x32_bf16 v[124:127], a[20:23], a[188:191], v[124:127]\n\t"
      "ds_read_b64_tr_b16 a[80:81], v23 offset:7360\n\t"
      "v_mfma_f32_16x16x32_bf16 v[128:131], a[24:27], a[176:179], v[128:131]\n\t"
      "v_mfma_f32_16x16x32_bf16 v[132:135], a[24:27], a[180:183], v[132:135]\n\t"
      "ds_read_b64_tr_b16 a[82:83], v23 offset:10944\n\t"
      "v_mfma_f32_16x16x32_bf16 v[136:139], a[24:27], a[184:187], v[136:139]\n\t"
      "global_load_lds_dwordx4 v21, s[40:41]\n\t"
      "s_add_u32 m0, m0, 0x400\n\t"
      "v_mfma_f32_16x16x32_bf16 v[140:143], a[24:27], a[188:191], v[140:143]\n\t"
      "ds_read_b64_tr_b16 a[84:85], v23 offset:21632\n\t"
      "v_mfma_f32_16x16x32_bf16 v[144:147], a[28:31], a[176:179], v[144:147]\n\t"
      "ds_read_b64_tr_b16 a[86:87], v23 offset:25216\n\t"
      "v_mfma_f32_16x16x32_bf16 v[148:151], a[28:31], a[180:183], v[148:151]\n\t"
      "v_mfma_f32_16x16x32_bf16 v[152:155], a[28:31], a[184:187], v[152:155]\n\t"
      "ds_read_b64_tr_b16 a[88:89], v23 offset:21664\n\t"
      "v_mfma_f32_16x16x32_bf16 v[156:159], a[28:31], a[188:191], v[156:159]\n\t"
      "v_mfma_f32_16x16x32_bf16 v[160:163], a[32:35], a[176:179], v[160:163]\n\t"
      "ds_read_b64_tr_b16 a[90:91], v23 offset:25248\n\t"
      "v_mfma_f32_16x16x32_bf16 v[164:167], a[32:35], a[180:183], v[164:167]\n\t"
      "v_mfma_f32_16x16x32_bf16 v[168:171], a[32:35], a[184:187], v[168:171]\n\t"
      "ds_read_b64_tr_b16 a[92:93], v23 offset:21696\n\t"
      "global_load_dwordx4 a[168:171], v28, s[42:43] offset:1024 sc1\n\t"
      "v_mfma_f32_16x16x32_bf16 v[172:175], a[32:35], a[188:191], v[172:175]\n\t"
      "v_mfma_f32_16x16x32_bf16 v[176:179], a[36:39], a[176:179], v[176:179]\n\t"
      "ds_read_b64_tr_b16 a[94:95], v23 offset:25280\n\t"
      "v_mfma_f32_16x16x32_bf16 v[180:183], a[36:39], a[180:183], v[180:183]\n\t"
      "v_mfma_f32_16x16x32_bf16 v[184:187], a[36:39], a[184:187], v[184:187]\n\t"
      "ds_read_b64_tr_b16 a[96:97], v23 offset:21728\n\t"
      "v_mfma_f32_16x16x32_bf16 v[188:191], a[36:39], a[188:191], v[188:191]\n\t"
      "v_mfma_f32_16x16x32_bf16 v[192:195], a[40:43], a[176:179], v[192:195]\n\t"
      "ds_read_b64_tr_b16 a[98:99], v23 offset:25312\n\t"
      "v_mfma_f32_16x16x32_bf16 v[196:199], a[40:43], a[180:183], v[196:199]\n\t"
      "ds_read_b64_tr_b16 a[100:101], v23 offset:21760\n\t"
      "v_mfma_f32_16x16x32_bf16 v[200:203], a[40:43], a[184:187], v[200:203]\n\t"
      "global_load_lds_dwordx4 v22, s[40:41]\n\t"
      "s_add_u32 s47, s47, 28928\n\t"
      "v_mfma_f32_16x16x32_bf16 v[204:207], a[40:43], a[188:191], v[204:207]\n\t"
      "ds_read_b64_tr_b16 a[102:103], v23 offset:25344\n\t"
      "s_cmp_eq_u32 s47, 115712\n\t"
      "s_cselect_b32 s47, 0, s47\n\t"
      "v_mfma_f32_16x16x32_bf16 v[208:211], a[44:47], a[176:179], v[208:211]\n\t"
      "v_add_u32 v24, s47, v25\n\t"
      "v_mfma_f32_16x16x32_bf16 v[212:215], a[44:47], a[180:183], v[212:215]\n\t"
      "ds_read_b64_tr_b16 a[104:105], v23 offset:21792\n\t"
      "v_mfma_f32_16x16x32_bf16 v[216:219], a[44:47], a[184:187], v[216:219]\n\t"
      "s_add_u32 s40, s40, 0x70000\n\t"
      "s_addc_u32 s41, s41, 0\n\t"
      "v_mfma_f32_16x16x32_bf16 v[220:223], a[44:47], a[188:191], v[220:223]\n\t"
      "ds_read_b64_tr_b16 a[106:107], v23 offset:25376\n\t"
      "v_mfma_f32_16x16x32_bf16 v[224:227], a[48:51], a[176:179], v[224:227]\n\t"
      "global_load_dwordx4 a[172:175], v29, s[42:43] offset:1024 sc1\n\t"
      "v_mfma_f32_16x16x32_bf16 v[228:231], a[48:51], a[180:183], v[228:231]\n\t"
      "ds_read_b64_tr_b16 a[108:109], v23 offset:21824\n\t"
      "v_mfma_f32_16x16x32_bf16 v[232:235], a[48:51], a[184:187], v[232:235]\n\t"
      "s_add_u32 s46, s46, 28928\n\t"
      "v_mfma_f32_16x16x32_bf16 v[236:239], a[48:51], a[188:191], v[236:239]\n\t"
      "ds_read_b64_tr_b16 a[110:111], v23 offset:25408\n\t"
      "v_mfma_f32_16x16x32_bf16 v[240:243], a[52:55], a[176:179], v[240:243]\n\t"
      "buffer_load_dword v30, v14, %[wsrd], s53 offen\n\t"
      "v_mfma_f32_16x16x32_bf16 v[244:247], a[52:55], a[180:183], v[244:247]\n\t"
      "s_cmp_eq_u32 s46, s50\n\t"
      "s_cselect_b32 s46, s49, s46\n\t"
      "v_mfma_f32_16x16x32_bf16 v[248:251], a[52:55], a[184:187], v[248:251]\n\t"
      "s_mov_b32 m0, s46\n\t"
      "v_mfma_f32_16x16x32_bf16 v[252:255], a[52:55], a[188:191], v[252:255]\n\t"
      "s_add_u32 s42, s42, 0x800\n\t"
      "s_addc_u32 s43, s43, 0\n\t"
      "s_waitcnt vmcnt(42)\n\t"
      "s_waitcnt lgkmcnt(0)\n\t"
      "v_mfma_f32_16x16x32_bf16 v[32:35], a[56:59], a[192:195], v[32:35]\n\t"
      "s_add_u32 s53, s53, 0x70000\n\t"
      "v_mfma_f32_16x16x32_bf16 v[36:39], a[56:59], a[196:199], v[36:39]\n\t"
      "v_mfma_f32_16x16x32_bf16 v[40:43], a[56:59], a[200:203], v[40:43]\n\t"
      "v_mfma_f32_16x16x32_bf16 v[44:47], a[56:59], a[204:207], v[44:47]\n\t"
      "v_mfma_f32_16x16x32_bf16 v[48:51], a[60:63], a[192:195], v[48:51]\n\t"
      "v_mfma_f32_16x16x32_bf16 v[52:55], a[60:63], a[196:199], v[52:55]\n\t"
      "v_mfma_f32_16x16x32_bf16 v[56:59], a[60:63], a[200:203], v[56:59]\n\t"
      "global_load_dwordx4 a[176:179], v26, s[42:43] offset:0 sc1\n\t"
      "v_mfma_f32_16x16x32_bf16 v[60:63], a[60:63], a[204:207], v[60:63]\n\t"
      "v_mfma_f32_16x16x32_bf16 v[64:67], a[64:67], a[192:195], v[64:67]\n\t"
      "s_waitcnt lgkmcnt(0)\n\t"
      "s_waitcnt vmcnt(41)\n\t"
      "s_barrier\n\t"
      "v_mfma_f32_16x16x32_bf16 v[68:71], a[64:67], a[196:199], v[68:71]\n\t"
      "ds_read_b64_tr_b16 a[0:1], v24 offset:0\n\t"
      "v_mfma_f32_16x16x32_bf16 v[72:75], a[64:67], a[200:203], v[72:75]\n\t"
      "ds_read_b64_tr_b16 a[2:3], v24 offset:3584\n\t"
      "v_mfma_f32_16x16x32_bf16 v[76:79], a[64:67], a[204:207], v[76:79]\n\t"
      "ds_read_b64_tr_b16 a[4:5], v24 offset:32\n\t"
      "v_mfma_f32_16x16x32_bf16 v[80:83], a[68:71], a[192:195], v[80:83]\n\t"
      "ds_read_b64_tr_b16 a[6:7], v24 offset:3616\n\t"
      "v_mfma_f32_16x16x32_bf16 v[84:87], a[68:71], a[196:199], v[84:87]\n\t"
      "ds_read_b64_tr_b16 a[8:9], v24 offset:64\n\t"
      "v_mfma_f32_16x16x32_bf16 v[88:91], a[68:71], a[200:203], v[88:91]\n\t"
      "ds_read_b64_tr_b16 a[10:11], v24 offset:3648\n\t"
      "v_mfma_f32_16x16x32_bf16 v[92:95], a[68:71], a[204:207], v[92:95]\n\t"
      "ds_read_b64_tr_b16 a[12:13], v24 offset:96\n\t"
      "v_mfma_f32_16x16x32_bf16 v[96:99], a[72:75], a[192:195], v[96:99]\n\t"
      "ds_read_b64_tr_b16 a[14:15], v24 offset:3680\n\t"
      "v_mfma_f32_16x16x32_bf16 v[100:103], a[72:75], a[196:199], v[100:103]\n\t"
      "ds_read_b64_tr_b16 a[16:17], v24 offset:128\n\t"
      "v_mfma_f32_16x16x32_bf16 v[104:107], a[72:75], a[200:203], v[104:107]\n\t"
      "ds_read_b64_tr_b16 a[18:19], v24 offset:3712\n\t"
      "global_load_lds_dwordx4 v16, s[40:41]\n\t"
      "s_add_u32 m0, m0, 0x400\n\t"
      "v_mfma_f32_16x16x32_bf16 v[108:111], a[72:75], a[204:207], v[108:111]\n\t"
      "ds_read_b64_tr_b16 a[20:21], v24 offset:160\n\t"
      "v_mfma_f32_16x16x32_bf16 v[112:115], a[76:79], a[192:195], v[112:115]\n\t"
      "global_load_dwordx4 a[180:183], v27, s[42:43] offset:0 sc1\n\t"
      "v_mfma_f32_16x16x32_bf16 v[116:119], a[76:79], a[196:199], v[116:119]\n\t"
      "ds_read_b64_tr_b16 a[22:23], v24 offset:3744\n\t"
      "v_mfma_f32_16x16x32_bf16 v[120:123], a[76:79], a[200:203], v[120:123]\n\t"
      "v_mfma_f32_16x16x32_bf16 v[124:127], a[76:79], a[204:207], v[124:127]\n\t"
      "ds_read_b64_tr_b16 a[24:25], v24 offset:192\n\t"
      "v_mfma_f32_16x16x32_bf16 v[128:131], a[80:83], a[192:195], v[128:131]\n\t"
      "v_mfma_f32_16x16x32_bf16 v[132:135], a[80:83], a[196:199], v[132:135]\n\t"
      "ds_read_b64_tr_b16 a[26:27], v24 offset:3776\n\t"
      "v_mfma_f32_16x16x32_bf16 v[136:139], a[80:83], a[200:203], v[136:139]\n\t"
      "v_mfma_f32_16x16x32_bf16 v[140:143], a[80:83], a[204:207], v[140:143]\n\t"
      "ds_read_b64_tr_b16 a[28:29], v24 offset:14464\n\t"
      "v_mfma_f32_16x16x32_bf16 v[144:147], a[84:87], a[192:195], v[144:147]\n\t"
      "ds_read_b64_tr_b16 a[30:31], v24 offset:18048\n\t"
      "v_mfma_f32_16x16x32_bf16 v[148:151], a[84:87], a[196:199], v[148:151]\n\t"
      "v_mfma_f32_16x16x32_bf16 v[152:155], a[84:87], a[200:203], v[152:155]\n\t"
      "ds_read_b64_tr_b16 a[32:33], v24 offset:14496\n\t"
      "global_load_lds_dwordx4 v17, s[40:41]\n\t"
      "s_add_u32 m0, m0, 0x400\n\t"
      "v_mfma_f32_16x16x32_bf16 v[156:159], a[84:87], a[204:207], v[156:159]\n\t"
      "v_mfma_f32_16x16x32_bf16 v[160:163], a[88:91], a[192:195], v[160:163]\n\t"
      "ds_read_b64_tr_b16 a[34:35], v24 offset:18080\n\t"
      "v_mfma_f32_16x16x32_bf16 v[164:167], a[88:91], a[196:199], v[164:167]\n\t"
      "v_mfma_f32_16x16x32_bf16 v[168:171], a[88:91], a[200:203], v[168:171]\n\t"
      "ds_read_b64_tr_b16 a[36:37], v24 offset:14528\n\t"
      "global_load_dwordx4 a[184:187], v28, s[42:43] offset:0 sc1\n\t"
      "v_mfma_f32_16x16x32_bf16 v[172:175], a[88:91], a[204:207], v[172:175]\n\t"
      "v_mfma_f32_16x16x32_bf16 v[176:179], a[92:95], a[192:195], v[176:179]\n\t"
      "ds_read_b64_tr_b16 a[38:39], v24 offset:18112\n\t"
      "v_mfma_f32_16x16x32_bf16 v[180:183], a[92:95], a[196:199], v[180:183]\n\t"
      "v_mfma_f32_16x16x32_bf16 v[184:187], a[92:95], a[200:203], v[184:187]\n\t"
      "ds_read_b64_tr_b16 a[40:41], v24 offset:14560\n\t"
      "v_mfma_f32_16x16x32_bf16 v[188:191], a[92:95], a[204:207], v[188:191]\n\t"
      "v_mfma_f32_16x16x32_bf16 v[192:195], a[96:99], a[192:195], v[192:195]\n\t"
      "ds_read_b64_tr_b16 a[42:43], v24 offset:18144\n\t"
      "v_mfma_f32_16x16x32_bf16 v[196:199], a[96:99], a[196:199], v[196:199]\n\t"
      "ds_read_b64_tr_b16 a[44:45], v24 offset:14592\n\t"
      "v_mfma_f32_16x16x32_bf16 v[200:203], a[96:99], a[200:203], v[200:203]\n\t"
      "global_load_lds_dwordx4 v18, s[40:41]\n\t"
      "s_add_u32 m0, m0, 0x400\n\t"
      "v_mfma_f32_16x16x32_bf16 v[204:207], a[96:99], a[204:207], v[204:207]\n\t"
      "ds_read_b64_tr_b16 a[46:47], v24 offset:18176\n\t"
      "v_mfma_f32_16x16x32_bf16 v[208:211], a[100:103], a[192:195], v[208:211]\n\t"
      "global_store_dwordx4 v26, a[240:243], s[62:63] sc1\n\t"
      "v_mfma_f32_16x16x32_bf16 v[212:215], a[100:103], a[196:199], v[212:215]\n\t"
      "ds_read_b64_tr_b16 a[48:49], v24 offset:14624\n\t"
      "v_mfma_f32_16x16x32_bf16 v[216:219], a[100:103], a[200:203], v[216:219]\n\t"
      "v_mfma_f32_16x16x32_bf16 v[220:223], a[100:103], a[204:207], v[220:223]\n\t"
      "ds_read_b64_tr_b16 a[50:51], v24 offset:18208\n\t"
      "v_mfma_f32_16x16x32_bf16 v[224:227], a[104:107], a[192:195], v[224:227]\n\t"
      "global_load_dwordx4 a[188:191], v29, s[42:43] offset:0 sc1\n\t"
      "s_add_u32 s62, s62, 0x400\n\t"
      "s_addc_u32 s63, s63, 0\n\t"
      "v_mfma_f32_16x16x32_bf16 v[228:231], a[104:107], a[196:199], v[228:231]\n\t"
      "ds_read_b64_tr_b16 a[52:53], v24 offset:14656\n\t"
      "v_mfma_f32_16x16x32_bf16 v[232:235], a[104:107], a[200:203], v[232:235]\n\t"
      "v_mfma_f32_16x16x32_bf16 v[236:239], a[104:107], a[204:207], v[236:239]\n\t"
      "ds_read_b64_tr_b16 a[54:55], v24 offset:18240\n\t"
      "v_mfma_f32_16x16x32_bf16 v[240:243], a[108:111], a[192:195], v[240:243]\n\t"
      "v_mfma_f32_16x16x32_bf16 v[244:247], a[108:111], a[196:199], v[244:247]\n\t"
      "v_mfma_f32_16x16x32_bf16 v[248:251], a[108:111], a[200:203], v[248:251]\n\t"
      "global_load_lds_dwordx4 v19, s[40:41]\n\t"
      "s_add_u32 m0, m0, 0x400\n\t"
      "v_mfma_f32_16x16x32_bf16 v[252:255], a[108:111], a[204:207], v[252:255]\n\t"
      "s_waitcnt vmcnt(45)\n\t"
      "s_waitcnt lgkmcnt(0)\n\t"
      "v_mfma_f32_16x16x32_bf16 v[32:35], a[0:3], a[208:211], v[32:35]\n\t"
      "v_mfma_f32_16x16x32_bf16 v[36:39], a[0:3], a[212:215], v[36:39]\n\t"
      "ds_read_b64_tr_b16 a[56:57], v24 offset:7168\n\t"
      "v_mfma_f32_16x16x32_bf16 v[40:43], a[0:3], a[216:219], v[40:43]\n\t"
      "ds_read_b64_tr_b16 a[58:59], v24 offset:10752\n\t"
      "v_mfma_f32_16x16x32_bf16 v[44:47], a[0:3], a[220:223], v[44:47]\n\t"
      "v_mfma_f32_16x16x32_bf16 v[48:51], a[4:7], a[208:211], v[48:51]\n\t"
      "ds_read_b64_tr_b16 a[60:61], v24 offset:7200\n\t"
      "v_mfma_f32_16x16x32_bf16 v[52:55], a[4:7], a[212:215], v[52:55]\n\t"
      "v_mfma_f32_16x16x32_bf16 v[56:59], a[4:7], a[216:219], v[56:59]\n\t"
      "ds_read_b64_tr_b16 a[62:63], v24 offset:10784\n\t"
      "global_load_dwordx4 a[192:195], v26, s[42:43] offset:1024 sc1\n\t"
      "v_mfma_f32_16x16x32_bf16 v[60:63], a[4:7], a[220:223], v[60:63]\n\t"
      "v_mfma_f32_16x16x32_bf16 v[64:67], a[8:11], a[208:211], v[64:67]\n\t"
      "ds_read_b64_tr_b16 a[64:65], v24 offset:7232\n\t"
      "v_mfma_f32_16x16x32_bf16 v[68:71], a[8:11], a[212:215], v[68:71]\n\t"
      "v_mfma_f32_16x16x32_bf16 v[72:75], a[8:11], a[216:219], v[72:75]\n\t"
      "ds_read_b64_tr_b16 a[66:67], v24 offset:10816\n\t"
      "global_load_lds_dwordx4 v20, s[40:41]\n\t"
      "s_add_u32 m0, m0, 0x400\n\t"
      "v_mfma_f32_16x16x32_bf16 v[76:79], a[8:11], a[220:223], v[76:79]\n\t"
      "v_mfma_f32_16x16x32_bf16 v[80:83], a[12:15], a[208:211], v[80:83]\n\t"
      "ds_read_b64_tr_b16 a[68:69], v24 offset:7264\n\t"
      "v_mfma_f32_16x16x32_bf16 v[84:87], a[12:15], a[212:215], v[84:87]\n\t"
      "v_mfma_f32_16x16x32_bf16 v[88:91], a[12:15], a[216:219], v[88:91]\n\t"
      "ds_read_b64_tr_b16 a[70:71], v24 offset:10848\n\t"
      "v_mfma_f32_16x16x32_bf16 v[92:95], a[12:15], a[220:223], v[92:95]\n\t"
      "ds_read_b64_tr_b16 a[72:73], v24 offset:7296\n\t"
      "v_mfma_f32_16x16x32_bf16 v[96:99], a[16:19], a[208:211], v[96:99]\n\t"
      "v_mfma_f32_16x16x32_bf16 v[100:103], a[16:19], a[212:215], v[100:103]\n\t"
      "ds_read_b64_tr_b16 a[74:75], v24 offset:10880\n\t"
      "v_mfma_f32_16x16x32_bf16 v[104:107], a[16:19], a[216:219], v[104:107]\n\t"
      "v_mfma_f32_16x16x32_bf16 v[108:111], a[16:19], a[220:223], v[108:111]\n\t"
      "ds_read_b64_tr_b16 a[76:77], v24 offset:7328\n\t"
      "v_mfma_f32_16x16x32_bf16 v[112:115], a[20:23], a[208:211], v[112:115]\n\t"
      "global_load_dwordx4 a[196:199], v27, s[42:43] offset:1024 sc1\n\t"
      "v_mfma_f32_16x16x32_bf16 v[116:119], a[20:23], a[212:215], v[116:119]\n\t"
      "ds_read_b64_tr_b16 a[78:79], v24 offset:10912\n\t"
      "v_mfma_f32_16x16x32_bf16 v[120:123], a[20:23], a[216:219], v[120:123]\n\t"
      "v_mfma_f32_16x16x32_bf16 v[124:127], a[20:23], a[220:223], v[124:127]\n\t"
      "ds_read_b64_tr_b16 a[80:81], v24 offset:7360\n\t"
      "v_mfma_f32_16x16x32_bf16 v[128:131], a[24:27], a[208:211], v[128:131]\n\t"
      "v_mfma_f32_16x16x32_bf16 v[132:135], a[24:27], a[212:215], v[132:135]\n\t"
      "ds_read_b64_tr_b16 a[82:83], v24 offset:10944\n\t"
      "v_mfma_f32_16x16x32_bf16 v[136:139], a[24:27], a[216:219], v[136:139]\n\t"
      "global_load_lds_dwordx4 v21, s[40:41]\n\t"
      "s_add_u32 m0, m0, 0x400\n\t"
      "v_mfma_f32_16x16x32_bf16 v[140:143], a[24:27], a[220:223], v[140:143]\n\t"
      "ds_read_b64_tr_b16 a[84:85], v24 offset:21632\n\t"
      "v_mfma_f32_16x16x32_bf16 v[144:147], a[28:31], a[208:211], v[144:147]\n\t"
      "ds_read_b64_tr_b16 a[86:87], v24 offset:25216\n\t"
      "v_mfma_f32_16x16x32_bf16 v[148:151], a[28:31], a[212:215], v[148:151]\n\t"
      "v_mfma_f32_16x16x32_bf16 v[152:155], a[28:31], a[216:219], v[152:155]\n\t"
      "ds_read_b64_tr_b16 a[88:89], v24 offset:21664\n\t"
      "v_mfma_f32_16x16x32_bf16 v[156:159], a[28:31], a[220:223], v[156:159]\n\t"
      "v_mfma_f32_16x16x32_bf16 v[160:163], a[32:35], a[208:211], v[160:163]\n\t"
      "ds_read_b64_tr_b16 a[90:91], v24 offset:25248\n\t"
      "v_mfma_f32_16x16x32_bf16 v[164:167], a[32:35], a[212:215], v[164:167]\n\t"
      "v_mfma_f32_16x16x32_bf16 v[168:171], a[32:35], a[216:219], v[168:171]\n\t"
      "ds_read_b64_tr_b16 a[92:93], v24 offset:21696\n\t"
      "global_load_dwordx4 a[200:203], v28, s[42:43] offset:1024 sc1\n\t"
      "v_mfma_f32_16x16x32_bf16 v[172:175], a[32:35], a[220:223], v[172:175]\n\t"
      "v_mfma_f32_16x16x32_bf16 v[176:179], a[36:39], a[208:211], v[176:179]\n\t"
      "ds_read_b64_tr_b16 a[94:95], v24 offset:25280\n\t"
      "v_mfma_f32_16x16x32_bf16 v[180:183], a[36:39], a[212:215], v[180:183]\n\t"
      "v_mfma_f32_16x16x32_bf16 v[184:187], a[36:39], a[216:219], v[184:187]\n\t"
      "ds_read_b64_tr_b16 a[96:97], v24 offset:21728\n\t"
      "v_mfma_f32_16x16x32_bf16 v[188:191], a[36:39], a[220:223], v[188:191]\n\t"
      "v_mfma_f32_16x16x32_bf16 v[192:195], a[40:43], a[208:211], v[192:195]\n\t"
      "ds_read_b64_tr_b16 a[98:99], v24 offset:25312\n\t"
      "v_mfma_f32_16x16x32_bf16 v[196:199], a[40:43], a[212:215], v[196:199]\n\t"
      "ds_read_b64_tr_b16 a[100:101], v24 offset:21760\n\t"
      "v_mfma_f32_16x16x32_bf16 v[200:203], a[40:43], a[216:219], v[200:203]\n\t"
      "global_load_lds_dwordx4 v22, s[40:41]\n\t"
      "s_add_u32 s47, s47, 28928\n\t"
      "v_mfma_f32_16x16x32_bf16 v[204:207], a[40:43], a[220:223], v[204:207]\n\t"
      "ds_read_b64_tr_b16 a[102:103], v24 offset:25344\n\t"
      "s_cmp_eq_u32 s47, 115712\n\t"
      "s_cselect_b32 s47, 0, s47\n\t"
      "v_mfma_f32_16x16x32_bf16 v[208:211], a[44:47], a[208:211], v[208:211]\n\t"
      "v_add_u32 v23, s47, v25\n\t"
      "v_mfma_f32_16x16x32_bf16 v[212:215], a[44:47], a[212:215], v[212:215]\n\t"
      "ds_read_b64_tr_b16 a[104:105], v24 offset:21792\n\t"
      "v_mfma_f32_16x16x32_bf16 v[216:219], a[44:47], a[216:219], v[216:219]\n\t"
      "s_add_u32 s40, s40, 0x70000\n\t"
      "s_addc_u32 s41, s41, 0\n\t"
      "v_mfma_f32_16x16x32_bf16 v[220:223], a[44:47], a[220:223], v[220:223]\n\t"
      "ds_read_b64_tr_b16 a[106:107], v24 offset:25376\n\t"
      "v_mfma_f32_16x16x32_bf16 v[224:227], a[48:51], a[208:211], v[224:227]\n\t"
      "global_load_dwordx4 a[204:207], v29, s[42:43] offset:1024 sc1\n\t"
      "v_mfma_f32_16x16x32_bf16 v[228:231], a[48:51], a[212:215], v[228:231]\n\t"
      "ds_read_b64_tr_b16 a[108:109], v24 offset:21824\n\t"
      "v_mfma_f32_16x16x32_bf16 v[232:235], a[48:51], a[216:219], v[232:235]\n\t"
      "s_add_u32 s46, s46, 28928\n\t"
      "v_mfma_f32_16x16x32_bf16 v[236:239], a[48:51], a[220:223], v[236:239]\n\t"
      "ds_read_b64_tr_b16 a[110:111], v24 offset:25408\n\t"
      "v_mfma_f32_16x16x32_bf16 v[240:243], a[52:55], a[208:211], v[240:243]\n\t"
      "buffer_load_dword v30, v14, %[wsrd], s53 offen\n\t"
      "v_mfma_f32_16x16x32_bf16 v[244:247], a[52:55], a[212:215], v[244:247]\n\t"
      "s_cmp_eq_u32 s46, s50\n\t"
      "s_cselect_b32 s46, s49, s46\n\t"
      "v_mfma_f32_16x16x32_bf16 v[248:251], a[52:55], a[216:219], v[248:251]\n\t"
      "s_mov_b32 m0, s46\n\t"
      "v_mfma_f32_16x16x32_bf16 v[252:255], a[52:55], a[220:223], v[252:255]\n\t"
      "s_add_u32 s42, s42, 0x800\n\t"
      "s_addc_u32 s43, s43, 0\n\t"
      "s_waitcnt vmcnt(47)\n\t"
      "s_waitcnt lgkmcnt(0)\n\t"
      "v_mfma_f32_16x16x32_bf16 v[32:35], a[56:59], a[224:227], v[32:35]\n\t"
      "s_add_u32 s53, s53, 0x70000\n\t"
      "v_mfma_f32_16x16x32_bf16 v[36:39], a[56:59], a[228:231], v[36:39]\n\t"
      "v_mfma_f32_16x16x32_bf16 v[40:43], a[56:59], a[232:235], v[40:43]\n\t"
      "v_mfma_f32_16x16x32_bf16 v[44:47], a[56:59], a[236:239], v[44:47]\n\t"
      "v_mfma_f32_16x16x32_bf16 v[48:51], a[60:63], a[224:227], v[48:51]\n\t"
      "v_mfma_f32_16x16x32_bf16 v[52:55], a[60:63], a[228:231], v[52:55]\n\t"
      "v_mfma_f32_16x16x32_bf16 v[56:59], a[60:63], a[232:235], v[56:59]\n\t"
      "global_load_dwordx4 a[208:211], v26, s[42:43] offset:0 sc1\n\t"
      "v_mfma_f32_16x16x32_bf16 v[60:63], a[60:63], a[236:239], v[60:63]\n\t"
      "v_mfma_f32_16x16x32_bf16 v[64:67], a[64:67], a[224:227], v[64:67]\n\t"
      "s_waitcnt lgkmcnt(0)\n\t"
      "s_waitcnt vmcnt(37)\n\t"
      "s_barrier\n\t"
      "v_mfma_f32_16x16x32_bf16 v[68:71], a[64:67], a[228:231], v[68:71]\n\t"
      "ds_read_b64_tr_b16 a[0:1], v23 offset:0\n\t"
      "v_mfma_f32_16x16x32_bf16 v[72:75], a[64:67], a[232:235], v[72:75]\n\t"
      "ds_read_b64_tr_b16 a[2:3], v23 offset:3584\n\t"
      "v_mfma_f32_16x16x32_bf16 v[76:79], a[64:67], a[236:239], v[76:79]\n\t"
      "ds_read_b64_tr_b16 a[4:5], v23 offset:32\n\t"
      "v_mfma_f32_16x16x32_bf16 v[80:83], a[68:71], a[224:227], v[80:83]\n\t"
      "ds_read_b64_tr_b16 a[6:7], v23 offset:3616\n\t"
      "v_mfma_f32_16x16x32_bf16 v[84:87], a[68:71], a[228:231], v[84:87]\n\t"
      "ds_read_b64_tr_b16 a[8:9], v23 offset:64\n\t"
      "v_mfma_f32_16x16x32_bf16 v[88:91], a[68:71], a[232:235], v[88:91]\n\t"
      "ds_read_b64_tr_b16 a[10:11], v23 offset:3648\n\t"
      "v_mfma_f32_16x16x32_bf16 v[92:95], a[68:71], a[236:239], v[92:95]\n\t"
      "ds_read_b64_tr_b16 a[12:13], v23 offset:96\n\t"
      "v_mfma_f32_16x16x32_bf16 v[96:99], a[72:75], a[224:227], v[96:99]\n\t"
      "ds_read_b64_tr_b16 a[14:15], v23 offset:3680\n\t"
      "v_mfma_f32_16x16x32_bf16 v[100:103], a[72:75], a[228:231], v[100:103]\n\t"
      "ds_read_b64_tr_b16 a[16:17], v23 offset:128\n\t"
      "v_mfma_f32_16x16x32_bf16 v[104:107], a[72:75], a[232:235], v[104:107]\n\t"
      "ds_read_b64_tr_b16 a[18:19], v23 offset:3712\n\t"
      "global_load_lds_dwordx4 v16, s[40:41]\n\t"
      "s_add_u32 m0, m0, 0x400\n\t"
      "v_mfma_f32_16x16x32_bf16 v[108:111], a[72:75], a[236:239], v[108:111]\n\t"
      "ds_read_b64_tr_b16 a[20:21], v23 offset:160\n\t"
      "v_mfma_f32_16x16x32_bf16 v[112:115], a[76:79], a[224:227], v[112:115]\n\t"
      "global_load_dwordx4 a[212:215], v27, s[42:43] offset:0 sc1\n\t"
      "v_mfma_f32_16x16x32_bf16 v[116:119], a[76:79], a[228:231], v[116:119]\n\t"
      "ds_read_b64_tr_b16 a[22:23], v23 offset:3744\n\t"
      "v_mfma_f32_16x16x32_bf16 v[120:123], a[76:79], a[232:235], v[120:123]\n\t"
      "v_mfma_f32_16x16x32_bf16 v[124:127], a[76:79], a[236:239], v[124:127]\n\t"
      "ds_read_b64_tr_b16 a[24:25], v23 offset:192\n\t"
      "v_mfma_f32_16x16x32_bf16 v[128:131], a[80:83], a[224:227], v[128:131]\n\t"
      "v_mfma_f32_16x16x32_bf16 v[132:135], a[80:83], a[228:231], v[132:135]\n\t"
      "ds_read_b64_tr_b16 a[26:27], v23 offset:3776\n\t"
      "v_mfma_f32_16x16x32_bf16 v[136:139], a[80:83], a[232:235], v[136:139]\n\t"
      "v_mfma_f32_16x16x32_bf16 v[140:143], a[80:83], a[236:239], v[140:143]\n\t"
      "ds_read_b64_tr_b16 a[28:29], v23 offset:14464\n\t"
      "v_mfma_f32_16x16x32_bf16 v[144:147], a[84:87], a[224:227], v[144:147]\n\t"
      "ds_read_b64_tr_b16 a[30:31], v23 offset:18048\n\t"
      "v_mfma_f32_16x16x32_bf16 v[148:151], a[84:87], a[228:231], v[148:151]\n\t"
      "v_mfma_f32_16x16x32_bf16 v[152:155], a[84:87], a[232:235], v[152:155]\n\t"
      "ds_read_b64_tr_b16 a[32:33], v23 offset:14496\n\t"
      "global_load_lds_dwordx4 v17, s[40:41]\n\t"
      "s_add_u32 m0, m0, 0x400\n\t"
      "v_mfma_f32_16x16x32_bf16 v[156:159], a[84:87], a[236:239], v[156:159]\n\t"
      "v_mfma_f32_16x16x32_bf16 v[160:163], a[88:91], a[224:227], v[160:163]\n\t"
      "ds_read_b64_tr_b16 a[34:35], v23 offset:18080\n\t"
      "v_mfma_f32_16x16x32_bf16 v[164:167], a[88:91], a[228:231], v[164:167]\n\t"
      "v_mfma_f32_16x16x32_bf16 v[168:171], a[88:91], a[232:235], v[168:171]\n\t"
      "ds_read_b64_tr_b16 a[36:37], v23 offset:14528\n\t"
      "global_load_dwordx4 a[216:219], v28, s[42:43] offset:0 sc1\n\t"
      "v_mfma_f32_16x16x32_bf16 v[172:175], a[88:91], a[236:239], v[172:175]\n\t"
      "v_mfma_f32_16x16x32_bf16 v[176:179], a[92:95], a[224:227], v[176:179]\n\t"
      "ds_read_b64_tr_b16 a[38:39], v23 offset:18112\n\t"
      "v_mfma_f32_16x16x32_bf16 v[180:183], a[92:95], a[228:231], v[180:183]\n\t"
      "v_mfma_f32_16x16x32_bf16 v[184:187], a[92:95], a[232:235], v[184:187]\n\t"
      "ds_read_b64_tr_b16 a[40:41], v23 offset:14560\n\t"
      "v_mfma_f32_16x16x32_bf16 v[188:191], a[92:95], a[236:239], v[188:191]\n\t"
      "v_mfma_f32_16x16x32_bf16 v[192:195], a[96:99], a[224:227], v[192:195]\n\t"
      "ds_read_b64_tr_b16 a[42:43], v23 offset:18144\n\t"
      "v_mfma_f32_16x16x32_bf16 v[196:199], a[96:99], a[228:231], v[196:199]\n\t"
      "ds_read_b64_tr_b16 a[44:45], v23 offset:14592\n\t"
      "v_mfma_f32_16x16x32_bf16 v[200:203], a[96:99], a[232:235], v[200:203]\n\t"
      "global_load_lds_dwordx4 v18, s[40:41]\n\t"
      "s_add_u32 m0, m0, 0x400\n\t"
      "v_mfma_f32_16x16x32_bf16 v[204:207], a[96:99], a[236:239], v[204:207]\n\t"
      "ds_read_b64_tr_b16 a[46:47], v23 offset:18176\n\t"
      "v_mfma_f32_16x16x32_bf16 v[208:211], a[100:103], a[224:227], v[208:211]\n\t"
      "global_store_dwordx4 v26, a[240:243], s[62:63] sc1\n\t"
      "v_mfma_f32_16x16x32_bf16 v[212:215], a[100:103], a[228:231], v[212:215]\n\t"
      "ds_read_b64_tr_b16 a[48:49], v23 offset:14624\n\t"
      "v_mfma_f32_16x16x32_bf16 v[216:219], a[100:103], a[232:235], v[216:219]\n\t"
      "v_mfma_f32_16x16x32_bf16 v[220:223], a[100:103], a[236:239], v[220:223]\n\t"
      "ds_read_b64_tr_b16 a[50:51], v23 offset:18208\n\t"
      "v_mfma_f32_16x16x32_bf16 v[224:227], a[104:107], a[224:227], v[224:227]\n\t"
      "global_load_dwordx4 a[220:223], v29, s[42:43] offset:0 sc1\n\t"
      "s_add_u32 s62, s62, 0x400\n\t"
      "s_addc_u32 s63, s63, 0\n\t"
      "v_mfma_f32_16x16x32_bf16 v[228:231], a[104:107], a[228:231], v[228:231]\n\t"
      "ds_read_b64_tr_b16 a[52:53], v23 offset:14656\n\t"
      "v_mfma_f32_16x16x32_bf16 v[232:235], a[104:107], a[232:235], v[232:235]\n\t"
      "v_mfma_f32_16x16x32_bf16 v[236:239], a[104:107], a[236:239], v[236:239]\n\t"
      "ds_read_b64_tr_b16 a[54:55], v23 offset:18240\n\t"
      "v_mfma_f32_16x16x32_bf16 v[240:243], a[108:111], a[224:227], v[240:243]\n\t"
      "v_mfma_f32_16x16x32_bf16 v[244:247], a[108:111], a[228:231], v[244:247]\n\t"
      "v_mfma_f32_16x16x32_bf16 v[248:251], a[108:111], a[232:235], v[248:251]\n\t"
      "global_load_lds_dwordx4 v19, s[40:41]\n\t"
      "s_add_u32 m0, m0, 0x400\n\t"
      "v_mfma_f32_16x16x32_bf16 v[252:255], a[108:111], a[236:239], v[252:255]\n\t"
      "s_waitcnt vmcnt(49)\n\t"
      "s_waitcnt lgkmcnt(0)\n\t"
      "v_mfma_f32_16x16x32_bf16 v[32:35], a[0:3], a[112:115], v[32:35]\n\t"
      "v_mfma_f32_16x16x32_bf16 v[36:39], a[0:3], a[116:119], v[36:39]\n\t"
      "ds_read_b64_tr_b16 a[56:57], v23 offset:7168\n\t"
      "v_mfma_f32_16x16x32_bf16 v[40:43], a[0:3], a[120:123], v[40:43]\n\t"
      "ds_read_b64_tr_b16 a[58:59], v23 offset:10752\n\t"
      "v_mfma_f32_16x16x32_bf16 v[44:47], a[0:3], a[124:127], v[44:47]\n\t"
      "v_mfma_f32_16x16x32_bf16 v[48:51], a[4:7], a[112:115], v[48:51]\n\t"
      "ds_read_b64_tr_b16 a[60:61], v23 offset:7200\n\t"
      "v_mfma_f32_16x16x32_bf16 v[52:55], a[4:7], a[116:119], v[52:55]\n\t"
      "v_mfma_f32_16x16x32_bf16 v[56:59], a[4:7], a[120:123], v[56:59]\n\t"
      "ds_read_b64_tr_b16 a[62:63], v23 offset:10784\n\t"
      "global_load_dwordx4 a[224:227], v26, s[42:43] offset:1024 sc1\n\t"
      "v_mfma_f32_16x16x32_bf16 v[60:63], a[4:7], a[124:127], v[60:63]\n\t"
      "v_mfma_f32_16x16x32_bf16 v[64:67], a[8:11], a[112:115], v[64:67]\n\t"
      "ds_read_b64_tr_b16 a[64:65], v23 offset:7232\n\t"
      "v_mfma_f32_16x16x32_bf16 v[68:71], a[8:11], a[116:119], v[68:71]\n\t"
      "v_mfma_f32_16x16x32_bf16 v[72:75], a[8:11], a[120:123], v[72:75]\n\t"
      "ds_read_b64_tr_b16 a[66:67], v23 offset:10816\n\t"
      "global_load_lds_dwordx4 v20, s[40:41]\n\t"
      "s_add_u32 m0, m0, 0x400\n\t"
      "v_mfma_f32_16x16x32_bf16 v[76:79], a[8:11], a[124:127], v[76:79]\n\t"
      "v_mfma_f32_16x16x32_bf16 v[80:83], a[12:15], a[112:115], v[80:83]\n\t"
      "ds_read_b64_tr_b16 a[68:69], v23 offset:7264\n\t"
      "v_mfma_f32_16x16x32_bf16 v[84:87], a[12:15], a[116:119], v[84:87]\n\t"
      "v_mfma_f32_16x16x32_bf16 v[88:91], a[12:15], a[120:123], v[88:91]\n\t"
      "ds_read_b64_tr_b16 a[70:71], v23 offset:10848\n\t"
      "v_mfma_f32_16x16x32_bf16 v[92:95], a[12:15], a[124:127], v[92:95]\n\t"
      "ds_read_b64_tr_b16 a[72:73], v23 offset:7296\n\t"
      "v_mfma_f32_16x16x32_bf16 v[96:99], a[16:19], a[112:115], v[96:99]\n\t"
      "v_mfma_f32_16x16x32_bf16 v[100:103], a[16:19], a[116:119], v[100:103]\n\t"
      "ds_read_b64_tr_b16 a[74:75], v23 offset:10880\n\t"
      "v_mfma_f32_16x16x32_bf16 v[104:107], a[16:19], a[120:123], v[104:107]\n\t"
      "v_mfma_f32_16x16x32_bf16 v[108:111], a[16:19], a[124:127], v[108:111]\n\t"
      "ds_read_b64_tr_b16 a[76:77], v23 offset:7328\n\t"
      "v_mfma_f32_16x16x32_bf16 v[112:115], a[20:23], a[112:115], v[112:115]\n\t"
      "global_load_dwordx4 a[228:231], v27, s[42:43] offset:1024 sc1\n\t"
      "v_mfma_f32_16x16x32_bf16 v[116:119], a[20:23], a[116:119], v[116:119]\n\t"
      "ds_read_b64_tr_b16 a[78:79], v23 offset:10912\n\t"
      "v_mfma_f32_16x16x32_bf16 v[120:123], a[20:23], a[120:123], v[120:123]\n\t"
      "v_mfma_f32_16x16x32_bf16 v[124:127], a[20:23], a[124:127], v[124:127]\n\t"
      "ds_read_b64_tr_b16 a[80:81], v23 offset:7360\n\t"
      "v_mfma_f32_16x16x32_bf16 v[128:131], a[24:27], a[112:115], v[128:131]\n\t"
      "v_mfma_f32_16x16x32_bf16 v[132:135], a[24:27], a[116:119], v[132:135]\n\t"
      "ds_read_b64_tr_b16 a[82:83], v23 offset:10944\n\t"
      "v_mfma_f32_16x16x32_bf16 v[136:139], a[24:27], a[120:123], v[136:139]\n\t"
      "global_load_lds_dwordx4 v21, s[40:41]\n\t"
      "s_add_u32 m0, m0, 0x400\n\t"
      "v_mfma_f32_16x16x32_bf16 v[140:143], a[24:27], a[124:127], v[140:143]\n\t"
      "ds_read_b64_tr_b16 a[84:85], v23 offset:21632\n\t"
      "v_mfma_f32_16x16x32_bf16 v[144:147], a[28:31], a[112:115], v[144:147]\n\t"
      "ds_read_b64_tr_b16 a[86:87], v23 offset:25216\n\t"
      "v_mfma_f32_16x16x32_bf16 v[148:151], a[28:31], a[116:119], v[148:151]\n\t"
      "v_mfma_f32_16x16x32_bf16 v[152:155], a[28:31], a[120:123], v[152:155]\n\t"
      "ds_read_b64_tr_b16 a[88:89], v23 offset:21664\n\t"
      "v_mfma_f32_16x16x32_bf16 v[156:159], a[28:31], a[124:127], v[156:159]\n\t"
      "v_mfma_f32_16x16x32_bf16 v[160:163], a[32:35], a[112:115], v[160:163]\n\t"
      "ds_read_b64_tr_b16 a[90:91], v23 offset:25248\n\t"
      "v_mfma_f32_16x16x32_bf16 v[164:167], a[32:35], a[116:119], v[164:167]\n\t"
      "v_mfma_f32_16x16x32_bf16 v[168:171], a[32:35], a[120:123], v[168:171]\n\t"
      "ds_read_b64_tr_b16 a[92:93], v23 offset:21696\n\t"
      "global_load_dwordx4 a[232:235], v28, s[42:43] offset:1024 sc1\n\t"
      "v_mfma_f32_16x16x32_bf16 v[172:175], a[32:35], a[124:127], v[172:175]\n\t"
      "v_mfma_f32_16x16x32_bf16 v[176:179], a[36:39], a[112:115], v[176:179]\n\t"
      "ds_read_b64_tr_b16 a[94:95], v23 offset:25280\n\t"
      "v_mfma_f32_16x16x32_bf16 v[180:183], a[36:39], a[116:119], v[180:183]\n\t"
      "v_mfma_f32_16x16x32_bf16 v[184:187], a[36:39], a[120:123], v[184:187]\n\t"
      "ds_read_b64_tr_b16 a[96:97], v23 offset:21728\n\t"
      "v_mfma_f32_16x16x32_bf16 v[188:191], a[36:39], a[124:127], v[188:191]\n\t"
      "v_mfma_f32_16x16x32_bf16 v[192:195], a[40:43], a[112:115], v[192:195]\n\t"
      "ds_read_b64_tr_b16 a[98:99], v23 offset:25312\n\t"
      "v_mfma_f32_16x16x32_bf16 v[196:199], a[40:43], a[116:119], v[196:199]\n\t"
      "ds_read_b64_tr_b16 a[100:101], v23 offset:21760\n\t"
      "v_mfma_f32_16x16x32_bf16 v[200:203], a[40:43], a[120:123], v[200:203]\n\t"
      "global_load_lds_dwordx4 v22, s[40:41]\n\t"
      "s_add_u32 s47, s47, 28928\n\t"
      "v_mfma_f32_16x16x32_bf16 v[204:207], a[40:43], a[124:127], v[204:207]\n\t"
      "ds_read_b64_tr_b16 a[102:103], v23 offset:25344\n\t"
      "s_cmp_eq_u32 s47, 115712\n\t"
      "s_cselect_b32 s47, 0, s47\n\t"
      "v_mfma_f32_16x16x32_bf16 v[208:211], a[44:47], a[112:115], v[208:211]\n\t"
      "v_add_u32 v24, s47, v25\n\t"
      "v_mfma_f32_16x16x32_bf16 v[212:215], a[44:47], a[116:119], v[212:215]\n\t"
      "ds_read_b64_tr_b16 a[104:105], v23 offset:21792\n\t"
      "v_mfma_f32_16x16x32_bf16 v[216:219], a[44:47], a[120:123], v[216:219]\n\t"
      "s_add_u32 s40, s40, 0x70000\n\t"
      "s_addc_u32 s41, s41, 0\n\t"
      "v_mfma_f32_16x16x32_bf16 v[220:223], a[44:47], a[124:127], v[220:223]\n\t"
      "ds_read_b64_tr_b16 a[106:107], v23 offset:25376\n\t"
      "v_mfma_f32_16x16x32_bf16 v[224:227], a[48:51], a[112:115], v[224:227]\n\t"
      "global_load_dwordx4 a[236:239], v29, s[42:43] offset:1024 sc1\n\t"
      "v_mfma_f32_16x16x32_bf16 v[228:231], a[48:51], a[116:119], v[228:231]\n\t"
      "ds_read_b64_tr_b16 a[108:109], v23 offset:21824\n\t"
      "v_mfma_f32_16x16x32_bf16 v[232:235], a[48:51], a[120:123], v[232:235]\n\t"
      "s_add_u32 s46, s46, 28928\n\t"
      "v_mfma_f32_16x16x32_bf16 v[236:239], a[48:51], a[124:127], v[236:239]\n\t"
      "ds_read_b64_tr_b16 a[110:111], v23 offset:25408\n\t"
      "v_mfma_f32_16x16x32_bf16 v[240:243], a[52:55], a[112:115], v[240:243]\n\t"
      "buffer_load_dword v30, v14, %[wsrd], s53 offen\n\t"
      "v_mfma_f32_16x16x32_bf16 v[244:247], a[52:55], a[116:119], v[244:247]\n\t"
      "s_cmp_eq_u32 s46, s50\n\t"
      "s_cselect_b32 s46, s49, s46\n\t"
      "v_mfma_f32_16x16x32_bf16 v[248:251], a[52:55], a[120:123], v[248:251]\n\t"
      "s_mov_b32 m0, s46\n\t"
      "v_mfma_f32_16x16x32_bf16 v[252:255], a[52:55], a[124:127], v[252:255]\n\t"
      "s_add_u32 s42, s42, 0x800\n\t"
      "s_addc_u32 s43, s43, 0\n\t"
      "s_waitcnt vmcnt(51)\n\t"
      "s_waitcnt lgkmcnt(0)\n\t"
      "v_mfma_f32_16x16x32_bf16 v[32:35], a[56:59], a[128:131], v[32:35]\n\t"
      "s_add_u32 s53, s53, 0x70000\n\t"
      "v_mfma_f32_16x16x32_bf16 v[36:39], a[56:59], a[132:135], v[36:39]\n\t"
      "v_mfma_f32_16x16x32_bf16 v[40:43], a[56:59], a[136:139], v[40:43]\n\t"
      "v_mfma_f32_16x16x32_bf16 v[44:47], a[56:59], a[140:143], v[44:47]\n\t"
      "v_mfma_f32_16x16x32_bf16 v[48:51], a[60:63], a[128:131], v[48:51]\n\t"
      "v_mfma_f32_16x16x32_bf16 v[52:55], a[60:63], a[132:135], v[52:55]\n\t"
      "v_mfma_f32_16x16x32_bf16 v[56:59], a[60:63], a[136:139], v[56:59]\n\t"
      "global_load_dwordx4 a[112:115], v26, s[42:43] offset:0 sc1\n\t"
      "v_mfma_f32_16x16x32_bf16 v[60:63], a[60:63], a[140:143], v[60:63]\n\t"
      "v_mfma_f32_16x16x32_bf16 v[64:67], a[64:67], a[128:131], v[64:67]\n\t"
      "s_waitcnt lgkmcnt(0)\n\t"
      "s_waitcnt vmcnt(37)\n\t"
      "s_barrier\n\t"
      "v_mfma_f32_16x16x32_bf16 v[68:71], a[64:67], a[132:135], v[68:71]\n\t"
      "ds_read_b64_tr_b16 a[0:1], v24 offset:0\n\t"
      "v_mfma_f32_16x16x32_bf16 v[72:75], a[64:67], a[136:139], v[72:75]\n\t"
      "ds_read_b64_tr_b16 a[2:3], v24 offset:3584\n\t"
      "v_mfma_f32_16x16x32_bf16 v[76:79], a[64:67], a[140:143], v[76:79]\n\t"
      "ds_read_b64_tr_b16 a[4:5], v24 offset:32\n\t"
      "v_mfma_f32_16x16x32_bf16 v[80:83], a[68:71], a[128:131], v[80:83]\n\t"
      "ds_read_b64_tr_b16 a[6:7], v24 offset:3616\n\t"
      "v_mfma_f32_16x16x32_bf16 v[84:87], a[68:71], a[132:135], v[84:87]\n\t"
      "ds_read_b64_tr_b16 a[8:9], v24 offset:64\n\t"
      "v_mfma_f32_16x16x32_bf16 v[88:91], a[68:71], a[136:139], v[88:91]\n\t"
      "ds_read_b64_tr_b16 a[10:11], v24 offset:3648\n\t"
      "v_mfma_f32_16x16x32_bf16 v[92:95], a[68:71], a[140:143], v[92:95]\n\t"
      "ds_read_b64_tr_b16 a[12:13], v24 offset:96\n\t"
      "v_mfma_f32_16x16x32_bf16 v[96:99], a[72:75], a[128:131], v[96:99]\n\t"
      "ds_read_b64_tr_b16 a[14:15], v24 offset:3680\n\t"
      "v_mfma_f32_16x16x32_bf16 v[100:103], a[72:75], a[132:135], v[100:103]\n\t"
      "ds_read_b64_tr_b16 a[16:17], v24 offset:128\n\t"
      "v_mfma_f32_16x16x32_bf16 v[104:107], a[72:75], a[136:139], v[104:107]\n\t"
      "ds_read_b64_tr_b16 a[18:19], v24 offset:3712\n\t"
      "global_load_lds_dwordx4 v16, s[40:41]\n\t"
      "s_add_u32 m0, m0, 0x400\n\t"
      "v_mfma_f32_16x16x32_bf16 v[108:111], a[72:75], a[140:143], v[108:111]\n\t"
      "ds_read_b64_tr_b16 a[20:21], v24 offset:160\n\t"
      "v_mfma_f32_16x16x32_bf16 v[112:115], a[76:79], a[128:131], v[112:115]\n\t"
      "global_load_dwordx4 a[116:119], v27, s[42:43] offset:0 sc1\n\t"
      "v_mfma_f32_16x16x32_bf16 v[116:119], a[76:79], a[132:135], v[116:119]\n\t"
      "ds_read_b64_tr_b16 a[22:23], v24 offset:3744\n\t"
      "v_mfma_f32_16x16x32_bf16 v[120:123], a[76:79], a[136:139], v[120:123]\n\t"
      "v_mfma_f32_16x16x32_bf16 v[124:127], a[76:79], a[140:143], v[124:127]\n\t"
      "ds_read_b64_tr_b16 a[24:25], v24 offset:192\n\t"
      "v_mfma_f32_16x16x32_bf16 v[128:131], a[80:83], a[128:131], v[128:131]\n\t"
      "v_mfma_f32_16x16x32_bf16 v[132:135], a[80:83], a[132:135], v[132:135]\n\t"
      "ds_read_b64_tr_b16 a[26:27], v24 offset:3776\n\t"
      "v_mfma_f32_16x16x32_bf16 v[136:139], a[80:83], a[136:139], v[136:139]\n\t"
      "v_mfma_f32_16x16x32_bf16 v[140:143], a[80:83], a[140:143], v[140:143]\n\t"
      "ds_read_b64_tr_b16 a[28:29], v24 offset:14464\n\t"
      "v_mfma_f32_16x16x32_bf16 v[144:147], a[84:87], a[128:131], v[144:147]\n\t"
      "ds_read_b64_tr_b16 a[30:31], v24 offset:18048\n\t"
      "v_mfma_f32_16x16x32_bf16 v[148:151], a[84:87], a[132:135], v[148:151]\n\t"
      "v_mfma_f32_16x16x32_bf16 v[152:155], a[84:87], a[136:139], v[152:155]\n\t"
      "ds_read_b64_tr_b16 a[32:33], v24 offset:14496\n\t"
      "global_load_lds_dwordx4 v17, s[40:41]\n\t"
      "s_add_u32 m0, m0, 0x400\n\t"
      "v_mfma_f32_16x16x32_bf16 v[156:159], a[84:87], a[140:143], v[156:159]\n\t"
      "v_mfma_f32_16x16x32_bf16 v[160:163], a[88:91], a[128:131], v[160:163]\n\t"
      "ds_read_b64_tr_b16 a[34:35], v24 offset:18080\n\t"
      "v_mfma_f32_16x16x32_bf16 v[164:167], a[88:91], a[132:135], v[164:167]\n\t"
      "v_mfma_f32_16x16x32_bf16 v[168:171], a[88:91], a[136:139], v[168:171]\n\t"
      "ds_read_b64_tr_b16 a[36:37], v24 offset:14528\n\t"
      "global_load_dwordx4 a[120:123], v28, s[42:43] offset:0 sc1\n\t"
      "v_mfma_f32_16x16x32_bf16 v[172:175], a[88:91], a[140:143], v[172:175]\n\t"
      "v_mfma_f32_16x16x32_bf16 v[176:179], a[92:95], a[128:131], v[176:179]\n\t"
      "ds_read_b64_tr_b16 a[38:39], v24 offset:18112\n\t"
      "v_mfma_f32_16x16x32_bf16 v[180:183], a[92:95], a[132:135], v[180:183]\n\t"
      "v_mfma_f32_16x16x32_bf16 v[184:187], a[92:95], a[136:139], v[184:187]\n\t"
      "ds_read_b64_tr_b16 a[40:41], v24 offset:14560\n\t"
      "v_mfma_f32_16x16x32_bf16 v[188:191], a[92:95], a[140:143], v[188:191]\n\t"
      "v_mfma_f32_16x16x32_bf16 v[192:195], a[96:99], a[128:131], v[192:195]\n\t"
      "ds_read_b64_tr_b16 a[42:43], v24 offset:18144\n\t"
      "v_mfma_f32_16x16x32_bf16 v[196:199], a[96:99], a[132:135], v[196:199]\n\t"
      "ds_read_b64_tr_b16 a[44:45], v24 offset:14592\n\t"
      "v_mfma_f32_16x16x32_bf16 v[200:203], a[96:99], a[136:139], v[200:203]\n\t"
      "global_load_lds_dwordx4 v18, s[40:41]\n\t"
      "s_add_u32 m0, m0, 0x400\n\t"
      "v_mfma_f32_16x16x32_bf16 v[204:207], a[96:99], a[140:143], v[204:207]\n\t"
      "ds_read_b64_tr_b16 a[46:47], v24 offset:18176\n\t"
      "v_mfma_f32_16x16x32_bf16 v[208:211], a[100:103], a[128:131], v[208:211]\n\t"
      "global_store_dwordx4 v26, a[240:243], s[62:63] sc1\n\t"
      "v_mfma_f32_16x16x32_bf16 v[212:215], a[100:103], a[132:135], v[212:215]\n\t"
      "ds_read_b64_tr_b16 a[48:49], v24 offset:14624\n\t"
      "v_mfma_f32_16x16x32_bf16 v[216:219], a[100:103], a[136:139], v[216:219]\n\t"
      "v_mfma_f32_16x16x32_bf16 v[220:223], a[100:103], a[140:143], v[220:223]\n\t"
      "ds_read_b64_tr_b16 a[50:51], v24 offset:18208\n\t"
      "v_mfma_f32_16x16x32_bf16 v[224:227], a[104:107], a[128:131], v[224:227]\n\t"
      "global_load_dwordx4 a[124:127], v29, s[42:43] offset:0 sc1\n\t"
      "s_add_u32 s62, s62, 0x400\n\t"
      "s_addc_u32 s63, s63, 0\n\t"
      "v_mfma_f32_16x16x32_bf16 v[228:231], a[104:107], a[132:135], v[228:231]\n\t"
      "ds_read_b64_tr_b16 a[52:53], v24 offset:14656\n\t"
      "v_mfma_f32_16x16x32_bf16 v[232:235], a[104:107], a[136:139], v[232:235]\n\t"
      "v_mfma_f32_16x16x32_bf16 v[236:239], a[104:107], a[140:143], v[236:239]\n\t"
      "ds_read_b64_tr_b16 a[54:55], v24 offset:18240\n\t"
      "v_mfma_f32_16x16x32_bf16 v[240:243], a[108:111], a[128:131], v[240:243]\n\t"
      "v_mfma_f32_16x16x32_bf16 v[244:247], a[108:111], a[132:135], v[244:247]\n\t"
      "v_mfma_f32_16x16x32_bf16 v[248:251], a[108:111], a[136:139], v[248:251]\n\t"
      "global_load_lds_dwordx4 v19, s[40:41]\n\t"
      "s_add_u32 m0, m0, 0x400\n\t"
      "v_mfma_f32_16x16x32_bf16 v[252:255], a[108:111], a[140:143], v[252:255]\n\t"
      "s_waitcnt vmcnt(52)\n\t"
      "s_waitcnt lgkmcnt(0)\n\t"
      "v_mfma_f32_16x16x32_bf16 v[32:35], a[0:3], a[144:147], v[32:35]\n\t"
      "v_mfma_f32_16x16x32_bf16 v[36:39], a[0:3], a[148:151], v[36:39]\n\t"
      "ds_read_b64_tr_b16 a[56:57], v24 offset:7168\n\t"
      "v_mfma_f32_16x16x32_bf16 v[40:43], a[0:3], a[152:155], v[40:43]\n\t"
      "ds_read_b64_tr_b16 a[58:59], v24 offset:10752\n\t"
      "v_mfma_f32_16x16x32_bf16 v[44:47], a[0:3], a[156:159], v[44:47]\n\t"
      "v_mfma_f32_16x16x32_bf16 v[48:51], a[4:7], a[144:147], v[48:51]\n\t"
      "ds_read_b64_tr_b16 a[60:61], v24 offset:7200\n\t"
      "v_mfma_f32_16x16x32_bf16 v[52:55], a[4:7], a[148:151], v[52:55]\n\t"
      "v_mfma_f32_16x16x32_bf16 v[56:59], a[4:7], a[152:155], v[56:59]\n\t"
      "ds_read_b64_tr_b16 a[62:63], v24 offset:10784\n\t"
      "global_load_dwordx4 a[128:131], v26, s[42:43] offset:1024 sc1\n\t"
      "v_mfma_f32_16x16x32_bf16 v[60:63], a[4:7], a[156:159], v[60:63]\n\t"
      "v_mfma_f32_16x16x32_bf16 v[64:67], a[8:11], a[144:147], v[64:67]\n\t"
      "ds_read_b64_tr_b16 a[64:65], v24 offset:7232\n\t"
      "v_mfma_f32_16x16x32_bf16 v[68:71], a[8:11], a[148:151], v[68:71]\n\t"
      "v_mfma_f32_16x16x32_bf16 v[72:75], a[8:11], a[152:155], v[72:75]\n\t"
      "ds_read_b64_tr_b16 a[66:67], v24 offset:10816\n\t"
      "global_load_lds_dwordx4 v20, s[40:41]\n\t"
      "s_add_u32 m0, m0, 0x400\n\t"
      "v_mfma_f32_16x16x32_bf16 v[76:79], a[8:11], a[156:159], v[76:79]\n\t"
      "v_mfma_f32_16x16x32_bf16 v[80:83], a[12:15], a[144:147], v[80:83]\n\t"
      "ds_read_b64_tr_b16 a[68:69], v24 offset:7264\n\t"
      "v_mfma_f32_16x16x32_bf16 v[84:87], a[12:15], a[148:151], v[84:87]\n\t"
      "v_mfma_f32_16x16x32_bf16 v[88:91], a[12:15], a[152:155], v[88:91]\n\t"
      "ds_read_b64_tr_b16 a[70:71], v24 offset:10848\n\t"
      "v_mfma_f32_16x16x32_bf16 v[92:95], a[12:15], a[156:159], v[92:95]\n\t"
      "ds_read_b64_tr_b16 a[72:73], v24 offset:7296\n\t"
      "v_mfma_f32_16x16x32_bf16 v[96:99], a[16:19], a[144:147], v[96:99]\n\t"
      "v_mfma_f32_16x16x32_bf16 v[100:103], a[16:19], a[148:151], v[100:103]\n\t"
      "ds_read_b64_tr_b16 a[74:75], v24 offset:10880\n\t"
      "v_mfma_f32_16x16x32_bf16 v[104:107], a[16:19], a[152:155], v[104:107]\n\t"
      "v_mfma_f32_16x16x32_bf16 v[108:111], a[16:19], a[156:159], v[108:111]\n\t"
      "ds_read_b64_tr_b16 a[76:77], v24 offset:7328\n\t"
      "v_mfma_f32_16x16x32_bf16 v[112:115], a[20:23], a[144:147], v[112:115]\n\t"
      "global_load_dwordx4 a[132:135], v27, s[42:43] offset:1024 sc1\n\t"
      "v_mfma_f32_16x16x32_bf16 v[116:119], a[20:23], a[148:151], v[116:119]\n\t"
      "ds_read_b64_tr_b16 a[78:79], v24 offset:10912\n\t"
      "v_mfma_f32_16x16x32_bf16 v[120:123], a[20:23], a[152:155], v[120:123]\n\t"
      "v_mfma_f32_16x16x32_bf16 v[124:127], a[20:23], a[156:159], v[124:127]\n\t"
      "ds_read_b64_tr_b16 a[80:81], v24 offset:7360\n\t"
      "v_mfma_f32_16x16x32_bf16 v[128:131], a[24:27], a[144:147], v[128:131]\n\t"
      "v_mfma_f32_16x16x32_bf16 v[132:135], a[24:27], a[148:151], v[132:135]\n\t"
      "ds_read_b64_tr_b16 a[82:83], v24 offset:10944\n\t"
      "v_mfma_f32_16x16x32_bf16 v[136:139], a[24:27], a[152:155], v[136:139]\n\t"
      "global_load_lds_dwordx4 v21, s[40:41]\n\t"
      "s_add_u32 m0, m0, 0x400\n\t"
      "v_mfma_f32_16x16x32_bf16 v[140:143], a[24:27], a[156:159], v[140:143]\n\t"
      "ds_read_b64_tr_b16 a[84:85], v24 offset:21632\n\t"
      "v_mfma_f32_16x16x32_bf16 v[144:147], a[28:31], a[144:147], v[144:147]\n\t"
      "ds_read_b64_tr_b16 a[86:87], v24 offset:25216\n\t"
      "v_mfma_f32_16x16x32_bf16 v[148:151], a[28:31], a[148:151], v[148:151]\n\t"
      "v_mfma_f32_16x16x32_bf16 v[152:155], a[28:31], a[152:155], v[152:155]\n\t"
      "ds_read_b64_tr_b16 a[88:89], v24 offset:21664\n\t"
      "v_mfma_f32_16x16x32_bf16 v[156:159], a[28:31], a[156:159], v[156:159]\n\t"
      "v_mfma_f32_16x16x32_bf16 v[160:163], a[32:35], a[144:147], v[160:163]\n\t"
      "ds_read_b64_tr_b16 a[90:91], v24 offset:25248\n\t"
      "v_mfma_f32_16x16x32_bf16 v[164:167], a[32:35], a[148:151], v[164:167]\n\t"
      "v_mfma_f32_16x16x32_bf16 v[168:171], a[32:35], a[152:155], v[168:171]\n\t"
      "ds_read_b64_tr_b16 a[92:93], v24 offset:21696\n\t"
      "global_load_dwordx4 a[136:139], v28, s[42:43] offset:1024 sc1\n\t"
      "v_mfma_f32_16x16x32_bf16 v[172:175], a[32:35], a[156:159], v[172:175]\n\t"
      "v_mfma_f32_16x16x32_bf16 v[176:179], a[36:39], a[144:147], v[176:179]\n\t"
      "ds_read_b64_tr_b16 a[94:95], v24 offset:25280\n\t"
      "v_mfma_f32_16x16x32_bf16 v[180:183], a[36:39], a[148:151], v[180:183]\n\t"
      "v_mfma_f32_16x16x32_bf16 v[184:187], a[36:39], a[152:155], v[184:187]\n\t"
      "ds_read_b64_tr_b16 a[96:97], v24 offset:21728\n\t"
      "v_mfma_f32_16x16x32_bf16 v[188:191], a[36:39], a[156:159], v[188:191]\n\t"
      "v_mfma_f32_16x16x32_bf16 v[192:195], a[40:43], a[144:147], v[192:195]\n\t"
      "ds_read_b64_tr_b16 a[98:99], v24 offset:25312\n\t"
      "v_mfma_f32_16x16x32_bf16 v[196:199], a[40:43], a[148:151], v[196:199]\n\t"
      "ds_read_b64_tr_b16 a[100:101], v24 offset:21760\n\t"
      "v_mfma_f32_16x16x32_bf16 v[200:203], a[40:43], a[152:155], v[200:203]\n\t"
      "global_load_lds_dwordx4 v22, s[40:41]\n\t"
      "s_add_u32 s47, s47, 28928\n\t"
      "v_mfma_f32_16x16x32_bf16 v[204:207], a[40:43], a[156:159], v[204:207]\n\t"
      "ds_read_b64_tr_b16 a[102:103], v24 offset:25344\n\t"
      "s_cmp_eq_u32 s47, 115712\n\t"
      "s_cselect_b32 s47, 0, s47\n\t"
      "v_mfma_f32_16x16x32_bf16 v[208:211], a[44:47], a[144:147], v[208:211]\n\t"
      "v_add_u32 v23, s47, v25\n\t"
      "v_mfma_f32_16x16x32_bf16 v[212:215], a[44:47], a[148:151], v[212:215]\n\t"
      "ds_read_b64_tr_b16 a[104:105], v24 offset:21792\n\t"
      "v_mfma_f32_16x16x32_bf16 v[216:219], a[44:47], a[152:155], v[216:219]\n\t"
      "s_add_u32 s40, s40, 0x70000\n\t"
      "s_addc_u32 s41, s41, 0\n\t"
      "v_mfma_f32_16x16x32_bf16 v[220:223], a[44:47], a[156:159], v[220:223]\n\t"
      "ds_read_b64_tr_b16 a[106:107], v24 offset:25376\n\t"
      "v_mfma_f32_16x16x32_bf16 v[224:227], a[48:51], a[144:147], v[224:227]\n\t"
      "global_load_dwordx4 a[140:143], v29, s[42:43] offset:1024 sc1\n\t"
      "v_mfma_f32_16x16x32_bf16 v[228:231], a[48:51], a[148:151], v[228:231]\n\t"
      "ds_read_b64_tr_b16 a[108:109], v24 offset:21824\n\t"
      "v_mfma_f32_16x16x32_bf16 v[232:235], a[48:51], a[152:155], v[232:235]\n\t"
      "s_add_u32 s46, s46, 28928\n\t"
      "v_mfma_f32_16x16x32_bf16 v[236:239], a[48:51], a[156:159], v[236:239]\n\t"
      "ds_read_b64_tr_b16 a[110:111], v24 offset:25408\n\t"
      "v_mfma_f32_16x16x32_bf16 v[240:243], a[52:55], a[144:147], v[240:243]\n\t"
      "buffer_load_dword v30, v14, %[wsrd], s53 offen\n\t"
      "v_mfma_f32_16x16x32_bf16 v[244:247], a[52:55], a[148:151], v[244:247]\n\t"
      "s_cmp_eq_u32 s46, s50\n\t"
      "s_cselect_b32 s46, s49, s46\n\t"
      "v_mfma_f32_16x16x32_bf16 v[248:251], a[52:55], a[152:155], v[248:251]\n\t"
      "s_mov_b32 m0, s46\n\t"
      "v_mfma_f32_16x16x32_bf16 v[252:255], a[52:55], a[156:159], v[252:255]\n\t"
      "s_add_u32 s42, s42, 0x800\n\t"
      "s_addc_u32 s43, s43, 0\n\t"
      "s_waitcnt vmcnt(52)\n\t"
      "s_waitcnt lgkmcnt(0)\n\t"
      "v_mfma_f32_16x16x32_bf16 v[32:35], a[56:59], a[160:163], v[32:35]\n\t"
      "s_add_u32 s53, s53, 0x70000\n\t"
      "v_mfma_f32_16x16x32_bf16 v[36:39], a[56:59], a[164:167], v[36:39]\n\t"
      "v_mfma_f32_16x16x32_bf16 v[40:43], a[56:59], a[168:171], v[40:43]\n\t"
      "v_mfma_f32_16x16x32_bf16 v[44:47], a[56:59], a[172:175], v[44:47]\n\t"
      "v_mfma_f32_16x16x32_bf16 v[48:51], a[60:63], a[160:163], v[48:51]\n\t"
      "v_mfma_f32_16x16x32_bf16 v[52:55], a[60:63], a[164:167], v[52:55]\n\t"
      "v_mfma_f32_16x16x32_bf16 v[56:59], a[60:63], a[168:171], v[56:59]\n\t"
      "global_load_dwordx4 a[144:147], v26, s[42:43] offset:0 sc1\n\t"
      "v_mfma_f32_16x16x32_bf16 v[60:63], a[60:63], a[172:175], v[60:63]\n\t"
      "v_mfma_f32_16x16x32_bf16 v[64:67], a[64:67], a[160:163], v[64:67]\n\t"
      "s_waitcnt lgkmcnt(0)\n\t"
      "s_waitcnt vmcnt(37)\n\t"
      "s_barrier\n\t"
      "v_mfma_f32_16x16x32_bf16 v[68:71], a[64:67], a[164:167], v[68:71]\n\t"
      "ds_read_b64_tr_b16 a[0:1], v23 offset:0\n\t"
      "v_mfma_f32_16x16x32_bf16 v[72:75], a[64:67], a[168:171], v[72:75]\n\t"
      "ds_read_b64_tr_b16 a[2:3], v23 offset:3584\n\t"
      "v_mfma_f32_16x16x32_bf16 v[76:79], a[64:67], a[172:175], v[76:79]\n\t"
      "ds_read_b64_tr_b16 a[4:5], v23 offset:32\n\t"
      "v_mfma_f32_16x16x32_bf16 v[80:83], a[68:71], a[160:163], v[80:83]\n\t"
      "ds_read_b64_tr_b16 a[6:7], v23 offset:3616\n\t"
      "v_mfma_f32_16x16x32_bf16 v[84:87], a[68:71], a[164:167], v[84:87]\n\t"
      "ds_read_b64_tr_b16 a[8:9], v23 offset:64\n\t"
      "v_mfma_f32_16x16x32_bf16 v[88:91], a[68:71], a[168:171], v[88:91]\n\t"
      "ds_read_b64_tr_b16 a[10:11], v23 offset:3648\n\t"
      "v_mfma_f32_16x16x32_bf16 v[92:95], a[68:71], a[172:175], v[92:95]\n\t"
      "ds_read_b64_tr_b16 a[12:13], v23 offset:96\n\t"
      "v_mfma_f32_16x16x32_bf16 v[96:99], a[72:75], a[160:163], v[96:99]\n\t"
      "ds_read_b64_tr_b16 a[14:15], v23 offset:3680\n\t"
      "v_mfma_f32_16x16x32_bf16 v[100:103], a[72:75], a[164:167], v[100:103]\n\t"
      "ds_read_b64_tr_b16 a[16:17], v23 offset:128\n\t"
      "v_mfma_f32_16x16x32_bf16 v[104:107], a[72:75], a[168:171], v[104:107]\n\t"
      "ds_read_b64_tr_b16 a[18:19], v23 offset:3712\n\t"
      "global_load_lds_dwordx4 v16, s[40:41]\n\t"
      "s_add_u32 m0, m0, 0x400\n\t"
      "v_mfma_f32_16x16x32_bf16 v[108:111], a[72:75], a[172:175], v[108:111]\n\t"
      "ds_read_b64_tr_b16 a[20:21], v23 offset:160\n\t"
      "v_mfma_f32_16x16x32_bf16 v[112:115], a[76:79], a[160:163], v[112:115]\n\t"
      "global_load_dwordx4 a[148:151], v27, s[42:43] offset:0 sc1\n\t"
      "v_mfma_f32_16x16x32_bf16 v[116:119], a[76:79], a[164:167], v[116:119]\n\t"
      "ds_read_b64_tr_b16 a[22:23], v23 offset:3744\n\t"
      "v_mfma_f32_16x16x32_bf16 v[120:123], a[76:79], a[168:171], v[120:123]\n\t"
      "v_mfma_f32_16x16x32_bf16 v[124:127], a[76:79], a[172:175], v[124:127]\n\t"
      "ds_read_b64_tr_b16 a[24:25], v23 offset:192\n\t"
      "v_mfma_f32_16x16x32_bf16 v[128:131], a[80:83], a[160:163], v[128:131]\n\t"
      "v_mfma_f32_16x16x32_bf16 v[132:135], a[80:83], a[164:167], v[132:135]\n\t"
      "ds_read_b64_tr_b16 a[26:27], v23 offset:3776\n\t"
      "v_mfma_f32_16x16x32_bf16 v[136:139], a[80:83], a[168:171], v[136:139]\n\t"
      "v_mfma_f32_16x16x32_bf16 v[140:143], a[80:83], a[172:175], v[140:143]\n\t"
      "ds_read_b64_tr_b16 a[28:29], v23 offset:14464\n\t"
      "v_mfma_f32_16x16x32_bf16 v[144:147], a[84:87], a[160:163], v[144:147]\n\t"
      "ds_read_b64_tr_b16 a[30:31], v23 offset:18048\n\t"
      "v_mfma_f32_16x16x32_bf16 v[148:151], a[84:87], a[164:167], v[148:151]\n\t"
      "v_mfma_f32_16x16x32_bf16 v[152:155], a[84:87], a[168:171], v[152:155]\n\t"
      "ds_read_b64_tr_b16 a[32:33], v23 offset:14496\n\t"
      "global_load_lds_dwordx4 v17, s[40:41]\n\t"
      "s_add_u32 m0, m0, 0x400\n\t"
      "v_mfma_f32_16x16x32_bf16 v[156:159], a[84:87], a[172:175], v[156:159]\n\t"
      "v_mfma_f32_16x16x32_bf16 v[160:163], a[88:91], a[160:163], v[160:163]\n\t"
      "ds_read_b64_tr_b16 a[34:35], v23 offset:18080\n\t"
      "v_mfma_f32_16x16x32_bf16 v[164:167], a[88:91], a[164:167], v[164:167]\n\t"
      "v_mfma_f32_16x16x32_bf16 v[168:171], a[88:91], a[168:171], v[168:171]\n\t"
      "ds_read_b64_tr_b16 a[36:37], v23 offset:14528\n\t"
      "global_load_dwordx4 a[152:155], v28, s[42:43] offset:0 sc1\n\t"
      "v_mfma_f32_16x16x32_bf16 v[172:175], a[88:91], a[172:175], v[172:175]\n\t"
      "v_mfma_f32_16x16x32_bf16 v[176:179], a[92:95], a[160:163], v[176:179]\n\t"
      "ds_read_b64_tr_b16 a[38:39], v23 offset:18112\n\t"
      "v_mfma_f32_16x16x32_bf16 v[180:183], a[92:95], a[164:167], v[180:183]\n\t"
      "v_mfma_f32_16x16x32_bf16 v[184:187], a[92:95], a[168:171], v[184:187]\n\t"
      "ds_read_b64_tr_b16 a[40:41], v23 offset:14560\n\t"
      "v_mfma_f32_16x16x32_bf16 v[188:191], a[92:95], a[172:175], v[188:191]\n\t"
      "v_mfma_f32_16x16x32_bf16 v[192:195], a[96:99], a[160:163], v[192:195]\n\t"
      "ds_read_b64_tr_b16 a[42:43], v23 offset:18144\n\t"
      "v_mfma_f32_16x16x32_bf16 v[196:199], a[96:99], a[164:167], v[196:199]\n\t"
      "ds_read_b64_tr_b16 a[44:45], v23 offset:14592\n\t"
      "v_mfma_f32_16x16x32_bf16 v[200:203], a[96:99], a[168:171], v[200:203]\n\t"
      "global_load_lds_dwordx4 v18, s[40:41]\n\t"
      "s_add_u32 m0, m0, 0x400\n\t"
      "v_mfma_f32_16x16x32_bf16 v[204:207], a[96:99], a[172:175], v[204:207]\n\t"
      "ds_read_b64_tr_b16 a[46:47], v23 offset:18176\n\t"
      "v_mfma_f32_16x16x32_bf16 v[208:211], a[100:103], a[160:163], v[208:211]\n\t"
      "global_store_dwordx4 v26, a[240:243], s[62:63] sc1\n\t"
      "v_mfma_f32_16x16x32_bf16 v[212:215], a[100:103], a[164:167], v[212:215]\n\t"
      "ds_read_b64_tr_b16 a[48:49], v23 offset:14624\n\t"
      "v_mfma_f32_16x16x32_bf16 v[216:219], a[100:103], a[168:171], v[216:219]\n\t"
      "v_mfma_f32_16x16x32_bf16 v[220:223], a[100:103], a[172:175], v[220:223]\n\t"
      "ds_read_b64_tr_b16 a[50:51], v23 offset:18208\n\t"
      "v_mfma_f32_16x16x32_bf16 v[224:227], a[104:107], a[160:163], v[224:227]\n\t"
      "global_load_dwordx4 a[156:159], v29, s[42:43] offset:0 sc1\n\t"
      "s_add_u32 s62, s62, 0x400\n\t"
      "s_addc_u32 s63, s63, 0\n\t"
      "v_mfma_f32_16x16x32_bf16 v[228:231], a[104:107], a[164:167], v[228:231]\n\t"
      "ds_read_b64_tr_b16 a[52:53], v23 offset:14656\n\t"
      "v_mfma_f32_16x16x32_bf16 v[232:235], a[104:107], a[168:171], v[232:235]\n\t"
      "v_mfma_f32_16x16x32_bf16 v[236:239], a[104:107], a[172:175], v[236:239]\n\t"
      "ds_read_b64_tr_b16 a[54:55], v23 offset:18240\n\t"
      "v_mfma_f32_16x16x32_bf16 v[240:243], a[108:111], a[160:163], v[240:243]\n\t"
      "v_mfma_f32_16x16x32_bf16 v[244:247], a[108:111], a[164:167], v[244:247]\n\t"
      "v_mfma_f32_16x16x32_bf16 v[248:251], a[108:111], a[168:171], v[248:251]\n\t"
      "global_load_lds_dwordx4 v19, s[40:41]\n\t"
      "s_add_u32 m0, m0, 0x400\n\t"
      "v_mfma_f32_16x16x32_bf16 v[252:255], a[108:111], a[172:175], v[252:255]\n\t"
      "s_sub_u32 s48, s48, 1\n\t"
      "s_cmp_lg_u32 s48, 0\n\t"
      "s_cbranch_scc1 G1LOOP_%=\n\t"
      "s_waitcnt vmcnt(52)\n\t"
      "s_waitcnt lgkmcnt(0)\n\t"
      "v_mfma_f32_16x16x32_bf16 v[32:35], a[0:3], a[176:179], v[32:35]\n\t"
      "v_mfma_f32_16x16x32_bf16 v[36:39], a[0:3], a[180:183], v[36:39]\n\t"
      "ds_read_b64_tr_b16 a[56:57], v23 offset:7168\n\t"
      "v_mfma_f32_16x16x32_bf16 v[40:43], a[0:3], a[184:187], v[40:43]\n\t"
      "ds_read_b64_tr_b16 a[58:59], v23 offset:10752\n\t"
      "v_mfma_f32_16x16x32_bf16 v[44:47], a[0:3], a[188:191], v[44:47]\n\t"
      "v_mfma_f32_16x16x32_bf16 v[48:51], a[4:7], a[176:179], v[48:51]\n\t"
      "ds_read_b64_tr_b16 a[60:61], v23 offset:7200\n\t"
      "v_mfma_f32_16x16x32_bf16 v[52:55], a[4:7], a[180:183], v[52:55]\n\t"
      "v_mfma_f32_16x16x32_bf16 v[56:59], a[4:7], a[184:187], v[56:59]\n\t"
      "ds_read_b64_tr_b16 a[62:63], v23 offset:10784\n\t"
      "global_load_dwordx4 a[160:163], v26, s[42:43] offset:1024 sc1\n\t"
      "v_mfma_f32_16x16x32_bf16 v[60:63], a[4:7], a[188:191], v[60:63]\n\t"
      "v_mfma_f32_16x16x32_bf16 v[64:67], a[8:11], a[176:179], v[64:67]\n\t"
      "ds_read_b64_tr_b16 a[64:65], v23 offset:7232\n\t"
      "v_mfma_f32_16x16x32_bf16 v[68:71], a[8:11], a[180:183], v[68:71]\n\t"
      "v_mfma_f32_16x16x32_bf16 v[72:75], a[8:11], a[184:187], v[72:75]\n\t"
      "ds_read_b64_tr_b16 a[66:67], v23 offset:10816\n\t"
      "global_load_lds_dwordx4 v20, s[40:41]\n\t"
      "s_add_u32 m0, m0, 0x400\n\t"
      "v_mfma_f32_16x16x32_bf16 v[76:79], a[8:11], a[188:191], v[76:79]\n\t"
      "v_mfma_f32_16x16x32_bf16 v[80:83], a[12:15], a[176:179], v[80:83]\n\t"
      "ds_read_b64_tr_b16 a[68:69], v23 offset:7264\n\t"
      "v_mfma_f32_16x16x32_bf16 v[84:87], a[12:15], a[180:183], v[84:87]\n\t"
      "v_mfma_f32_16x16x32_bf16 v[88:91], a[12:15], a[184:187], v[88:91]\n\t"
      "ds_read_b64_tr_b16 a[70:71], v23 offset:10848\n\t"
      "v_mfma_f32_16x16x32_bf16 v[92:95], a[12:15], a[188:191], v[92:95]\n\t"
      "ds_read_b64_tr_b16 a[72:73], v23 offset:7296\n\t"
      "v_mfma_f32_16x16x32_bf16 v[96:99], a[16:19], a[176:179], v[96:99]\n\t"
      "v_mfma_f32_16x16x32_bf16 v[100:103], a[16:19], a[180:183], v[100:103]\n\t"
      "ds_read_b64_tr_b16 a[74:75], v23 offset:10880\n\t"
      "v_mfma_f32_16x16x32_bf16 v[104:107], a[16:19], a[184:187], v[104:107]\n\t"
      "v_mfma_f32_16x16x32_bf16 v[108:111], a[16:19], a[188:191], v[108:111]\n\t"
      "ds_read_b64_tr_b16 a[76:77], v23 offset:7328\n\t"
      "v_mfma_f32_16x16x32_bf16 v[112:115], a[20:23], a[176:179], v[112:115]\n\t"
      "global_load_dwordx4 a[164:167], v27, s[42:43] offset:1024 sc1\n\t"
      "v_mfma_f32_16x16x32_bf16 v[116:119], a[20:23], a[180:183], v[116:119]\n\t"
      "ds_read_b64_tr_b16 a[78:79], v23 offset:10912\n\t"
      "v_mfma_f32_16x16x32_bf16 v[120:123], a[20:23], a[184:187], v[120:123]\n\t"
      "v_mfma_f32_16x16x32_bf16 v[124:127], a[20:23], a[188:191], v[124:127]\n\t"
      "ds_read_b64_tr_b16 a[80:81], v23 offset:7360\n\t"
      "v_mfma_f32_16x16x32_bf16 v[128:131], a[24:27], a[176:179], v[128:131]\n\t"
      "v_mfma_f32_16x16x32_bf16 v[132:135], a[24:27], a[180:183], v[132:135]\n\t"
      "ds_read_b64_tr_b16 a[82:83], v23 offset:10944\n\t"
      "v_mfma_f32_16x16x32_bf16 v[136:139], a[24:27], a[184:187], v[136:139]\n\t"
      "global_load_lds_dwordx4 v21, s[40:41]\n\t"
      "s_add_u32 m0, m0, 0x400\n\t"
      "v_mfma_f32_16x16x32_bf16 v[140:143], a[24:27], a[188:191], v[140:143]\n\t"
      "ds_read_b64_tr_b16 a[84:85], v23 offset:21632\n\t"
      "v_mfma_f32_16x16x32_bf16 v[144:147], a[28:31], a[176:179], v[144:147]\n\t"
      "ds_read_b64_tr_b16 a[86:87], v23 offset:25216\n\t"
      "v_mfma_f32_16x16x32_bf16 v[148:151], a[28:31], a[180:183], v[148:151]\n\t"
      "v_mfma_f32_16x16x32_bf16 v[152:155], a[28:31], a[184:187], v[152:155]\n\t"
      "ds_read_b64_tr_b16 a[88:89], v23 offset:21664\n\t"
      "v_mfma_f32_16x16x32_bf16 v[156:159], a[28:31], a[188:191], v[156:159]\n\t"
      "v_mfma_f32_16x16x32_bf16 v[160:163], a[32:35], a[176:179], v[160:163]\n\t"
      "ds_read_b64_tr_b16 a[90:91], v23 offset:25248\n\t"
      "v_mfma_f32_16x16x32_bf16 v[164:167], a[32:35], a[180:183], v[164:167]\n\t"
      "v_mfma_f32_16x16x32_bf16 v[168:171], a[32:35], a[184:187], v[168:171]\n\t"
      "ds_read_b64_tr_b16 a[92:93], v23 offset:21696\n\t"
      "global_load_dwordx4 a[168:171], v28, s[42:43] offset:1024 sc1\n\t"
      "v_mfma_f32_16x16x32_bf16 v[172:175], a[32:35], a[188:191], v[172:175]\n\t"
      "v_mfma_f32_16x16x32_bf16 v[176:179], a[36:39], a[176:179], v[176:179]\n\t"
      "ds_read_b64_tr_b16 a[94:95], v23 offset:25280\n\t"
      "v_mfma_f32_16x16x32_bf16 v[180:183], a[36:39], a[180:183], v[180:183]\n\t"
      "v_mfma_f32_16x16x32_bf16 v[184:187], a[36:39], a[184:187], v[184:187]\n\t"
      "ds_read_b64_tr_b16 a[96:97], v23 offset:21728\n\t"
      "v_mfma_f32_16x16x32_bf16 v[188:191], a[36:39], a[188:191], v[188:191]\n\t"
      "v_mfma_f32_16x16x32_bf16 v[192:195], a[40:43], a[176:179], v[192:195]\n\t"
      "ds_read_b64_tr_b16 a[98:99], v23 offset:25312\n\t"
      "v_mfma_f32_16x16x32_bf16 v[196:199], a[40:43], a[180:183], v[196:199]\n\t"
      "ds_read_b64_tr_b16 a[100:101], v23 offset:21760\n\t"
      "v_mfma_f32_16x16x32_bf16 v[200:203], a[40:43], a[184:187], v[200:203]\n\t"
      "global_load_lds_dwordx4 v22, s[40:41]\n\t"
      "s_add_u32 s47, s47, 28928\n\t"
      "v_mfma_f32_16x16x32_bf16 v[204:207], a[40:43], a[188:191], v[204:207]\n\t"
      "ds_read_b64_tr_b16 a[102:103], v23 offset:25344\n\t"
      "s_cmp_eq_u32 s47, 115712\n\t"
      "s_cselect_b32 s47, 0, s47\n\t"
      "v_mfma_f32_16x16x32_bf16 v[208:211], a[44:47], a[176:179], v[208:211]\n\t"
      "v_add_u32 v24, s47, v25\n\t"
      "v_mfma_f32_16x16x32_bf16 v[212:215], a[44:47], a[180:183], v[212:215]\n\t"
      "ds_read_b64_tr_b16 a[104:105], v23 offset:21792\n\t"
      "v_mfma_f32_16x16x32_bf16 v[216:219], a[44:47], a[184:187], v[216:219]\n\t"
      "s_add_u32 s40, s40, 0x70000\n\t"
      "s_addc_u32 s41, s41, 0\n\t"
      "v_mfma_f32_16x16x32_bf16 v[220:223], a[44:47], a[188:191], v[220:223]\n\t"
      "ds_read_b64_tr_b16 a[106:107], v23 offset:25376\n\t"
      "v_mfma_f32_16x16x32_bf16 v[224:227], a[48:51], a[176:179], v[224:227]\n\t"
      "global_load_dwordx4 a[172:175], v29, s[42:43] offset:1024 sc1\n\t"
      "v_mfma_f32_16x16x32_bf16 v[228:231], a[48:51], a[180:183], v[228:231]\n\t"
      "ds_read_b64_tr_b16 a[108:109], v23 offset:21824\n\t"
      "v_mfma_f32_16x16x32_bf16 v[232:235], a[48:51], a[184:187], v[232:235]\n\t"
      "s_add_u32 s46, s46, 28928\n\t"
      "v_mfma_f32_16x16x32_bf16 v[236:239], a[48:51], a[188:191], v[236:239]\n\t"
      "ds_read_b64_tr_b16 a[110:111], v23 offset:25408\n\t"
      "v_mfma_f32_16x16x32_bf16 v[240:243], a[52:55], a[176:179], v[240:243]\n\t"
      "buffer_load_dword v30, v14, %[wsrd], s53 offen\n\t"
      "v_mfma_f32_16x16x32_bf16 v[244:247], a[52:55], a[180:183], v[244:247]\n\t"
      "s_cmp_eq_u32 s46, s50\n\t"
      "s_cselect_b32 s46, s49, s46\n\t"
      "v_mfma_f32_16x16x32_bf16 v[248:251], a[52:55], a[184:187], v[248:251]\n\t"
      "s_mov_b32 m0, s46\n\t"
      "v_mfma_f32_16x16x32_bf16 v[252:255], a[52:55], a[188:191], v[252:255]\n\t"
      "s_add_u32 s42, s42, 0x800\n\t"
      "s_addc_u32 s43, s43, 0\n\t"
      "s_waitcnt vmcnt(52)\n\t"
      "s_waitcnt lgkmcnt(0)\n\t"
      "v_mfma_f32_16x16x32_bf16 v[32:35], a[56:59], a[192:195], v[32:35]\n\t"
      "s_add_u32 s53, s53, 0x70000\n\t"
      "v_mfma_f32_16x16x32_bf16 v[36:39], a[56:59], a[196:199], v[36:39]\n\t"
      "v_mfma_f32_16x16x32_bf16 v[40:43], a[56:59], a[200:203], v[40:43]\n\t"
      "v_mfma_f32_16x16x32_bf16 v[44:47], a[56:59], a[204:207], v[44:47]\n\t"
      "v_mfma_f32_16x16x32_bf16 v[48:51], a[60:63], a[192:195], v[48:51]\n\t"
      "v_mfma_f32_16x16x32_bf16 v[52:55], a[60:63], a[196:199], v[52:55]\n\t"
      "v_mfma_f32_16x16x32_bf16 v[56:59], a[60:63], a[200:203], v[56:59]\n\t"
      "global_load_dwordx4 a[176:179], v26, s[42:43] offset:0 sc1\n\t"
      "v_mfma_f32_16x16x32_bf16 v[60:63], a[60:63], a[204:207], v[60:63]\n\t"
      "v_mfma_f32_16x16x32_bf16 v[64:67], a[64:67], a[192:195], v[64:67]\n\t"
      "s_waitcnt lgkmcnt(0)\n\t"
      "s_waitcnt vmcnt(37)\n\t"
      "s_barrier\n\t"
      "v_mfma_f32_16x16x32_bf16 v[68:71], a[64:67], a[196:199], v[68:71]\n\t"
      "ds_read_b64_tr_b16 a[0:1], v24 offset:0\n\t"
      "v_mfma_f32_16x16x32_bf16 v[72:75], a[64:67], a[200:203], v[72:75]\n\t"
      "ds_read_b64_tr_b16 a[2:3], v24 offset:3584\n\t"
      "v_mfma_f32_16x16x32_bf16 v[76:79], a[64:67], a[204:207], v[76:79]\n\t"
      "ds_read_b64_tr_b16 a[4:5], v24 offset:32\n\t"
      "v_mfma_f32_16x16x32_bf16 v[80:83], a[68:71], a[192:195], v[80:83]\n\t"
      "ds_read_b64_tr_b16 a[6:7], v24 offset:3616\n\t"
      "v_mfma_f32_16x16x32_bf16 v[84:87], a[68:71], a[196:199], v[84:87]\n\t"
      "ds_read_b64_tr_b16 a[8:9], v24 offset:64\n\t"
      "v_mfma_f32_16x16x32_bf16 v[88:91], a[68:71], a[200:203], v[88:91]\n\t"
      "ds_read_b64_tr_b16 a[10:11], v24 offset:3648\n\t"
      "v_mfma_f32_16x16x32_bf16 v[92:95], a[68:71], a[204:207], v[92:95]\n\t"
      "ds_read_b64_tr_b16 a[12:13], v24 offset:96\n\t"
      "v_mfma_f32_16x16x32_bf16 v[96:99], a[72:75], a[192:195], v[96:99]\n\t"
      "ds_read_b64_tr_b16 a[14:15], v24 offset:3680\n\t"
      "v_mfma_f32_16x16x32_bf16 v[100:103], a[72:75], a[196:199], v[100:103]\n\t"
      "ds_read_b64_tr_b16 a[16:17], v24 offset:128\n\t"
      "v_mfma_f32_16x16x32_bf16 v[104:107], a[72:75], a[200:203], v[104:107]\n\t"
      "ds_read_b64_tr_b16 a[18:19], v24 offset:3712\n\t"
      "global_load_lds_dwordx4 v16, s[40:41]\n\t"
      "s_add_u32 m0, m0, 0x400\n\t"
      "v_mfma_f32_16x16x32_bf16 v[108:111], a[72:75], a[204:207], v[108:111]\n\t"
      "ds_read_b64_tr_b16 a[20:21], v24 offset:160\n\t"
      "v_mfma_f32_16x16x32_bf16 v[112:115], a[76:79], a[192:195], v[112:115]\n\t"
      "global_load_dwordx4 a[180:183], v27, s[42:43] offset:0 sc1\n\t"
      "v_mfma_f32_16x16x32_bf16 v[116:119], a[76:79], a[196:199], v[116:119]\n\t"
      "ds_read_b64_tr_b16 a[22:23], v24 offset:3744\n\t"
      "v_mfma_f32_16x16x32_bf16 v[120:123], a[76:79], a[200:203], v[120:123]\n\t"
      "v_mfma_f32_16x16x32_bf16 v[124:127], a[76:79], a[204:207], v[124:127]\n\t"
      "ds_read_b64_tr_b16 a[24:25], v24 offset:192\n\t"
      "v_mfma_f32_16x16x32_bf16 v[128:131], a[80:83], a[192:195], v[128:131]\n\t"
      "v_mfma_f32_16x16x32_bf16 v[132:135], a[80:83], a[196:199], v[132:135]\n\t"
      "ds_read_b64_tr_b16 a[26:27], v24 offset:3776\n\t"
      "v_mfma_f32_16x16x32_bf16 v[136:139], a[80:83], a[200:203], v[136:139]\n\t"
      "v_mfma_f32_16x16x32_bf16 v[140:143], a[80:83], a[204:207], v[140:143]\n\t"
      "ds_read_b64_tr_b16 a[28:29], v24 offset:14464\n\t"
      "v_mfma_f32_16x16x32_bf16 v[144:147], a[84:87], a[192:195], v[144:147]\n\t"
      "ds_read_b64_tr_b16 a[30:31], v24 offset:18048\n\t"
      "v_mfma_f32_16x16x32_bf16 v[148:151], a[84:87], a[196:199], v[148:151]\n\t"
      "v_mfma_f32_16x16x32_bf16 v[152:155], a[84:87], a[200:203], v[152:155]\n\t"
      "ds_read_b64_tr_b16 a[32:33], v24 offset:14496\n\t"
      "global_load_lds_dwordx4 v17, s[40:41]\n\t"
      "s_add_u32 m0, m0, 0x400\n\t"
      "v_mfma_f32_16x16x32_bf16 v[156:159], a[84:87], a[204:207], v[156:159]\n\t"
      "v_mfma_f32_16x16x32_bf16 v[160:163], a[88:91], a[192:195], v[160:163]\n\t"
      "ds_read_b64_tr_b16 a[34:35], v24 offset:18080\n\t"
      "v_mfma_f32_16x16x32_bf16 v[164:167], a[88:91], a[196:199], v[164:167]\n\t"
      "v_mfma_f32_16x16x32_bf16 v[168:171], a[88:91], a[200:203], v[168:171]\n\t"
      "ds_read_b64_tr_b16 a[36:37], v24 offset:14528\n\t"
      "global_load_dwordx4 a[184:187], v28, s[42:43] offset:0 sc1\n\t"
      "v_mfma_f32_16x16x32_bf16 v[172:175], a[88:91], a[204:207], v[172:175]\n\t"
      "v_mfma_f32_16x16x32_bf16 v[176:179], a[92:95], a[192:195], v[176:179]\n\t"
      "ds_read_b64_tr_b16 a[38:39], v24 offset:18112\n\t"
      "v_mfma_f32_16x16x32_bf16 v[180:183], a[92:95], a[196:199], v[180:183]\n\t"
      "v_mfma_f32_16x16x32_bf16 v[184:187], a[92:95], a[200:203], v[184:187]\n\t"
      "ds_read_b64_tr_b16 a[40:41], v24 offset:14560\n\t"
      "v_mfma_f32_16x16x32_bf16 v[188:191], a[92:95], a[204:207], v[188:191]\n\t"
      "v_mfma_f32_16x16x32_bf16 v[192:195], a[96:99], a[192:195], v[192:195]\n\t"
      "ds_read_b64_tr_b16 a[42:43], v24 offset:18144\n\t"
      "v_mfma_f32_16x16x32_bf16 v[196:199], a[96:99], a[196:199], v[196:199]\n\t"
      "ds_read_b64_tr_b16 a[44:45], v24 offset:14592\n\t"
      "v_mfma_f32_16x16x32_bf16 v[200:203], a[96:99], a[200:203], v[200:203]\n\t"
      "global_load_lds_dwordx4 v18, s[40:41]\n\t"
      "s_add_u32 m0, m0, 0x400\n\t"
      "v_mfma_f32_16x16x32_bf16 v[204:207], a[96:99], a[204:207], v[204:207]\n\t"
      "ds_read_b64_tr_b16 a[46:47], v24 offset:18176\n\t"
      "v_mfma_f32_16x16x32_bf16 v[208:211], a[100:103], a[192:195], v[208:211]\n\t"
      "v_mfma_f32_16x16x32_bf16 v[212:215], a[100:103], a[196:199], v[212:215]\n\t"
      "ds_read_b64_tr_b16 a[48:49], v24 offset:14624\n\t"
      "v_mfma_f32_16x16x32_bf16 v[216:219], a[100:103], a[200:203], v[216:219]\n\t"
      "v_mfma_f32_16x16x32_bf16 v[220:223], a[100:103], a[204:207], v[220:223]\n\t"
      "ds_read_b64_tr_b16 a[50:51], v24 offset:18208\n\t"
      "v_mfma_f32_16x16x32_bf16 v[224:227], a[104:107], a[192:195], v[224:227]\n\t"
      "global_load_dwordx4 a[188:191], v29, s[42:43] offset:0 sc1\n\t"
      "v_mfma_f32_16x16x32_bf16 v[228:231], a[104:107], a[196:199], v[228:231]\n\t"
      "ds_read_b64_tr_b16 a[52:53], v24 offset:14656\n\t"
      "v_mfma_f32_16x16x32_bf16 v[232:235], a[104:107], a[200:203], v[232:235]\n\t"
      "v_mfma_f32_16x16x32_bf16 v[236:239], a[104:107], a[204:207], v[236:239]\n\t"
      "ds_read_b64_tr_b16 a[54:55], v24 offset:18240\n\t"
      "v_mfma_f32_16x16x32_bf16 v[240:243], a[108:111], a[192:195], v[240:243]\n\t"
      "v_mfma_f32_16x16x32_bf16 v[244:247], a[108:111], a[196:199], v[244:247]\n\t"
      "v_mfma_f32_16x16x32_bf16 v[248:251], a[108:111], a[200:203], v[248:251]\n\t"
      "global_load_lds_dwordx4 v19, s[40:41]\n\t"
      "s_add_u32 m0, m0, 0x400\n\t"
      "v_mfma_f32_16x16x32_bf16 v[252:255], a[108:111], a[204:207], v[252:255]\n\t"
      "s_waitcnt vmcnt(51)\n\t"
      "s_waitcnt lgkmcnt(0)\n\t"
      "v_mfma_f32_16x16x32_bf16 v[32:35], a[0:3], a[208:211], v[32:35]\n\t"
      "v_mfma_f32_16x16x32_bf16 v[36:39], a[0:3], a[212:215], v[36:39]\n\t"
      "ds_read_b64_tr_b16 a[56:57], v24 offset:7168\n\t"
      "v_mfma_f32_16x16x32_bf16 v[40:43], a[0:3], a[216:219], v[40:43]\n\t"
      "ds_read_b64_tr_b16 a[58:59], v24 offset:10752\n\t"
      "v_mfma_f32_16x16x32_bf16 v[44:47], a[0:3], a[220:223], v[44:47]\n\t"
      "v_mfma_f32_16x16x32_bf16 v[48:51], a[4:7], a[208:211], v[48:51]\n\t"
      "ds_read_b64_tr_b16 a[60:61], v24 offset:7200\n\t"
      "v_mfma_f32_16x16x32_bf16 v[52:55], a[4:7], a[212:215], v[52:55]\n\t"
      "v_mfma_f32_16x16x32_bf16 v[56:59], a[4:7], a[216:219], v[56:59]\n\t"
      "ds_read_b64_tr_b16 a[62:63], v24 offset:10784\n\t"
      "global_load_dwordx4 a[192:195], v26, s[42:43] offset:1024 sc1\n\t"
      "v_mfma_f32_16x16x32_bf16 v[60:63], a[4:7], a[220:223], v[60:63]\n\t"
      "v_mfma_f32_16x16x32_bf16 v[64:67], a[8:11], a[208:211], v[64:67]\n\t"
      "ds_read_b64_tr_b16 a[64:65], v24 offset:7232\n\t"
      "v_mfma_f32_16x16x32_bf16 v[68:71], a[8:11], a[212:215], v[68:71]\n\t"
      "v_mfma_f32_16x16x32_bf16 v[72:75], a[8:11], a[216:219], v[72:75]\n\t"
      "ds_read_b64_tr_b16 a[66:67], v24 offset:10816\n\t"
      "global_load_lds_dwordx4 v20, s[40:41]\n\t"
      "s_add_u32 m0, m0, 0x400\n\t"
      "v_mfma_f32_16x16x32_bf16 v[76:79], a[8:11], a[220:223], v[76:79]\n\t"
      "v_mfma_f32_16x16x32_bf16 v[80:83], a[12:15], a[208:211], v[80:83]\n\t"
      "ds_read_b64_tr_b16 a[68:69], v24 offset:7264\n\t"
      "v_mfma_f32_16x16x32_bf16 v[84:87], a[12:15], a[212:215], v[84:87]\n\t"
      "v_mfma_f32_16x16x32_bf16 v[88:91], a[12:15], a[216:219], v[88:91]\n\t"
      "ds_read_b64_tr_b16 a[70:71], v24 offset:10848\n\t"
      "v_mfma_f32_16x16x32_bf16 v[92:95], a[12:15], a[220:223], v[92:95]\n\t"
      "ds_read_b64_tr_b16 a[72:73], v24 offset:7296\n\t"
      "v_mfma_f32_16x16x32_bf16 v[96:99], a[16:19], a[208:211], v[96:99]\n\t"
      "v_mfma_f32_16x16x32_bf16 v[100:103], a[16:19], a[212:215], v[100:103]\n\t"
      "ds_read_b64_tr_b16 a[74:75], v24 offset:10880\n\t"
      "v_mfma_f32_16x16x32_bf16 v[104:107], a[16:19], a[216:219], v[104:107]\n\t"
      "v_mfma_f32_16x16x32_bf16 v[108:111], a[16:19], a[220:223], v[108:111]\n\t"
      "ds_read_b64_tr_b16 a[76:77], v24 offset:7328\n\t"
      "v_mfma_f32_16x16x32_bf16 v[112:115], a[20:23], a[208:211], v[112:115]\n\t"
      "v_and_b32 v15, 15, %[lane]\n\t"
      "v_lshlrev_b32 v15, 3, v15\n\t"
      "global_load_dwordx2 v[12:13], v15, %[fur] sc1\n\t"
      "global_load_dwordx4 a[196:199], v27, s[42:43] offset:1024 sc1\n\t"
      "v_mfma_f32_16x16x32_bf16 v[116:119], a[20:23], a[212:215], v[116:119]\n\t"
      "ds_read_b64_tr_b16 a[78:79], v24 offset:10912\n\t"
      "v_mfma_f32_16x16x32_bf16 v[120:123], a[20:23], a[216:219], v[120:123]\n\t"
      "v_mfma_f32_16x16x32_bf16 v[124:127], a[20:23], a[220:223], v[124:127]\n\t"
      "ds_read_b64_tr_b16 a[80:81], v24 offset:7360\n\t"
      "v_mfma_f32_16x16x32_bf16 v[128:131], a[24:27], a[208:211], v[128:131]\n\t"
      "v_mfma_f32_16x16x32_bf16 v[132:135], a[24:27], a[212:215], v[132:135]\n\t"
      "ds_read_b64_tr_b16 a[82:83], v24 offset:10944\n\t"
      "v_mfma_f32_16x16x32_bf16 v[136:139], a[24:27], a[216:219], v[136:139]\n\t"
      "global_load_lds_dwordx4 v21, s[40:41]\n\t"
      "s_add_u32 m0, m0, 0x400\n\t"
      "v_mfma_f32_16x16x32_bf16 v[140:143], a[24:27], a[220:223], v[140:143]\n\t"
      "ds_read_b64_tr_b16 a[84:85], v24 offset:21632\n\t"
      "v_mfma_f32_16x16x32_bf16 v[144:147], a[28:31], a[208:211], v[144:147]\n\t"
      "ds_read_b64_tr_b16 a[86:87], v24 offset:25216\n\t"
      "v_mfma_f32_16x16x32_bf16 v[148:151], a[28:31], a[212:215], v[148:151]\n\t"
      "v_mfma_f32_16x16x32_bf16 v[152:155], a[28:31], a[216:219], v[152:155]\n\t"
      "ds_read_b64_tr_b16 a[88:89], v24 offset:21664\n\t"
      "v_mfma_f32_16x16x32_bf16 v[156:159], a[28:31], a[220:223], v[156:159]\n\t"
      "v_mfma_f32_16x16x32_bf16 v[160:163], a[32:35], a[208:211], v[160:163]\n\t"
      "ds_read_b64_tr_b16 a[90:91], v24 offset:25248\n\t"
      "v_mfma_f32_16x16x32_bf16 v[164:167], a[32:35], a[212:215], v[164:167]\n\t"
      "v_mfma_f32_16x16x32_bf16 v[168:171], a[32:35], a[216:219], v[168:171]\n\t"
      "ds_read_b64_tr_b16 a[92:93], v24 offset:21696\n\t"
      "global_load_dwordx4 a[200:203], v28, s[42:43] offset:1024 sc1\n\t"
      "v_mfma_f32_16x16x32_bf16 v[172:175], a[32:35], a[220:223], v[172:175]\n\t"
      "v_mfma_f32_16x16x32_bf16 v[176:179], a[36:39], a[208:211], v[176:179]\n\t"
      "ds_read_b64_tr_b16 a[94:95], v24 offset:25280\n\t"
      "v_mfma_f32_16x16x32_bf16 v[180:183], a[36:39], a[212:215], v[180:183]\n\t"
      "v_mfma_f32_16x16x32_bf16 v[184:187], a[36:39], a[216:219], v[184:187]\n\t"
      "ds_read_b64_tr_b16 a[96:97], v24 offset:21728\n\t"
      "v_mfma_f32_16x16x32_bf16 v[188:191], a[36:39], a[220:223], v[188:191]\n\t"
      "v_mfma_f32_16x16x32_bf16 v[192:195], a[40:43], a[208:211], v[192:195]\n\t"
      "ds_read_b64_tr_b16 a[98:99], v24 offset:25312\n\t"
      "v_mfma_f32_16x16x32_bf16 v[196:199], a[40:43], a[212:215], v[196:199]\n\t"
      "ds_read_b64_tr_b16 a[100:101], v24 offset:21760\n\t"
      "v_mfma_f32_16x16x32_bf16 v[200:203], a[40:43], a[216:219], v[200:203]\n\t"
      "global_load_lds_dwordx4 v22, s[40:41]\n\t"
      "s_add_u32 s47, s47, 28928\n\t"
      "v_mfma_f32_16x16x32_bf16 v[204:207], a[40:43], a[220:223], v[204:207]\n\t"
      "ds_read_b64_tr_b16 a[102:103], v24 offset:25344\n\t"
      "s_cmp_eq_u32 s47, 115712\n\t"
      "s_cselect_b32 s47, 0, s47\n\t"
      "v_mfma_f32_16x16x32_bf16 v[208:211], a[44:47], a[208:211], v[208:211]\n\t"
      "v_add_u32 v23, s47, v25\n\t"
      "v_mfma_f32_16x16x32_bf16 v[212:215], a[44:47], a[212:215], v[212:215]\n\t"
      "ds_read_b64_tr_b16 a[104:105], v24 offset:21792\n\t"
      "v_mfma_f32_16x16x32_bf16 v[216:219], a[44:47], a[216:219], v[216:219]\n\t"
      "s_add_u32 s40, s40, 0x70000\n\t"
      "s_addc_u32 s41, s41, 0\n\t"
      "v_mfma_f32_16x16x32_bf16 v[220:223], a[44:47], a[220:223], v[220:223]\n\t"
      "ds_read_b64_tr_b16 a[106:107], v24 offset:25376\n\t"
      "v_mfma_f32_16x16x32_bf16 v[224:227], a[48:51], a[208:211], v[224:227]\n\t"
      "global_load_dwordx4 a[204:207], v29, s[42:43] offset:1024 sc1\n\t"
      "v_mfma_f32_16x16x32_bf16 v[228:231], a[48:51], a[212:215], v[228:231]\n\t"
      "ds_read_b64_tr_b16 a[108:109], v24 offset:21824\n\t"
      "v_mfma_f32_16x16x32_bf16 v[232:235], a[48:51], a[216:219], v[232:235]\n\t"
      "s_add_u32 s46, s46, 28928\n\t"
      "v_mfma_f32_16x16x32_bf16 v[236:239], a[48:51], a[220:223], v[236:239]\n\t"
      "ds_read_b64_tr_b16 a[110:111], v24 offset:25408\n\t"
      "v_mfma_f32_16x16x32_bf16 v[240:243], a[52:55], a[208:211], v[240:243]\n\t"
      "s_cmp_eq_u32 s46, s50\n\t"
      "s_cselect_b32 s46, s49, s46\n\t"
      "v_mfma_f32_16x16x32_bf16 v[244:247], a[52:55], a[212:215], v[244:247]\n\t"
      "s_mov_b32 m0, s46\n\t"
      "v_mfma_f32_16x16x32_bf16 v[248:251], a[52:55], a[216:219], v[248:251]\n\t"
      "s_add_u32 s42, s42, 0x800\n\t"
      "s_addc_u32 s43, s43, 0\n\t"
      "v_mfma_f32_16x16x32_bf16 v[252:255], a[52:55], a[220:223], v[252:255]\n\t"
      "s_waitcnt vmcnt(51)\n\t"
      "s_waitcnt lgkmcnt(0)\n\t"
      "v_mfma_f32_16x16x32_bf16 v[32:35], a[56:59], a[224:227], v[32:35]\n\t"
      "v_mfma_f32_16x16x32_bf16 v[36:39], a[56:59], a[228:231], v[36:39]\n\t"
      "v_mfma_f32_16x16x32_bf16 v[40:43], a[56:59], a[232:235], v[40:43]\n\t"
      "v_mfma_f32_16x16x32_bf16 v[44:47], a[56:59], a[236:239], v[44:47]\n\t"
      "v_mfma_f32_16x16x32_bf16 v[48:51], a[60:63], a[224:227], v[48:51]\n\t"
      "v_mfma_f32_16x16x32_bf16 v[52:55], a[60:63], a[228:231], v[52:55]\n\t"
      "v_mfma_f32_16x16x32_bf16 v[56:59], a[60:63], a[232:235], v[56:59]\n\t"
      "global_load_dwordx4 a[208:211], v26, s[42:43] offset:0 sc1\n\t"
      "v_mfma_f32_16x16x32_bf16 v[60:63], a[60:63], a[236:239], v[60:63]\n\t"
      "v_mfma_f32_16x16x32_bf16 v[64:67], a[64:67], a[224:227], v[64:67]\n\t"
      "s_waitcnt lgkmcnt(0)\n\t"
      "s_waitcnt vmcnt(36)\n\t"
      "s_barrier\n\t"
      "v_mfma_f32_16x16x32_bf16 v[68:71], a[64:67], a[228:231], v[68:71]\n\t"
      "ds_read_b64_tr_b16 a[0:1], v23 offset:0\n\t"
      "v_mfma_f32_16x16x32_bf16 v[72:75], a[64:67], a[232:235], v[72:75]\n\t"
      "ds_read_b64_tr_b16 a[2:3], v23 offset:3584\n\t"
      "global_load_lds_dwordx4 v16, s[40:41]\n\t"
      "s_add_u32 m0, m0, 0x400\n\t"
      "v_mfma_f32_16x16x32_bf16 v[76:79], a[64:67], a[236:239], v[76:79]\n\t"
      "ds_read_b64_tr_b16 a[4:5], v23 offset:32\n\t"
      "v_mfma_f32_16x16x32_bf16 v[80:83], a[68:71], a[224:227], v[80:83]\n\t"
      "ds_read_b64_tr_b16 a[6:7], v23 offset:3616\n\t"
      "v_mfma_f32_16x16x32_bf16 v[84:87], a[68:71], a[228:231], v[84:87]\n\t"
      "ds_read_b64_tr_b16 a[8:9], v23 offset:64\n\t"
      "v_mfma_f32_16x16x32_bf16 v[88:91], a[68:71], a[232:235], v[88:91]\n\t"
      "ds_read_b64_tr_b16 a[10:11], v23 offset:3648\n\t"
      "v_mfma_f32_16x16x32_bf16 v[92:95], a[68:71], a[236:239], v[92:95]\n\t"
      "ds_read_b64_tr_b16 a[12:13], v23 offset:96\n\t"
      "v_mfma_f32_16x16x32_bf16 v[96:99], a[72:75], a[224:227], v[96:99]\n\t"
      "ds_read_b64_tr_b16 a[14:15], v23 offset:3680\n\t"
      "global_load_lds_dwordx4 v17, s[40:41]\n\t"
      "s_add_u32 m0, m0, 0x400\n\t"
      "v_mfma_f32_16x16x32_bf16 v[100:103], a[72:75], a[228:231], v[100:103]\n\t"
      "ds_read_b64_tr_b16 a[16:17], v23 offset:128\n\t"
      "v_mfma_f32_16x16x32_bf16 v[104:107], a[72:75], a[232:235], v[104:107]\n\t"
      "ds_read_b64_tr_b16 a[18:19], v23 offset:3712\n\t"
      "v_mfma_f32_16x16x32_bf16 v[108:111], a[72:75], a[236:239], v[108:111]\n\t"
      "ds_read_b64_tr_b16 a[20:21], v23 offset:160\n\t"
      "v_mfma_f32_16x16x32_bf16 v[112:115], a[76:79], a[224:227], v[112:115]\n\t"
      "global_load_dwordx4 a[212:215], v27, s[42:43] offset:0 sc1\n\t"
      "v_mfma_f32_16x16x32_bf16 v[116:119], a[76:79], a[228:231], v[116:119]\n\t"
      "ds_read_b64_tr_b16 a[22:23], v23 offset:3744\n\t"
      "v_mfma_f32_16x16x32_bf16 v[120:123], a[76:79], a[232:235], v[120:123]\n\t"
      "global_load_lds_dwordx4 v18, s[40:41]\n\t"
      "s_add_u32 m0, m0, 0x400\n\t"
      "v_mfma_f32_16x16x32_bf16 v[124:127], a[76:79], a[236:239], v[124:127]\n\t"
      "ds_read_b64_tr_b16 a[24:25], v23 offset:192\n\t"
      "v_mfma_f32_16x16x32_bf16 v[128:131], a[80:83], a[224:227], v[128:131]\n\t"
      "v_mfma_f32_16x16x32_bf16 v[132:135], a[80:83], a[228:231], v[132:135]\n\t"
      "ds_read_b64_tr_b16 a[26:27], v23 offset:3776\n\t"
      "v_mfma_f32_16x16x32_bf16 v[136:139], a[80:83], a[232:235], v[136:139]\n\t"
      "v_mfma_f32_16x16x32_bf16 v[140:143], a[80:83], a[236:239], v[140:143]\n\t"
      "ds_read_b64_tr_b16 a[28:29], v23 offset:14464\n\t"
      "v_mfma_f32_16x16x32_bf16 v[144:147], a[84:87], a[224:227], v[144:147]\n\t"
      "ds_read_b64_tr_b16 a[30:31], v23 offset:18048\n\t"
      "global_load_lds_dwordx4 v19, s[40:41]\n\t"
      "s_add_u32 m0, m0, 0x400\n\t"
      "v_mfma_f32_16x16x32_bf16 v[148:151], a[84:87], a[228:231], v[148:151]\n\t"
      "v_mfma_f32_16x16x32_bf16 v[152:155], a[84:87], a[232:235], v[152:155]\n\t"
      "ds_read_b64_tr_b16 a[32:33], v23 offset:14496\n\t"
      "v_mfma_f32_16x16x32_bf16 v[156:159], a[84:87], a[236:239], v[156:159]\n\t"
      "v_mfma_f32_16x16x32_bf16 v[160:163], a[88:91], a[224:227], v[160:163]\n\t"
      "ds_read_b64_tr_b16 a[34:35], v23 offset:18080\n\t"
      "v_mfma_f32_16x16x32_bf16 v[164:167], a[88:91], a[228:231], v[164:167]\n\t"
      "v_mfma_f32_16x16x32_bf16 v[168:171], a[88:91], a[232:235], v[168:171]\n\t"
      "ds_read_b64_tr_b16 a[36:37], v23 offset:14528\n\t"
      "global_load_lds_dwordx4 v20, s[40:41]\n\t"
      "global_load_dwordx4 a[216:219], v28, s[42:43] offset:0 sc1\n\t"
      "s_add_u32 m0, m0, 0x400\n\t"
      "v_mfma_f32_16x16x32_bf16 v[172:175], a[88:91], a[236:239], v[172:175]\n\t"
      "v_mfma_f32_16x16x32_bf16 v[176:179], a[92:95], a[224:227], v[176:179]\n\t"
      "ds_read_b64_tr_b16 a[38:39], v23 offset:18112\n\t"
      "v_mfma_f32_16x16x32_bf16 v[180:183], a[92:95], a[228:231], v[180:183]\n\t"
      "v_mfma_f32_16x16x32_bf16 v[184:187], a[92:95], a[232:235], v[184:187]\n\t"
      "ds_read_b64_tr_b16 a[40:41], v23 offset:14560\n\t"
      "v_mfma_f32_16x16x32_bf16 v[188:191], a[92:95], a[236:239], v[188:191]\n\t"
      "v_mfma_f32_16x16x32_bf16 v[192:195], a[96:99], a[224:227], v[192:195]\n\t"
      "ds_read_b64_tr_b16 a[42:43], v23 offset:18144\n\t"
      "global_load_lds_dwordx4 v21, s[40:41]\n\t"
      "s_add_u32 m0, m0, 0x400\n\t"
      "v_mfma_f32_16x16x32_bf16 v[196:199], a[96:99], a[228:231], v[196:199]\n\t"
      "ds_read_b64_tr_b16 a[44:45], v23 offset:14592\n\t"
      "v_mfma_f32_16x16x32_bf16 v[200:203], a[96:99], a[232:235], v[200:203]\n\t"
      "v_mfma_f32_16x16x32_bf16 v[204:207], a[96:99], a[236:239], v[204:207]\n\t"
      "ds_read_b64_tr_b16 a[46:47], v23 offset:18176\n\t"
      "v_mfma_f32_16x16x32_bf16 v[208:211], a[100:103], a[224:227], v[208:211]\n\t"
      "v_mfma_f32_16x16x32_bf16 v[212:215], a[100:103], a[228:231], v[212:215]\n\t"
      "ds_read_b64_tr_b16 a[48:49], v23 offset:14624\n\t"
      "v_mfma_f32_16x16x32_bf16 v[216:219], a[100:103], a[232:235], v[216:219]\n\t"
      "global_load_lds_dwordx4 v22, s[40:41]\n\t"
      "v_mfma_f32_16x16x32_bf16 v[220:223], a[100:103], a[236:239], v[220:223]\n\t"
      "ds_read_b64_tr_b16 a[50:51], v23 offset:18208\n\t"
      "v_mfma_f32_16x16x32_bf16 v[224:227], a[104:107], a[224:227], v[224:227]\n\t"
      "global_load_dwordx4 a[220:223], v29, s[42:43] offset:0 sc1\n\t"
      "v_mfma_f32_16x16x32_bf16 v[228:231], a[104:107], a[228:231], v[228:231]\n\t"
      "ds_read_b64_tr_b16 a[52:53], v23 offset:14656\n\t"
      "v_mfma_f32_16x16x32_bf16 v[232:235], a[104:107], a[232:235], v[232:235]\n\t"
      "s_add_u32 s40, s40, 0x70000\n\t"
      "s_addc_u32 s41, s41, 0\n\t"
      "v_mfma_f32_16x16x32_bf16 v[236:239], a[104:107], a[236:239], v[236:239]\n\t"
      "ds_read_b64_tr_b16 a[54:55], v23 offset:18240\n\t"
      "v_mfma_f32_16x16x32_bf16 v[240:243], a[108:111], a[224:227], v[240:243]\n\t"
      "s_add_u32 s46, s46, 28928\n\t"
      "v_mfma_f32_16x16x32_bf16 v[244:247], a[108:111], a[228:231], v[244:247]\n\t"
      "s_cmp_eq_u32 s46, s50\n\t"
      "s_cselect_b32 s46, s49, s46\n\t"
      "v_mfma_f32_16x16x32_bf16 v[248:251], a[108:111], a[232:235], v[248:251]\n\t"
      "s_mov_b32 m0, s46\n\t"
      "v_mfma_f32_16x16x32_bf16 v[252:255], a[108:111], a[236:239], v[252:255]\n\t"
      "s_waitcnt vmcnt(53)\n\t"
      "s_waitcnt lgkmcnt(0)\n\t"
      "v_mfma_f32_16x16x32_bf16 v[32:35], a[0:3], a[112:115], v[32:35]\n\t"
      "v_mfma_f32_16x16x32_bf16 v[36:39], a[0:3], a[116:119], v[36:39]\n\t"
      "ds_read_b64_tr_b16 a[56:57], v23 offset:7168\n\t"
      "v_mfma_f32_16x16x32_bf16 v[40:43], a[0:3], a[120:123], v[40:43]\n\t"
      "ds_read_b64_tr_b16 a[58:59], v23 offset:10752\n\t"
      "v_mfma_f32_16x16x32_bf16 v[44:47], a[0:3], a[124:127], v[44:47]\n\t"
      "v_mfma_f32_16x16x32_bf16 v[48:51], a[4:7], a[112:115], v[48:51]\n\t"
      "ds_read_b64_tr_b16 a[60:61], v23 offset:7200\n\t"
      "v_mfma_f32_16x16x32_bf16 v[52:55], a[4:7], a[116:119], v[52:55]\n\t"
      "v_mfma_f32_16x16x32_bf16 v[56:59], a[4:7], a[120:123], v[56:59]\n\t"
      "ds_read_b64_tr_b16 a[62:63], v23 offset:10784\n\t"
      "global_load_dwordx4 a[224:227], v26, s[42:43] offset:1024 sc1\n\t"
      "v_mfma_f32_16x16x32_bf16 v[60:63], a[4:7], a[124:127], v[60:63]\n\t"
      "v_mfma_f32_16x16x32_bf16 v[64:67], a[8:11], a[112:115], v[64:67]\n\t"
      "ds_read_b64_tr_b16 a[64:65], v23 offset:7232\n\t"
      "v_mfma_f32_16x16x32_bf16 v[68:71], a[8:11], a[116:119], v[68:71]\n\t"
      "v_mfma_f32_16x16x32_bf16 v[72:75], a[8:11], a[120:123], v[72:75]\n\t"
      "ds_read_b64_tr_b16 a[66:67], v23 offset:10816\n\t"
      "v_mfma_f32_16x16x32_bf16 v[76:79], a[8:11], a[124:127], v[76:79]\n\t"
      "v_mfma_f32_16x16x32_bf16 v[80:83], a[12:15], a[112:115], v[80:83]\n\t"
      "ds_read_b64_tr_b16 a[68:69], v23 offset:7264\n\t"
      "v_mfma_f32_16x16x32_bf16 v[84:87], a[12:15], a[116:119], v[84:87]\n\t"
      "v_mfma_f32_16x16x32_bf16 v[88:91], a[12:15], a[120:123], v[88:91]\n\t"
      "ds_read_b64_tr_b16 a[70:71], v23 offset:10848\n\t"
      "v_mfma_f32_16x16x32_bf16 v[92:95], a[12:15], a[124:127], v[92:95]\n\t"
      "ds_read_b64_tr_b16 a[72:73], v23 offset:7296\n\t"
      "v_mfma_f32_16x16x32_bf16 v[96:99], a[16:19], a[112:115], v[96:99]\n\t"
      "v_mfma_f32_16x16x32_bf16 v[100:103], a[16:19], a[116:119], v[100:103]\n\t"
      "ds_read_b64_tr_b16 a[74:75], v23 offset:10880\n\t"
      "v_mfma_f32_16x16x32_bf16 v[104:107], a[16:19], a[120:123], v[104:107]\n\t"
      "v_mfma_f32_16x16x32_bf16 v[108:111], a[16:19], a[124:127], v[108:111]\n\t"
      "ds_read_b64_tr_b16 a[76:77], v23 offset:7328\n\t"
      "v_mfma_f32_16x16x32_bf16 v[112:115], a[20:23], a[112:115], v[112:115]\n\t"
      "s_waitcnt vmcnt(17)\n\t"
      "v_cmp_eq_u32 vcc, %[eplo], v12\n\t"
      "v_cmp_eq_u32 s[56:57], %[ephi], v13\n\t"
      "s_nop 1\n\t"
      "s_and_b64 vcc, vcc, s[56:57]\n\t"
      "s_cmp_eq_u64 vcc, exec\n\t"
      "s_cbranch_scc1 PUOK_%=\n\t"
      "s_mov_b32 s55, 0\n\t"
      "PULOOP_%=:\n\t"
      "s_sleep 1\n\t"
      "global_load_dwordx2 v[12:13], v15, %[fur] sc1\n\t"
      "s_waitcnt vmcnt(0)\n\t"
      "v_cmp_eq_u32 vcc, %[eplo], v12\n\t"
      "v_cmp_eq_u32 s[56:57], %[ephi], v13\n\t"
      "s_nop 1\n\t"
      "s_and_b64 vcc, vcc, s[56:57]\n\t"
      "s_cmp_eq_u64 vcc, exec\n\t"
      "s_cbranch_scc1 PUOK_%=\n\t"
      "s_add_u32 s55, s55, 1\n\t"
      "s_cmp_lt_u32 s55, 0x100000\n\t"
      "s_cbranch_scc1 PULOOP_%=\n\t"
      "v_mov_b32 v12, %[eplo]\n\t"
      "v_mov_b32 v13, %[ephi]\n\t"
      "v_mov_b32 v15, 0\n\t"
      "global_store_dwordx2 v15, v[12:13], %[tmo] sc1\n\t"
      "s_waitcnt vmcnt(0)\n\t"
      "PUOK_%=:\n\t"
      "global_load_dwordx4 a[228:231], v27, s[42:43] offset:1024 sc1\n\t"
      "v_mfma_f32_16x16x32_bf16 v[116:119], a[20:23], a[116:119], v[116:119]\n\t"
      "ds_read_b64_tr_b16 a[78:79], v23 offset:10912\n\t"
      "v_mfma_f32_16x16x32_bf16 v[120:123], a[20:23], a[120:123], v[120:123]\n\t"
      "v_mfma_f32_16x16x32_bf16 v[124:127], a[20:23], a[124:127], v[124:127]\n\t"
      "ds_read_b64_tr_b16 a[80:81], v23 offset:7360\n\t"
      "v_mfma_f32_16x16x32_bf16 v[128:131], a[24:27], a[112:115], v[128:131]\n\t"
      "global_load_dwordx4 a[240:243], v26, %[ub] offset:0 sc1\n\t"
      "v_mfma_f32_16x16x32_bf16 v[132:135], a[24:27], a[116:119], v[132:135]\n\t"
      "ds_read_b64_tr_b16 a[82:83], v23 offset:10944\n\t"
      "v_mfma_f32_16x16x32_bf16 v[136:139], a[24:27], a[120:123], v[136:139]\n\t"
      "v_mfma_f32_16x16x32_bf16 v[140:143], a[24:27], a[124:127], v[140:143]\n\t"
      "ds_read_b64_tr_b16 a[84:85], v23 offset:21632\n\t"
      "v_mfma_f32_16x16x32_bf16 v[144:147], a[28:31], a[112:115], v[144:147]\n\t"
      "ds_read_b64_tr_b16 a[86:87], v23 offset:25216\n\t"
      "v_mfma_f32_16x16x32_bf16 v[148:151], a[28:31], a[116:119], v[148:151]\n\t"
      "v_mfma_f32_16x16x32_bf16 v[152:155], a[28:31], a[120:123], v[152:155]\n\t"
      "ds_read_b64_tr_b16 a[88:89], v23 offset:21664\n\t"
      "global_load_dwordx4 a[244:247], v26, %[ub] offset:1024 sc1\n\t"
      "v_mfma_f32_16x16x32_bf16 v[156:159], a[28:31], a[124:127], v[156:159]\n\t"
      "v_mfma_f32_16x16x32_bf16 v[160:163], a[32:35], a[112:115], v[160:163]\n\t"
      "ds_read_b64_tr_b16 a[90:91], v23 offset:25248\n\t"
      "v_mfma_f32_16x16x32_bf16 v[164:167], a[32:35], a[116:119], v[164:167]\n\t"
      "v_mfma_f32_16x16x32_bf16 v[168:171], a[32:35], a[120:123], v[168:171]\n\t"
      "ds_read_b64_tr_b16 a[92:93], v23 offset:21696\n\t"
      "global_load_dwordx4 a[232:235], v28, s[42:43] offset:1024 sc1\n\t"
      "v_mfma_f32_16x16x32_bf16 v[172:175], a[32:35], a[124:127], v[172:175]\n\t"
      "v_mfma_f32_16x16x32_bf16 v[176:179], a[36:39], a[112:115], v[176:179]\n\t"
      "ds_read_b64_tr_b16 a[94:95], v23 offset:25280\n\t"
      "global_load_dwordx4 a[248:251], v26, %[ub] offset:2048 sc1\n\t"
      "v_mfma_f32_16x16x32_bf16 v[180:183], a[36:39], a[116:119], v[180:183]\n\t"
      "v_mfma_f32_16x16x32_bf16 v[184:187], a[36:39], a[120:123], v[184:187]\n\t"
      "ds_read_b64_tr_b16 a[96:97], v23 offset:21728\n\t"
      "v_mfma_f32_16x16x32_bf16 v[188:191], a[36:39], a[124:127], v[188:191]\n\t"
      "v_mfma_f32_16x16x32_bf16 v[192:195], a[40:43], a[112:115], v[192:195]\n\t"
      "ds_read_b64_tr_b16 a[98:99], v23 offset:25312\n\t"
      "v_mfma_f32_16x16x32_bf16 v[196:199], a[40:43], a[116:119], v[196:199]\n\t"
      "ds_read_b64_tr_b16 a[100:101], v23 offset:21760\n\t"
      "v_mfma_f32_16x16x32_bf16 v[200:203], a[40:43], a[120:123], v[200:203]\n\t"
      "global_load_dwordx4 a[252:255], v26, %[ub] offset:3072 sc1\n\t"
      "v_mfma_f32_16x16x32_bf16 v[204:207], a[40:43], a[124:127], v[204:207]\n\t"
      "ds_read_b64_tr_b16 a[102:103], v23 offset:25344\n\t"
      "v_mfma_f32_16x16x32_bf16 v[208:211], a[44:47], a[112:115], v[208:211]\n\t"
      "s_add_u32 s47, s47, 28928\n\t"
      "v_mfma_f32_16x16x32_bf16 v[212:215], a[44:47], a[116:119], v[212:215]\n\t"
      "ds_read_b64_tr_b16 a[104:105], v23 offset:21792\n\t"
      "v_mfma_f32_16x16x32_bf16 v[216:219], a[44:47], a[120:123], v[216:219]\n\t"
      "s_cmp_eq_u32 s47, 115712\n\t"
      "s_cselect_b32 s47, 0, s47\n\t"
      "v_mfma_f32_16x16x32_bf16 v[220:223], a[44:47], a[124:127], v[220:223]\n\t"
      "ds_read_b64_tr_b16 a[106:107], v23 offset:25376\n\t"
      "v_mfma_f32_16x16x32_bf16 v[224:227], a[48:51], a[112:115], v[224:227]\n\t"
      "global_load_dwordx4 a[236:239], v29, s[42:43] offset:1024 sc1\n\t"
      "v_mfma_f32_16x16x32_bf16 v[228:231], a[48:51], a[116:119], v[228:231]\n\t"
      "ds_read_b64_tr_b16 a[108:109], v23 offset:21824\n\t"
      "v_mfma_f32_16x16x32_bf16 v[232:235], a[48:51], a[120:123], v[232:235]\n\t"
      "s_mov_b32 m0, %[mc]\n\t"
      "s_nop 0\n\t"
      "buffer_load_dwordx4 v26, %[srd], 4 offen offset:0 lds\n\t"
      "v_mfma_f32_16x16x32_bf16 v[236:239], a[48:51], a[124:127], v[236:239]\n\t"
      "ds_read_b64_tr_b16 a[110:111], v23 offset:25408\n\t"
      "v_mfma_f32_16x16x32_bf16 v[240:243], a[52:55], a[112:115], v[240:243]\n\t"
      "v_add_u32 v24, s47, v25\n\t"
      "v_mfma_f32_16x16x32_bf16 v[244:247], a[52:55], a[116:119], v[244:247]\n\t"
      "s_add_u32 s42, s42, 0x800\n\t"
      "s_addc_u32 s43, s43, 0\n\t"
      "v_mfma_f32_16x16x32_bf16 v[248:251], a[52:55], a[120:123], v[248:251]\n\t"
      "v_mfma_f32_16x16x32_bf16 v[252:255], a[52:55], a[124:127], v[252:255]\n\t"
      "s_waitcnt vmcnt(54)\n\t"
      "s_waitcnt lgkmcnt(0)\n\t"
      "v_mfma_f32_16x16x32_bf16 v[32:35], a[56:59], a[128:131], v[32:35]\n\t"
      "v_mfma_f32_16x16x32_bf16 v[36:39], a[56:59], a[132:135], v[36:39]\n\t"
      "buffer_load_dwordx4 v26, %[srd], 4 offen offset:1024 lds\n\t"
      "v_mfma_f32_16x16x32_bf16 v[40:43], a[56:59], a[136:139], v[40:43]\n\t"
      "v_mfma_f32_16x16x32_bf16 v[44:47], a[56:59], a[140:143], v[44:47]\n\t"
      "v_mfma_f32_16x16x32_bf16 v[48:51], a[60:63], a[128:131], v[48:51]\n\t"
      "v_mfma_f32_16x16x32_bf16 v[52:55], a[60:63], a[132:135], v[52:55]\n\t"
      "v_mfma_f32_16x16x32_bf16 v[56:59], a[60:63], a[136:139], v[56:59]\n\t"
      "v_add_u32 v23, 57856, v24\n\t"
      "v_mfma_f32_16x16x32_bf16 v[60:63], a[60:63], a[140:143], v[60:63]\n\t"
      "v_mfma_f32_16x16x32_bf16 v[64:67], a[64:67], a[128:131], v[64:67]\n\t"
      "s_waitcnt lgkmcnt(0)\n\t"
      "s_waitcnt vmcnt(11)\n\t"
      "s_barrier\n\t"
      "buffer_load_dwordx4 v26, %[srd], 4 offen offset:2048 lds\n\t"
      "v_mfma_f32_16x16x32_bf16 v[68:71], a[64:67], a[132:135], v[68:71]\n\t"
      "ds_read_b64_tr_b16 a[0:1], v24 offset:0\n\t"
      "v_mfma_f32_16x16x32_bf16 v[72:75], a[64:67], a[136:139], v[72:75]\n\t"
      "ds_read_b64_tr_b16 a[2:3], v24 offset:3584\n\t"
      "v_mfma_f32_16x16x32_bf16 v[76:79], a[64:67], a[140:143], v[76:79]\n\t"
      "v_mfma_f32_16x16x32_bf16 v[80:83], a[68:71], a[128:131], v[80:83]\n\t"
      "ds_read_b64_tr_b16 a[4:5], v24 offset:14464\n\t"
      "v_mfma_f32_16x16x32_bf16 v[84:87], a[68:71], a[132:135], v[84:87]\n\t"
      "v_mfma_f32_16x16x32_bf16 v[88:91], a[68:71], a[136:139], v[88:91]\n\t"
      "ds_read_b64_tr_b16 a[6:7], v24 offset:18048\n\t"
      "v_mfma_f32_16x16x32_bf16 v[92:95], a[68:71], a[140:143], v[92:95]\n\t"
      "buffer_load_dwordx4 v26, %[srd], 4 offen offset:3072 lds\n\t"
      "v_mfma_f32_16x16x32_bf16 v[96:99], a[72:75], a[128:131], v[96:99]\n\t"
      "ds_read_b64_tr_b16 a[8:9], v24 offset:7168\n\t"
      "v_mfma_f32_16x16x32_bf16 v[100:103], a[72:75], a[132:135], v[100:103]\n\t"
      "v_mfma_f32_16x16x32_bf16 v[104:107], a[72:75], a[136:139], v[104:107]\n\t"
      "ds_read_b64_tr_b16 a[10:11], v24 offset:10752\n\t"
      "v_mfma_f32_16x16x32_bf16 v[108:111], a[72:75], a[140:143], v[108:111]\n\t"
      "v_mfma_f32_16x16x32_bf16 v[112:115], a[76:79], a[128:131], v[112:115]\n\t"
      "ds_read_b64_tr_b16 a[12:13], v24 offset:21632\n\t"
      "v_mfma_f32_16x16x32_bf16 v[116:119], a[76:79], a[132:135], v[116:119]\n\t"
      "ds_read_b64_tr_b16 a[14:15], v24 offset:25216\n\t"
      "v_mfma_f32_16x16x32_bf16 v[120:123], a[76:79], a[136:139], v[120:123]\n\t"
      "s_add_u32 m0, m0, 4224\n\t"
      "s_nop 0\n\t"
      "buffer_load_dwordx4 v26, %[srd], 8 offen offset:0 lds\n\t"
      "v_mfma_f32_16x16x32_bf16 v[124:127], a[76:79], a[140:143], v[124:127]\n\t"
      "ds_read_b64_tr_b16 a[16:17], v24 offset:28928\n\t"
      "v_mfma_f32_16x16x32_bf16 v[128:131], a[80:83], a[128:131], v[128:131]\n\t"
      "v_mfma_f32_16x16x32_bf16 v[132:135], a[80:83], a[132:135], v[132:135]\n\t"
      "ds_read_b64_tr_b16 a[18:19], v24 offset:32512\n\t"
      "v_mfma_f32_16x16x32_bf16 v[136:139], a[80:83], a[136:139], v[136:139]\n\t"
      "v_mfma_f32_16x16x32_bf16 v[140:143], a[80:83], a[140:143], v[140:143]\n\t"
      "ds_read_b64_tr_b16 a[20:21], v24 offset:43392\n\t"
      "v_mfma_f32_16x16x32_bf16 v[144:147], a[84:87], a[128:131], v[144:147]\n\t"
      "v_mfma_f32_16x16x32_bf16 v[148:151], a[84:87], a[132:135], v[148:151]\n\t"
      "ds_read_b64_tr_b16 a[22:23], v24 offset:46976\n\t"
      "buffer_load_dwordx4 v26, %[srd], 8 offen offset:1024 lds\n\t"
      "v_mfma_f32_16x16x32_bf16 v[152:155], a[84:87], a[136:139], v[152:155]\n\t"
      "v_mfma_f32_16x16x32_bf16 v[156:159], a[84:87], a[140:143], v[156:159]\n\t"
      "ds_read_b64_tr_b16 a[24:25], v24 offset:36096\n\t"
      "v_mfma_f32_16x16x32_bf16 v[160:163], a[88:91], a[128:131], v[160:163]\n\t"
      "ds_read_b64_tr_b16 a[26:27], v24 offset:39680\n\t"
      "v_mfma_f32_16x16x32_bf16 v[164:167], a[88:91], a[132:135], v[164:167]\n\t"
      "v_mfma_f32_16x16x32_bf16 v[168:171], a[88:91], a[136:139], v[168:171]\n\t"
      "ds_read_b64_tr_b16 a[28:29], v24 offset:50560\n\t"
      "v_mfma_f32_16x16x32_bf16 v[172:175], a[88:91], a[140:143], v[172:175]\n\t"
      "v_mfma_f32_16x16x32_bf16 v[176:179], a[92:95], a[128:131], v[176:179]\n\t"
      "ds_read_b64_tr_b16 a[30:31], v24 offset:54144\n\t"
      "buffer_load_dwordx4 v26, %[srd], 8 offen offset:2048 lds\n\t"
      "v_mfma_f32_16x16x32_bf16 v[180:183], a[92:95], a[132:135], v[180:183]\n\t"
      "v_mfma_f32_16x16x32_bf16 v[184:187], a[92:95], a[136:139], v[184:187]\n\t"
      "ds_read_b64_tr_b16 a[32:33], v23 offset:0\n\t"
      "v_mfma_f32_16x16x32_bf16 v[188:191], a[92:95], a[140:143], v[188:191]\n\t"
      "v_mfma_f32_16x16x32_bf16 v[192:195], a[96:99], a[128:131], v[192:195]\n\t"
      "ds_read_b64_tr_b16 a[34:35], v23 offset:3584\n\t"
      "v_mfma_f32_16x16x32_bf16 v[196:199], a[96:99], a[132:135], v[196:199]\n\t"
      "v_mfma_f32_16x16x32_bf16 v[200:203], a[96:99], a[136:139], v[200:203]\n\t"
      "ds_read_b64_tr_b16 a[36:37], v23 offset:14464\n\t"
      "v_mfma_f32_16x16x32_bf16 v[204:207], a[96:99], a[140:143], v[204:207]\n\t"
      "ds_read_b64_tr_b16 a[38:39], v23 offset:18048\n\t"
      "buffer_load_dwordx4 v26, %[srd], 8 offen offset:3072 lds\n\t"
      "v_mfma_f32_16x16x32_bf16 v[208:211], a[100:103], a[128:131], v[208:211]\n\t"
      "v_mfma_f32_16x16x32_bf16 v[212:215], a[100:103], a[132:135], v[212:215]\n\t"
      "ds_read_b64_tr_b16 a[40:41], v23 offset:7168\n\t"
      "v_mfma_f32_16x16x32_bf16 v[216:219], a[100:103], a[136:139], v[216:219]\n\t"
      "v_mfma_f32_16x16x32_bf16 v[220:223], a[100:103], a[140:143], v[220:223]\n\t"
      "ds_read_b64_tr_b16 a[42:43], v23 offset:10752\n\t"
      "v_mfma_f32_16x16x32_bf16 v[224:227], a[104:107], a[128:131], v[224:227]\n\t"
      "v_mfma_f32_16x16x32_bf16 v[228:231], a[104:107], a[132:135], v[228:231]\n\t"
      "ds_read_b64_tr_b16 a[44:45], v23 offset:21632\n\t"
      "v_mfma_f32_16x16x32_bf16 v[232:235], a[104:107], a[136:139], v[232:235]\n\t"
      "v_mfma_f32_16x16x32_bf16 v[236:239], a[104:107], a[140:143], v[236:239]\n\t"
      "ds_read_b64_tr_b16 a[46:47], v23 offset:25216\n\t"
      "v_mfma_f32_16x16x32_bf16 v[240:243], a[108:111], a[128:131], v[240:243]\n\t"
      "v_mfma_f32_16x16x32_bf16 v[244:247], a[108:111], a[132:135], v[244:247]\n\t"
      "v_mfma_f32_16x16x32_bf16 v[248:251], a[108:111], a[136:139], v[248:251]\n\t"
      "v_mfma_f32_16x16x32_bf16 v[252:255], a[108:111], a[140:143], v[252:255]\n\t"
      "s_mov_b32 s40, 0xbfb8aa3b\n\t"
      "s_mov_b32 s41, 0xbfb8aa3b\n\t"
      "s_mov_b32 s42, 1.0\n\t"
      "s_mov_b32 s43, 1.0\n\t"
      "v_add_u32 v27, 0x14000, v27\n\t"
      "v_add_u32 v28, 0x28000, v28\n\t"
      "v_add_u32 v29, 0x3c000, v29\n\t"
      "s_waitcnt lgkmcnt(0)\n\t"
      "s_waitcnt vmcnt(52)\n\t"
      "v_mfma_f32_16x16x32_bf16 v[32:35], a[0:3], a[144:147], v[32:35]\n\t"
      "ds_read_b64_tr_b16 a[48:49], v24 offset:32\n\t"
      "v_mfma_f32_16x16x32_bf16 v[36:39], a[0:3], a[148:151], v[36:39]\n\t"
      "ds_read_b64_tr_b16 a[50:51], v24 offset:3616\n\t"
      "v_mfma_f32_16x16x32_bf16 v[40:43], a[0:3], a[152:155], v[40:43]\n\t"
      "v_mfma_f32_16x16x32_bf16 v[44:47], a[0:3], a[156:159], v[44:47]\n\t"
      "ds_read_b64_tr_b16 a[52:53], v24 offset:14496\n\t"
      "v_mfma_f32_16x16x32_bf16 v[144:147], a[4:7], a[144:147], v[144:147]\n\t"
      "v_mfma_f32_16x16x32_bf16 v[148:151], a[4:7], a[148:151], v[148:151]\n\t"
      "ds_read_b64_tr_b16 a[54:55], v24 offset:18080\n\t"
      "v_mfma_f32_16x16x32_bf16 v[152:155], a[4:7], a[152:155], v[152:155]\n\t"
      "ds_read_b64_tr_b16 a[56:57], v24 offset:7200\n\t"
      "v_mfma_f32_16x16x32_bf16 v[156:159], a[4:7], a[156:159], v[156:159]\n\t"
      "s_waitcnt vmcnt(44)\n\t"
      "v_mfma_f32_16x16x32_bf16 v[32:35], a[8:11], a[160:163], v[32:35]\n\t"
      "ds_read_b64_tr_b16 a[58:59], v24 offset:10784\n\t"
      "v_mfma_f32_16x16x32_bf16 v[36:39], a[8:11], a[164:167], v[36:39]\n\t"
      "v_mfma_f32_16x16x32_bf16 v[40:43], a[8:11], a[168:171], v[40:43]\n\t"
      "ds_read_b64_tr_b16 a[60:61], v24 offset:21664\n\t"
      "v_mfma_f32_16x16x32_bf16 v[44:47], a[8:11], a[172:175], v[44:47]\n\t"
      "ds_read_b64_tr_b16 a[62:63], v24 offset:25248\n\t"
      "v_mfma_f32_16x16x32_bf16 v[144:147], a[12:15], a[160:163], v[144:147]\n\t"
      "v_mfma_f32_16x16x32_bf16 v[148:151], a[12:15], a[164:167], v[148:151]\n\t"
      "ds_read_b64_tr_b16 a[64:65], v24 offset:28960\n\t"
      "v_mfma_f32_16x16x32_bf16 v[152:155], a[12:15], a[168:171], v[152:155]\n\t"
      "v_mfma_f32_16x16x32_bf16 v[156:159], a[12:15], a[172:175], v[156:159]\n\t"
      "ds_read_b64_tr_b16 a[66:67], v24 offset:32544\n\t"
      "s_waitcnt vmcnt(36)\n\t"
      "v_mfma_f32_16x16x32_bf16 v[32:35], a[16:19], a[176:179], v[32:35]\n\t"
      "ds_read_b64_tr_b16 a[68:69], v24 offset:43424\n\t"
      "v_mfma_f32_16x16x32_bf16 v[36:39], a[16:19], a[180:183], v[36:39]\n\t"
      "v_mfma_f32_16x16x32_bf16 v[40:43], a[16:19], a[184:187], v[40:43]\n\t"
      "ds_read_b64_tr_b16 a[70:71], v24 offset:47008\n\t"
      "v_mfma_f32_16x16x32_bf16 v[44:47], a[16:19], a[188:191], v[44:47]\n\t"
      "v_mfma_f32_16x16x32_bf16 v[144:147], a[20:23], a[176:179], v[144:147]\n\t"
      "ds_read_b64_tr_b16 a[72:73], v24 offset:36128\n\t"
      "v_mfma_f32_16x16x32_bf16 v[148:151], a[20:23], a[180:183], v[148:151]\n\t"
      "ds_read_b64_tr_b16 a[74:75], v24 offset:39712\n\t"
      "v_mfma_f32_16x16x32_bf16 v[152:155], a[20:23], a[184:187], v[152:155]\n\t"
      "v_mfma_f32_16x16x32_bf16 v[156:159], a[20:23], a[188:191], v[156:159]\n\t"
      "ds_read_b64_tr_b16 a[76:77], v24 offset:50592\n\t"
      "s_waitcnt vmcnt(27)\n\t"
      "v_mfma_f32_16x16x32_bf16 v[32:35], a[24:27], a[192:195], v[32:35]\n\t"
      "v_mfma_f32_16x16x32_bf16 v[36:39], a[24:27], a[196:199], v[36:39]\n\t"
      "ds_read_b64_tr_b16 a[78:79], v24 offset:54176\n\t"
      "v_mfma_f32_16x16x32_bf16 v[40:43], a[24:27], a[200:203], v[40:43]\n\t"
      "ds_read_b64_tr_b16 a[80:81], v23 offset:32\n\t"
      "v_mfma_f32_16x16x32_bf16 v[44:47], a[24:27], a[204:207], v[44:47]\n\t"
      "v_mfma_f32_16x16x32_bf16 v[144:147], a[28:31], a[192:195], v[144:147]\n\t"
      "ds_read_b64_tr_b16 a[82:83], v23 offset:3616\n\t"
      "v_mfma_f32_16x16x32_bf16 v[148:151], a[28:31], a[196:199], v[148:151]\n\t"
      "v_mfma_f32_16x16x32_bf16 v[152:155], a[28:31], a[200:203], v[152:155]\n\t"
      "ds_read_b64_tr_b16 a[84:85], v23 offset:14496\n\t"
      "v_mfma_f32_16x16x32_bf16 v[156:159], a[28:31], a[204:207], v[156:159]\n\t"
      "ds_read_b64_tr_b16 a[86:87], v23 offset:18080\n\t"
      "s_waitcnt vmcnt(16)\n\t"
      "v_mfma_f32_16x16x32_bf16 v[32:35], a[32:35], a[208:211], v[32:35]\n\t"
      "v_mfma_f32_16x16x32_bf16 v[36:39], a[32:35], a[212:215], v[36:39]\n\t"
      "ds_read_b64_tr_b16 a[88:89], v23 offset:7200\n\t"
      "v_mfma_f32_16x16x32_bf16 v[40:43], a[32:35], a[216:219], v[40:43]\n\t"
      "v_mfma_f32_16x16x32_bf16 v[44:47], a[32:35], a[220:223], v[44:47]\n\t"
      "ds_read_b64_tr_b16 a[90:91], v23 offset:10784\n\t"
      "v_mfma_f32_16x16x32_bf16 v[144:147], a[36:39], a[208:211], v[144:147]\n\t"
      "ds_read_b64_tr_b16 a[92:93], v23 offset:21664\n\t"
      "v_mfma_f32_16x16x32_bf16 v[148:151], a[36:39], a[212:215], v[148:151]\n\t"
      "v_mfma_f32_16x16x32_bf16 v[152:155], a[36:39], a[216:219], v[152:155]\n\t"
      "ds_read_b64_tr_b16 a[94:95], v23 offset:25248\n\t"
      "v_mfma_f32_16x16x32_bf16 v[156:159], a[36:39], a[220:223], v[156:159]\n\t"
      "s_waitcnt vmcnt(8)\n\t"
      "v_mfma_f32_16x16x32_bf16 v[32:35], a[40:43], a[224:227], v[32:35]\n\t"
      "v_mfma_f32_16x16x32_bf16 v[36:39], a[40:43], a[228:231], v[36:39]\n\t"
      "v_mfma_f32_16x16x32_bf16 v[40:43], a[40:43], a[232:235], v[40:43]\n\t"
      "v_mfma_f32_16x16x32_bf16 v[44:47], a[40:43], a[236:239], v[44:47]\n\t"
      "v_mfma_f32_16x16x32_bf16 v[144:147], a[44:47], a[224:227], v[144:147]\n\t"
      "v_mfma_f32_16x16x32_bf16 v[148:151], a[44:47], a[228:231], v[148:151]\n\t"
      "v_mfma_f32_16x16x32_bf16 v[152:155], a[44:47], a[232:235], v[152:155]\n\t"
      "v_mfma_f32_16x16x32_bf16 v[156:159], a[44:47], a[236:239], v[156:159]\n\t"
      "s_waitcnt lgkmcnt(0)\n\t"
      "v_mfma_f32_16x16x32_bf16 v[48:51], a[48:51], a[144:147], v[48:51]\n\t"
      "ds_read_b64_tr_b16 a[0:1], v24 offset:64\n\t"
      "v_mfma_f32_16x16x32_bf16 v[52:55], a[48:51], a[148:151], v[52:55]\n\t"
      "ds_read_b64_tr_b16 a[2:3], v24 offset:3648\n\t"
      "v_mfma_f32_16x16x32_bf16 v[56:59], a[48:51], a[152:155], v[56:59]\n\t"
      "v_mfma_f32_16x16x32_bf16 v[60:63], a[48:51], a[156:159], v[60:63]\n\t"
      "ds_read_b64_tr_b16 a[4:5], v24 offset:14528\n\t"
      "v_mfma_f32_16x16x32_bf16 v[160:163], a[52:55], a[144:147], v[160:163]\n\t"
      "v_mfma_f32_16x16x32_bf16 v[164:167], a[52:55], a[148:151], v[164:167]\n\t"
      "ds_read_b64_tr_b16 a[6:7], v24 offset:18112\n\t"
      "v_mfma_f32_16x16x32_bf16 v[168:171], a[52:55], a[152:155], v[168:171]\n\t"
      "ds_read_b64_tr_b16 a[8:9], v24 offset:7232\n\t"
      "v_mfma_f32_16x16x32_bf16 v[172:175], a[52:55], a[156:159], v[172:175]\n\t"
      "v_mfma_f32_16x16x32_bf16 v[48:51], a[56:59], a[160:163], v[48:51]\n\t"
      "ds_read_b64_tr_b16 a[10:11], v24 offset:10816\n\t"
      "v_mul_f32 v32, s40, v32\n\t"
      "v_mul_f32 v33, s40, v33\n\t"
      "v_mul_f32 v34, s40, v34\n\t"
      "v_mfma_f32_16x16x32_bf16 v[52:55], a[56:59], a[164:167], v[52:55]\n\t"
      "v_mul_f32 v35, s40, v35\n\t"
      "v_exp_f32 v32, v32\n\t"
      "v_mfma_f32_16x16x32_bf16 v[56:59], a[56:59], a[168:171], v[56:59]\n\t"
      "ds_read_b64_tr_b16 a[12:13], v24 offset:21696\n\t"
      "v_exp_f32 v33, v33\n\t"
      "v_exp_f32 v34, v34\n\t"
      "v_mfma_f32_16x16x32_bf16 v[60:63], a[56:59], a[172:175], v[60:63]\n\t"
      "ds_read_b64_tr_b16 a[14:15], v24 offset:25280\n\t"
      "v_exp_f32 v35, v35\n\t"
      "v_add_f32 v32, 1.0, v32\n\t"
      "v_add_f32 v33, 1.0, v33\n\t"
      "v_mfma_f32_16x16x32_bf16 v[160:163], a[60:63], a[160:163], v[160:163]\n\t"
      "v_add_f32 v34, 1.0, v34\n\t"
      "v_add_f32 v35, 1.0, v35\n\t"
      "v_mfma_f32_16x16x32_bf16 v[164:167], a[60:63], a[164:167], v[164:167]\n\t"
      "ds_read_b64_tr_b16 a[16:17], v24 offset:28992\n\t"
      "v_rcp_f32 v32, v32\n\t"
      "v_rcp_f32 v33, v33\n\t"
      "v_mfma_f32_16x16x32_bf16 v[168:171], a[60:63], a[168:171], v[168:171]\n\t"
      "v_rcp_f32 v34, v34\n\t"
      "v_rcp_f32 v35, v35\n\t"
      "v_mfma_f32_16x16x32_bf16 v[172:175], a[60:63], a[172:175], v[172:175]\n\t"
      "ds_read_b64_tr_b16 a[18:19], v24 offset:32576\n\t"
      "v_mul_f32 v32, v32, v144\n\t"
      "v_mul_f32 v33, v33, v145\n\t"
      "v_mul_f32 v34, v34, v146\n\t"
      "v_mfma_f32_16x16x32_bf16 v[48:51], a[64:67], a[176:179], v[48:51]\n\t"
      "ds_read_b64_tr_b16 a[20:21], v24 offset:43456\n\t"
      "v_mul_f32 v35, v35, v147\n\t"
      "v_cvt_pk_bf16_f32 v32, v32, v33\n\t"
      "v_mfma_f32_16x16x32_bf16 v[52:55], a[64:67], a[180:183], v[52:55]\n\t"
      "v_cvt_pk_bf16_f32 v33, v34, v35\n\t"
      "v_mul_f32 v36, s40, v36\n\t"
      "v_mfma_f32_16x16x32_bf16 v[56:59], a[64:67], a[184:187], v[56:59]\n\t"
      "ds_read_b64_tr_b16 a[22:23], v24 offset:47040\n\t"
      "v_mul_f32 v37, s40, v37\n\t"
      "v_mul_f32 v38, s40, v38\n\t"
      "v_mfma_f32_16x16x32_bf16 v[60:63], a[64:67], a[188:191], v[60:63]\n\t"
      "v_mul_f32 v39, s40, v39\n\t"
      "v_exp_f32 v36, v36\n\t"
      "v_exp_f32 v37, v37\n\t"
      "v_mfma_f32_16x16x32_bf16 v[160:163], a[68:71], a[176:179], v[160:163]\n\t"
      "ds_read_b64_tr_b16 a[24:25], v24 offset:36160\n\t"
      "v_exp_f32 v38, v38\n\t"
      "v_exp_f32 v39, v39\n\t"
      "v_min_u32 v31, 55, %[lane]\n\t"
      "v_lshlrev_b32 v31, 7, v31\n\t"
      "global_load_dword v31, v31, %[pfw]\n\t"
      "v_mfma_f32_16x16x32_bf16 v[164:167], a[68:71], a[180:183], v[164:167]\n\t"
      "ds_read_b64_tr_b16 a[26:27], v24 offset:39744\n\t"
      "v_add_f32 v36, 1.0, v36\n\t"
      "v_add_f32 v37, 1.0, v37\n\t"
      "v_mfma_f32_16x16x32_bf16 v[168:171], a[68:71], a[184:187], v[168:171]\n\t"
      "v_add_f32 v38, 1.0, v38\n\t"
      "v_add_f32 v39, 1.0, v39\n\t"
      "v_mfma_f32_16x16x32_bf16 v[172:175], a[68:71], a[188:191], v[172:175]\n\t"
      "ds_read_b64_tr_b16 a[28:29], v24 offset:50624\n\t"
      "v_rcp_f32 v36, v36\n\t"
      "v_rcp_f32 v37, v37\n\t"
      "v_rcp_f32 v38, v38\n\t"
      "v_mfma_f32_16x16x32_bf16 v[48:51], a[72:75], a[192:195], v[48:51]\n\t"
      "v_rcp_f32 v39, v39\n\t"
      "v_mul_f32 v36, v36, v148\n\t"
      "v_mfma_f32_16x16x32_bf16 v[52:55], a[72:75], a[196:199], v[52:55]\n\t"
      "ds_read_b64_tr_b16 a[30:31], v24 offset:54208\n\t"
      "v_mul_f32 v37, v37, v149\n\t"
      "v_mul_f32 v38, v38, v150\n\t"
      "v_mfma_f32_16x16x32_bf16 v[56:59], a[72:75], a[200:203], v[56:59]\n\t"
      "ds_read_b64_tr_b16 a[32:33], v23 offset:64\n\t"
      "v_mul_f32 v39, v39, v151\n\t"
      "v_cvt_pk_bf16_f32 v36, v36, v37\n\t"
      "v_mfma_f32_16x16x32_bf16 v[60:63], a[72:75], a[204:207], v[60:63]\n\t"
      "v_cvt_pk_bf16_f32 v37, v38, v39\n\t"
      "v_mul_f32 v40, s40, v40\n\t"
      "v_mul_f32 v41, s40, v41\n\t"
      "v_mfma_f32_16x16x32_bf16 v[160:163], a[76:79], a[192:195], v[160:163]\n\t"
      "ds_read_b64_tr_b16 a[34:35], v23 offset:3648\n\t"
      "v_mul_f32 v42, s40, v42\n\t"
      "v_mul_f32 v43, s40, v43\n\t"
      "v_mfma_f32_16x16x32_bf16 v[164:167], a[76:79], a[196:199], v[164:167]\n\t"
      "v_exp_f32 v40, v40\n\t"
      "v_exp_f32 v41, v41\n\t"
      "v_mfma_f32_16x16x32_bf16 v[168:171], a[76:79], a[200:203], v[168:171]\n\t"
      "ds_read_b64_tr_b16 a[36:37], v23 offset:14528\n\t"
      "v_exp_f32 v42, v42\n\t"
      "v_exp_f32 v43, v43\n\t"
      "v_mfma_f32_16x16x32_bf16 v[172:175], a[76:79], a[204:207], v[172:175]\n\t"
      "ds_read_b64_tr_b16 a[38:39], v23 offset:18112\n\t"
      "v_add_f32 v40, 1.0, v40\n\t"
      "v_add_f32 v41, 1.0, v41\n\t"
      "v_add_f32 v42, 1.0, v42\n\t"
      "v_mfma_f32_16x16x32_bf16 v[48:51], a[80:83], a[208:211], v[48:51]\n\t"
      "v_add_f32 v43, 1.0, v43\n\t"
      "v_rcp_f32 v40, v40\n\t"
      "v_mfma_f32_16x16x32_bf16 v[52:55], a[80:83], a[212:215], v[52:55]\n\t"
      "ds_read_b64_tr_b16 a[40:41], v23 offset:7232\n\t"
      "v_rcp_f32 v41, v41\n\t"
      "v_rcp_f32 v42, v42\n\t"
      "v_mfma_f32_16x16x32_bf16 v[56:59], a[80:83], a[216:219], v[56:59]\n\t"
      "v_rcp_f32 v43, v43\n\t"
      "v_mul_f32 v40, v40, v152\n\t"
      "v_mfma_f32_16x16x32_bf16 v[60:63], a[80:83], a[220:223], v[60:63]\n\t"
      "ds_read_b64_tr_b16 a[42:43], v23 offset:10816\n\t"
      "v_mul_f32 v41, v41, v153\n\t"
      "v_mul_f32 v42, v42, v154\n\t"
      "v_mul_f32 v43, v43, v155\n\t"
      "v_mfma_f32_16x16x32_bf16 v[160:163], a[84:87], a[208:211], v[160:163]\n\t"
      "ds_read_b64_tr_b16 a[44:45], v23 offset:21696\n\t"
      "v_cvt_pk_bf16_f32 v40, v40, v41\n\t"
      "v_cvt_pk_bf16_f32 v41, v42, v43\n\t"
      "v_mfma_f32_16x16x32_bf16 v[164:167], a[84:87], a[212:215], v[164:167]\n\t"
      "v_mul_f32 v44, s40, v44\n\t"
      "v_mul_f32 v45, s40, v45\n\t"
      "v_mfma_f32_16x16x32_bf16 v[168:171], a[84:87], a[216:219], v[168:171]\n\t"
      "ds_read_b64_tr_b16 a[46:47], v23 offset:25280\n\t"
      "v_mul_f32 v46, s40, v46\n\t"
      "v_mul_f32 v47, s40, v47\n\t"
      "v_mfma_f32_16x16x32_bf16 v[172:175], a[84:87], a[220:223], v[172:175]\n\t"
      "v_exp_f32 v44, v44\n\t"
      "v_exp_f32 v45, v45\n\t"
      "v_exp_f32 v46, v46\n\t"
      "v_mfma_f32_16x16x32_bf16 v[48:51], a[88:91], a[224:227], v[48:51]\n\t"
      "v_exp_f32 v47, v47\n\t"
      "v_add_f32 v44, 1.0, v44\n\t"
      "v_mfma_f32_16x16x32_bf16 v[52:55], a[88:91], a[228:231], v[52:55]\n\t"
      "v_add_f32 v45, 1.0, v45\n\t"
      "v_add_f32 v46, 1.0, v46\n\t"
      "v_mfma_f32_16x16x32_bf16 v[56:59], a[88:91], a[232:235], v[56:59]\n\t"
      "v_add_f32 v47, 1.0, v47\n\t"
      "v_rcp_f32 v44, v44\n\t"
      "v_mfma_f32_16x16x32_bf16 v[60:63], a[88:91], a[236:239], v[60:63]\n\t"
      "v_rcp_f32 v45, v45\n\t"
      "v_rcp_f32 v46, v46\n\t"
      "v_rcp_f32 v47, v47\n\t"
      "v_mfma_f32_16x16x32_bf16 v[160:163], a[92:95], a[224:227], v[160:163]\n\t"
      "v_mul_f32 v44, v44, v156\n\t"
      "v_mul_f32 v45, v45, v157\n\t"
      "v_mfma_f32_16x16x32_bf16 v[164:167], a[92:95], a[228:231], v[164:167]\n\t"
      "v_mul_f32 v46, v46, v158\n\t"
      "v_mul_f32 v47, v47, v159\n\t"
      "v_mfma_f32_16x16x32_bf16 v[168:171], a[92:95], a[232:235], v[168:171]\n\t"
      "v_cvt_pk_bf16_f32 v44, v44, v45\n\t"
      "v_cvt_pk_bf16_f32 v45, v46, v47\n\t"
      "v_mfma_f32_16x16x32_bf16 v[172:175], a[92:95], a[236:239], v[172:175]\n\t"
      "s_waitcnt lgkmcnt(0)\n\t"
      "v_mfma_f32_16x16x32_bf16 v[64:67], a[0:3], a[144:147], v[64:67]\n\t"
      "ds_read_b64_tr_b16 a[48:49], v24 offset:96\n\t"
      "v_mfma_f32_16x16x32_bf16 v[68:71], a[0:3], a[148:151], v[68:71]\n\t"
      "ds_read_b64_tr_b16 a[50:51], v24 offset:3680\n\t"
      "v_mfma_f32_16x16x32_bf16 v[72:75], a[0:3], a[152:155], v[72:75]\n\t"
      "v_mfma_f32_16x16x32_bf16 v[76:79], a[0:3], a[156:159], v[76:79]\n\t"
      "ds_read_b64_tr_b16 a[52:53], v24 offset:14560\n\t"
      "v_mfma_f32_16x16x32_bf16 v[176:179], a[4:7], a[144:147], v[176:179]\n\t"
      "v_mfma_f32_16x16x32_bf16 v[180:183], a[4:7], a[148:151], v[180:183]\n\t"
      "ds_read_b64_tr_b16 a[54:55], v24 offset:18144\n\t"
      "v_mfma_f32_16x16x32_bf16 v[184:187], a[4:7], a[152:155], v[184:187]\n\t"
      "ds_read_b64_tr_b16 a[56:57], v24 offset:7264\n\t"
      "v_mfma_f32_16x16x32_bf16 v[188:191], a[4:7], a[156:159], v[188:191]\n\t"
      "v_mfma_f32_16x16x32_bf16 v[64:67], a[8:11], a[160:163], v[64:67]\n\t"
      "ds_read_b64_tr_b16 a[58:59], v24 offset:10848\n\t"
      "v_mul_f32 v48, s40, v48\n\t"
      "v_mul_f32 v49, s40, v49\n\t"
      "v_mul_f32 v50, s40, v50\n\t"
      "v_mfma_f32_16x16x32_bf16 v[68:71], a[8:11], a[164:167], v[68:71]\n\t"
      "v_mul_f32 v51, s40, v51\n\t"
      "v_exp_f32 v48, v48\n\t"
      "v_mfma_f32_16x16x32_bf16 v[72:75], a[8:11], a[168:171], v[72:75]\n\t"
      "ds_read_b64_tr_b16 a[60:61], v24 offset:21728\n\t"
      "v_exp_f32 v49, v49\n\t"
      "v_exp_f32 v50, v50\n\t"
      "v_exp_f32 v51, v51\n\t"
      "s_waitcnt vmcnt(10)\n\t"
      "v_add_u32 v15, %[mu], v26\n\t"
      "v_mfma_f32_16x16x32_bf16 v[76:79], a[8:11], a[172:175], v[76:79]\n\t"
      "ds_read_b64_tr_b16 a[62:63], v24 offset:25312\n\t"
      "v_add_f32 v48, 1.0, v48\n\t"
      "v_add_f32 v49, 1.0, v49\n\t"
      "v_mfma_f32_16x16x32_bf16 v[176:179], a[12:15], a[160:163], v[176:179]\n\t"
      "v_add_f32 v50, 1.0, v50\n\t"
      "v_add_f32 v51, 1.0, v51\n\t"
      "v_mfma_f32_16x16x32_bf16 v[180:183], a[12:15], a[164:167], v[180:183]\n\t"
      "ds_read_b64_tr_b16 a[64:65], v24 offset:29024\n\t"
      "v_rcp_f32 v48, v48\n\t"
      "v_rcp_f32 v49, v49\n\t"
      "v_rcp_f32 v50, v50\n\t"
      "v_mfma_f32_16x16x32_bf16 v[184:187], a[12:15], a[168:171], v[184:187]\n\t"
      "v_rcp_f32 v51, v51\n\t"
      "v_mul_f32 v48, v48, v160\n\t"
      "v_mfma_f32_16x16x32_bf16 v[188:191], a[12:15], a[172:175], v[188:191]\n\t"
      "ds_read_b64_tr_b16 a[66:67], v24 offset:32608\n\t"
      "v_mul_f32 v49, v49, v161\n\t"
      "v_mul_f32 v50, v50, v162\n\t"
      "v_mfma_f32_16x16x32_bf16 v[64:67], a[16:19], a[176:179], v[64:67]\n\t"
      "ds_read_b64_tr_b16 a[68:69], v24 offset:43488\n\t"
      "v_mul_f32 v51, v51, v163\n\t"
      "v_cvt_pk_bf16_f32 v34, v48, v49\n\t"
      "v_cvt_pk_bf16_f32 v35, v50, v51\n\t"
      "ds_write_b128 v15, a[240:243] offset:0\n\t"
      "v_mfma_f32_16x16x32_bf16 v[68:71], a[16:19], a[180:183], v[68:71]\n\t"
      "global_store_dwordx4 v26, v[32:35], %[hbq] offset:0 sc1\n\t"
      "v_mul_f32 v52, s40, v52\n\t"
      "v_mfma_f32_16x16x32_bf16 v[72:75], a[16:19], a[184:187], v[72:75]\n\t"
      "ds_read_b64_tr_b16 a[70:71], v24 offset:47072\n\t"
      "v_mul_f32 v53, s40, v53\n\t"
      "v_mul_f32 v54, s40, v54\n\t"
      "v_mfma_f32_16x16x32_bf16 v[76:79], a[16:19], a[188:191], v[76:79]\n\t"
      "v_mul_f32 v55, s40, v55\n\t"
      "v_exp_f32 v52, v52\n\t"
      "v_exp_f32 v53, v53\n\t"
      "v_mfma_f32_16x16x32_bf16 v[176:179], a[20:23], a[176:179], v[176:179]\n\t"
      "ds_read_b64_tr_b16 a[72:73], v24 offset:36192\n\t"
      "v_exp_f32 v54, v54\n\t"
      "v_exp_f32 v55, v55\n\t"
      "v_mfma_f32_16x16x32_bf16 v[180:183], a[20:23], a[180:183], v[180:183]\n\t"
      "ds_read_b64_tr_b16 a[74:75], v24 offset:39776\n\t"
      "v_add_f32 v52, 1.0, v52\n\t"
      "v_add_f32 v53, 1.0, v53\n\t"
      "v_add_f32 v54, 1.0, v54\n\t"
      "v_mfma_f32_16x16x32_bf16 v[184:187], a[20:23], a[184:187], v[184:187]\n\t"
      "v_add_f32 v55, 1.0, v55\n\t"
      "v_rcp_f32 v52, v52\n\t"
      "ds_write_b128 v15, a[244:247] offset:1024\n\t"
      "v_mfma_f32_16x16x32_bf16 v[188:191], a[20:23], a[188:191], v[188:191]\n\t"
      "ds_read_b64_tr_b16 a[76:77], v24 offset:50656\n\t"
      "v_rcp_f32 v53, v53\n\t"
      "v_rcp_f32 v54, v54\n\t"
      "v_mfma_f32_16x16x32_bf16 v[64:67], a[24:27], a[192:195], v[64:67]\n\t"
      "v_rcp_f32 v55, v55\n\t"
      "v_mul_f32 v52, v52, v164\n\t"
      "v_mul_f32 v53, v53, v165\n\t"
      "v_mfma_f32_16x16x32_bf16 v[68:71], a[24:27], a[196:199], v[68:71]\n\t"
      "ds_read_b64_tr_b16 a[78:79], v24 offset:54240\n\t"
      "v_mul_f32 v54, v54, v166\n\t"
      "v_mul_f32 v55, v55, v167\n\t"
      "v_mfma_f32_16x16x32_bf16 v[72:75], a[24:27], a[200:203], v[72:75]\n\t"
      "ds_read_b64_tr_b16 a[80:81], v23 offset:96\n\t"
      "v_cvt_pk_bf16_f32 v38, v52, v53\n\t"
      "v_cvt_pk_bf16_f32 v39, v54, v55\n\t"
      "v_mfma_f32_16x16x32_bf16 v[76:79], a[24:27], a[204:207], v[76:79]\n\t"
      "global_store_dwordx4 v27, v[36:39], %[hbq] offset:0 sc1\n\t"
      "v_mul_f32 v56, s40, v56\n\t"
      "v_mul_f32 v57, s40, v57\n\t"
      "v_mfma_f32_16x16x32_bf16 v[176:179], a[28:31], a[192:195], v[176:179]\n\t"
      "ds_read_b64_tr_b16 a[82:83], v23 offset:3680\n\t"
      "v_mul_f32 v58, s40, v58\n\t"
      "v_mul_f32 v59, s40, v59\n\t"
      "ds_write_b128 v15, a[248:251] offset:2048\n\t"
      "v_mfma_f32_16x16x32_bf16 v[180:183], a[28:31], a[196:199], v[180:183]\n\t"
      "v_exp_f32 v56, v56\n\t"
      "v_exp_f32 v57, v57\n\t"
      "v_mfma_f32_16x16x32_bf16 v[184:187], a[28:31], a[200:203], v[184:187]\n\t"
      "ds_read_b64_tr_b16 a[84:85], v23 offset:14560\n\t"
      "v_exp_f32 v58, v58\n\t"
      "v_exp_f32 v59, v59\n\t"
      "v_add_f32 v56, 1.0, v56\n\t"
      "v_mfma_f32_16x16x32_bf16 v[188:191], a[28:31], a[204:207], v[188:191]\n\t"
      "ds_read_b64_tr_b16 a[86:87], v23 offset:18144\n\t"
      "v_add_f32 v57, 1.0, v57\n\t"
      "v_add_f32 v58, 1.0, v58\n\t"
      "v_mfma_f32_16x16x32_bf16 v[64:67], a[32:35], a[208:211], v[64:67]\n\t"
      "v_add_f32 v59, 1.0, v59\n\t"
      "v_rcp_f32 v56, v56\n\t"
      "v_mfma_f32_16x16x32_bf16 v[68:71], a[32:35], a[212:215], v[68:71]\n\t"
      "ds_read_b64_tr_b16 a[88:89], v23 offset:7264\n\t"
      "v_rcp_f32 v57, v57\n\t"
      "v_rcp_f32 v58, v58\n\t"
      "v_rcp_f32 v59, v59\n\t"
      "v_mfma_f32_16x16x32_bf16 v[72:75], a[32:35], a[216:219], v[72:75]\n\t"
      "v_mul_f32 v56, v56, v168\n\t"
      "v_mul_f32 v57, v57, v169\n\t"
      "ds_write_b128 v15, a[252:255] offset:3072\n\t"
      "v_mfma_f32_16x16x32_bf16 v[76:79], a[32:35], a[220:223], v[76:79]\n\t"
      "ds_read_b64_tr_b16 a[90:91], v23 offset:10848\n\t"
      "v_mul_f32 v58, v58, v170\n\t"
      "v_mul_f32 v59, v59, v171\n\t"
      "v_cvt_pk_bf16_f32 v42, v56, v57\n\t"
      "v_mfma_f32_16x16x32_bf16 v[176:179], a[36:39], a[208:211], v[176:179]\n\t"
      "ds_read_b64_tr_b16 a[92:93], v23 offset:21728\n\t"
      "v_cvt_pk_bf16_f32 v43, v58, v59\n\t"
      "global_store_dwordx4 v28, v[40:43], %[hbq] offset:0 sc1\n\t"
      "v_mfma_f32_16x16x32_bf16 v[180:183], a[36:39], a[212:215], v[180:183]\n\t"
      "v_mul_f32 v60, s40, v60\n\t"
      "v_mul_f32 v61, s40, v61\n\t"
      "v_mfma_f32_16x16x32_bf16 v[184:187], a[36:39], a[216:219], v[184:187]\n\t"
      "ds_read_b64_tr_b16 a[94:95], v23 offset:25312\n\t"
      "v_mul_f32 v62, s40, v62\n\t"
      "v_mul_f32 v63, s40, v63\n\t"
      "v_exp_f32 v60, v60\n\t"
      "v_mfma_f32_16x16x32_bf16 v[188:191], a[36:39], a[220:223], v[188:191]\n\t"
      "v_exp_f32 v61, v61\n\t"
      "v_exp_f32 v62, v62\n\t"
      "v_mfma_f32_16x16x32_bf16 v[64:67], a[40:43], a[224:227], v[64:67]\n\t"
      "v_exp_f32 v63, v63\n\t"
      "v_add_f32 v60, 1.0, v60\n\t"
      "v_mfma_f32_16x16x32_bf16 v[68:71], a[40:43], a[228:231], v[68:71]\n\t"
      "v_add_f32 v61, 1.0, v61\n\t"
      "v_add_f32 v62, 1.0, v62\n\t"
      "v_add_f32 v63, 1.0, v63\n\t"
      "v_mfma_f32_16x16x32_bf16 v[72:75], a[40:43], a[232:235], v[72:75]\n\t"
      "v_rcp_f32 v60, v60\n\t"
      "v_rcp_f32 v61, v61\n\t"
      "v_mfma_f32_16x16x32_bf16 v[76:79], a[40:43], a[236:239], v[76:79]\n\t"
      "v_rcp_f32 v62, v62\n\t"
      "v_rcp_f32 v63, v63\n\t"
      "v_mfma_f32_16x16x32_bf16 v[176:179], a[44:47], a[224:227], v[176:179]\n\t"
      "v_mul_f32 v60, v60, v172\n\t"
      "v_mul_f32 v61, v61, v173\n\t"
      "v_mul_f32 v62, v62, v174\n\t"
      "v_mfma_f32_16x16x32_bf16 v[180:183], a[44:47], a[228:231], v[180:183]\n\t"
      "v_mul_f32 v63, v63, v175\n\t"
      "v_cvt_pk_bf16_f32 v46, v60, v61\n\t"
      "v_mfma_f32_16x16x32_bf16 v[184:187], a[44:47], a[232:235], v[184:187]\n\t"
      "v_cvt_pk_bf16_f32 v47, v62, v63\n\t"
      "global_store_dwordx4 v29, v[44:47], %[hbq] offset:0 sc1\n\t"
      "v_mfma_f32_16x16x32_bf16 v[188:191], a[44:47], a[236:239], v[188:191]\n\t"
      "s_waitcnt lgkmcnt(0)\n\t"
      "v_mfma_f32_16x16x32_bf16 v[80:83], a[48:51], a[144:147], v[80:83]\n\t"
      "ds_read_b64_tr_b16 a[0:1], v24 offset:128\n\t"
      "v_mfma_f32_16x16x32_bf16 v[84:87], a[48:51], a[148:151], v[84:87]\n\t"
      "ds_read_b64_tr_b16 a[2:3], v24 offset:3712\n\t"
      "v_mfma_f32_16x16x32_bf16 v[88:91], a[48:51], a[152:155], v[88:91]\n\t"
      "v_mfma_f32_16x16x32_bf16 v[92:95], a[48:51], a[156:159], v[92:95]\n\t"
      "ds_read_b64_tr_b16 a[4:5], v24 offset:14592\n\t"
      "v_mfma_f32_16x16x32_bf16 v[192:195], a[52:55], a[144:147], v[192:195]\n\t"
      "v_mfma_f32_16x16x32_bf16 v[196:199], a[52:55], a[148:151], v[196:199]\n\t"
      "ds_read_b64_tr_b16 a[6:7], v24 offset:18176\n\t"
      "v_mfma_f32_16x16x32_bf16 v[200:203], a[52:55], a[152:155], v[200:203]\n\t"
      "ds_read_b64_tr_b16 a[8:9], v24 offset:7296\n\t"
      "v_mfma_f32_16x16x32_bf16 v[204:207], a[52:55], a[156:159], v[204:207]\n\t"
      "v_mfma_f32_16x16x32_bf16 v[80:83], a[56:59], a[160:163], v[80:83]\n\t"
      "ds_read_b64_tr_b16 a[10:11], v24 offset:10880\n\t"
      "v_mul_f32 v64, s40, v64\n\t"
      "v_mul_f32 v65, s40, v65\n\t"
      "v_mul_f32 v66, s40, v66\n\t"
      "v_mfma_f32_16x16x32_bf16 v[84:87], a[56:59], a[164:167], v[84:87]\n\t"
      "v_mul_f32 v67, s40, v67\n\t"
      "v_exp_f32 v64, v64\n\t"
      "v_mfma_f32_16x16x32_bf16 v[88:91], a[56:59], a[168:171], v[88:91]\n\t"
      "ds_read_b64_tr_b16 a[12:13], v24 offset:21760\n\t"
      "v_exp_f32 v65, v65\n\t"
      "v_exp_f32 v66, v66\n\t"
      "v_mfma_f32_16x16x32_bf16 v[92:95], a[56:59], a[172:175], v[92:95]\n\t"
      "ds_read_b64_tr_b16 a[14:15], v24 offset:25344\n\t"
      "v_exp_f32 v67, v67\n\t"
      "v_add_f32 v64, 1.0, v64\n\t"
      "v_add_f32 v65, 1.0, v65\n\t"
      "v_mfma_f32_16x16x32_bf16 v[192:195], a[60:63], a[160:163], v[192:195]\n\t"
      "v_add_f32 v66, 1.0, v66\n\t"
      "v_add_f32 v67, 1.0, v67\n\t"
      "v_mfma_f32_16x16x32_bf16 v[196:199], a[60:63], a[164:167], v[196:199]\n\t"
      "ds_read_b64_tr_b16 a[16:17], v24 offset:29056\n\t"
      "v_rcp_f32 v64, v64\n\t"
      "v_rcp_f32 v65, v65\n\t"
      "v_mfma_f32_16x16x32_bf16 v[200:203], a[60:63], a[168:171], v[200:203]\n\t"
      "v_rcp_f32 v66, v66\n\t"
      "v_rcp_f32 v67, v67\n\t"
      "v_mfma_f32_16x16x32_bf16 v[204:207], a[60:63], a[172:175], v[204:207]\n\t"
      "ds_read_b64_tr_b16 a[18:19], v24 offset:32640\n\t"
      "v_mul_f32 v64, v64, v176\n\t"
      "v_mul_f32 v65, v65, v177\n\t"
      "v_mul_f32 v66, v66, v178\n\t"
      "v_mfma_f32_16x16x32_bf16 v[80:83], a[64:67], a[176:179], v[80:83]\n\t"
      "ds_read_b64_tr_b16 a[20:21], v24 offset:43520\n\t"
      "v_mul_f32 v67, v67, v179\n\t"
      "v_cvt_pk_bf16_f32 v64, v64, v65\n\t"
      "v_mfma_f32_16x16x32_bf16 v[84:87], a[64:67], a[180:183], v[84:87]\n\t"
      "v_cvt_pk_bf16_f32 v65, v66, v67\n\t"
      "v_mul_f32 v68, s40, v68\n\t"
      "v_mfma_f32_16x16x32_bf16 v[88:91], a[64:67], a[184:187], v[88:91]\n\t"
      "ds_read_b64_tr_b16 a[22:23], v24 offset:47104\n\t"
      "v_mul_f32 v69, s40, v69\n\t"
      "v_mul_f32 v70, s40, v70\n\t"
      "v_mfma_f32_16x16x32_bf16 v[92:95], a[64:67], a[188:191], v[92:95]\n\t"
      "v_mul_f32 v71, s40, v71\n\t"
      "v_exp_f32 v68, v68\n\t"
      "v_exp_f32 v69, v69\n\t"
      "v_mfma_f32_16x16x32_bf16 v[192:195], a[68:71], a[176:179], v[192:195]\n\t"
      "ds_read_b64_tr_b16 a[24:25], v24 offset:36224\n\t"
      "v_exp_f32 v70, v70\n\t"
      "v_exp_f32 v71, v71\n\t"
      "v_mfma_f32_16x16x32_bf16 v[196:199], a[68:71], a[180:183], v[196:199]\n\t"
      "ds_read_b64_tr_b16 a[26:27], v24 offset:39808\n\t"
      "v_add_f32 v68, 1.0, v68\n\t"
      "v_add_f32 v69, 1.0, v69\n\t"
      "v_mfma_f32_16x16x32_bf16 v[200:203], a[68:71], a[184:187], v[200:203]\n\t"
      "v_add_f32 v70, 1.0, v70\n\t"
      "v_add_f32 v71, 1.0, v71\n\t"
      "v_mfma_f32_16x16x32_bf16 v[204:207], a[68:71], a[188:191], v[204:207]\n\t"
      "ds_read_b64_tr_b16 a[28:29], v24 offset:50688\n\t"
      "v_rcp_f32 v68, v68\n\t"
      "v_rcp_f32 v69, v69\n\t"
      "v_rcp_f32 v70, v70\n\t"
      "v_mfma_f32_16x16x32_bf16 v[80:83], a[72:75], a[192:195], v[80:83]\n\t"
      "v_rcp_f32 v71, v71\n\t"
      "v_mul_f32 v68, v68, v180\n\t"
      "v_mfma_f32_16x16x32_bf16 v[84:87], a[72:75], a[196:199], v[84:87]\n\t"
      "ds_read_b64_tr_b16 a[30:31], v24 offset:54272\n\t"
      "v_mul_f32 v69, v69, v181\n\t"
      "v_mul_f32 v70, v70, v182\n\t"
      "v_mfma_f32_16x16x32_bf16 v[88:91], a[72:75], a[200:203], v[88:91]\n\t"
      "ds_read_b64_tr_b16 a[32:33], v23 offset:128\n\t"
      "v_mul_f32 v71, v71, v183\n\t"
      "v_cvt_pk_bf16_f32 v68, v68, v69\n\t"
      "v_mfma_f32_16x16x32_bf16 v[92:95], a[72:75], a[204:207], v[92:95]\n\t"
      "v_cvt_pk_bf16_f32 v69, v70, v71\n\t"
      "v_mul_f32 v72, s40, v72\n\t"
      "v_mul_f32 v73, s40, v73\n\t"
      "v_mfma_f32_16x16x32_bf16 v[192:195], a[76:79], a[192:195], v[192:195]\n\t"
      "ds_read_b64_tr_b16 a[34:35], v23 offset:3712\n\t"
      "v_mul_f32 v74, s40, v74\n\t"
      "v_mul_f32 v75, s40, v75\n\t"
      "v_mfma_f32_16x16x32_bf16 v[196:199], a[76:79], a[196:199], v[196:199]\n\t"
      "v_exp_f32 v72, v72\n\t"
      "v_exp_f32 v73, v73\n\t"
      "v_mfma_f32_16x16x32_bf16 v[200:203], a[76:79], a[200:203], v[200:203]\n\t"
      "ds_read_b64_tr_b16 a[36:37], v23 offset:14592\n\t"
      "v_exp_f32 v74, v74\n\t"
      "v_exp_f32 v75, v75\n\t"
      "v_mfma_f32_16x16x32_bf16 v[204:207], a[76:79], a[204:207], v[204:207]\n\t"
      "ds_read_b64_tr_b16 a[38:39], v23 offset:18176\n\t"
      "v_add_f32 v72, 1.0, v72\n\t"
      "v_add_f32 v73, 1.0, v73\n\t"
      "v_add_f32 v74, 1.0, v74\n\t"
      "v_mfma_f32_16x16x32_bf16 v[80:83], a[80:83], a[208:211], v[80:83]\n\t"
      "v_add_f32 v75, 1.0, v75\n\t"
      "v_rcp_f32 v72, v72\n\t"
      "v_mfma_f32_16x16x32_bf16 v[84:87], a[80:83], a[212:215], v[84:87]\n\t"
      "ds_read_b64_tr_b16 a[40:41], v23 offset:7296\n\t"
      "v_rcp_f32 v73, v73\n\t"
      "v_rcp_f32 v74, v74\n\t"
      "v_mfma_f32_16x16x32_bf16 v[88:91], a[80:83], a[216:219], v[88:91]\n\t"
      "v_rcp_f32 v75, v75\n\t"
      "v_mul_f32 v72, v72, v184\n\t"
      "v_mfma_f32_16x16x32_bf16 v[92:95], a[80:83], a[220:223], v[92:95]\n\t"
      "ds_read_b64_tr_b16 a[42:43], v23 offset:10880\n\t"
      "v_mul_f32 v73, v73, v185\n\t"
      "v_mul_f32 v74, v74, v186\n\t"
      "v_mul_f32 v75, v75, v187\n\t"
      "v_mfma_f32_16x16x32_bf16 v[192:195], a[84:87], a[208:211], v[192:195]\n\t"
      "ds_read_b64_tr_b16 a[44:45], v23 offset:21760\n\t"
      "v_cvt_pk_bf16_f32 v72, v72, v73\n\t"
      "v_cvt_pk_bf16_f32 v73, v74, v75\n\t"
      "v_mfma_f32_16x16x32_bf16 v[196:199], a[84:87], a[212:215], v[196:199]\n\t"
      "v_mul_f32 v76, s40, v76\n\t"
      "v_mul_f32 v77, s40, v77\n\t"
      "v_mfma_f32_16x16x32_bf16 v[200:203], a[84:87], a[216:219], v[200:203]\n\t"
      "ds_read_b64_tr_b16 a[46:47], v23 offset:25344\n\t"
      "v_mul_f32 v78, s40, v78\n\t"
      "v_mul_f32 v79, s40, v79\n\t"
      "v_mfma_f32_16x16x32_bf16 v[204:207], a[84:87], a[220:223], v[204:207]\n\t"
      "v_exp_f32 v76, v76\n\t"
      "v_exp_f32 v77, v77\n\t"
      "v_exp_f32 v78, v78\n\t"
      "v_mfma_f32_16x16x32_bf16 v[80:83], a[88:91], a[224:227], v[80:83]\n\t"
      "v_exp_f32 v79, v79\n\t"
      "v_add_f32 v76, 1.0, v76\n\t"
      "v_mfma_f32_16x16x32_bf16 v[84:87], a[88:91], a[228:231], v[84:87]\n\t"
      "v_add_f32 v77, 1.0, v77\n\t"
      "v_add_f32 v78, 1.0, v78\n\t"
      "v_mfma_f32_16x16x32_bf16 v[88:91], a[88:91], a[232:235], v[88:91]\n\t"
      "v_add_f32 v79, 1.0, v79\n\t"
      "v_rcp_f32 v76, v76\n\t"
      "v_mfma_f32_16x16x32_bf16 v[92:95], a[88:91], a[236:239], v[92:95]\n\t"
      "v_rcp_f32 v77, v77\n\t"
      "v_rcp_f32 v78, v78\n\t"
      "v_rcp_f32 v79, v79\n\t"
      "v_mfma_f32_16x16x32_bf16 v[192:195], a[92:95], a[224:227], v[192:195]\n\t"
      "v_mul_f32 v76, v76, v188\n\t"
      "v_mul_f32 v77, v77, v189\n\t"
      "v_mfma_f32_16x16x32_bf16 v[196:199], a[92:95], a[228:231], v[196:199]\n\t"
      "v_mul_f32 v78, v78, v190\n\t"
      "v_mul_f32 v79, v79, v191\n\t"
      "v_mfma_f32_16x16x32_bf16 v[200:203], a[92:95], a[232:235], v[200:203]\n\t"
      "v_cvt_pk_bf16_f32 v76, v76, v77\n\t"
      "v_cvt_pk_bf16_f32 v77, v78, v79\n\t"
      "v_mfma_f32_16x16x32_bf16 v[204:207], a[92:95], a[236:239], v[204:207]\n\t"
      "s_waitcnt lgkmcnt(0)\n\t"
      "v_mfma_f32_16x16x32_bf16 v[96:99], a[0:3], a[144:147], v[96:99]\n\t"
      "ds_read_b64_tr_b16 a[48:49], v24 offset:160\n\t"
      "v_mfma_f32_16x16x32_bf16 v[100:103], a[0:3], a[148:151], v[100:103]\n\t"
      "ds_read_b64_tr_b16 a[50:51], v24 offset:3744\n\t"
      "v_mfma_f32_16x16x32_bf16 v[104:107], a[0:3], a[152:155], v[104:107]\n\t"
      "v_mfma_f32_16x16x32_bf16 v[108:111], a[0:3], a[156:159], v[108:111]\n\t"
      "ds_read_b64_tr_b16 a[52:53], v24 offset:14624\n\t"
      "v_mfma_f32_16x16x32_bf16 v[208:211], a[4:7], a[144:147], v[208:211]\n\t"
      "v_mfma_f32_16x16x32_bf16 v[212:215], a[4:7], a[148:151], v[212:215]\n\t"
      "ds_read_b64_tr_b16 a[54:55], v24 offset:18208\n\t"
      "v_mfma_f32_16x16x32_bf16 v[216:219], a[4:7], a[152:155], v[216:219]\n\t"
      "ds_read_b64_tr_b16 a[56:57], v24 offset:7328\n\t"
      "v_mfma_f32_16x16x32_bf16 v[220:223], a[4:7], a[156:159], v[220:223]\n\t"
      "v_mfma_f32_16x16x32_bf16 v[96:99], a[8:11], a[160:163], v[96:99]\n\t"
      "ds_read_b64_tr_b16 a[58:59], v24 offset:10912\n\t"
      "v_mul_f32 v80, s40, v80\n\t"
      "v_mul_f32 v81, s40, v81\n\t"
      "v_mul_f32 v82, s40, v82\n\t"
      "v_mfma_f32_16x16x32_bf16 v[100:103], a[8:11], a[164:167], v[100:103]\n\t"
      "v_mul_f32 v83, s40, v83\n\t"
      "v_exp_f32 v80, v80\n\t"
      "v_mfma_f32_16x16x32_bf16 v[104:107], a[8:11], a[168:171], v[104:107]\n\t"
      "ds_read_b64_tr_b16 a[60:61], v24 offset:21792\n\t"
      "v_exp_f32 v81, v81\n\t"
      "v_exp_f32 v82, v82\n\t"
      "v_exp_f32 v83, v83\n\t"
      "v_mfma_f32_16x16x32_bf16 v[108:111], a[8:11], a[172:175], v[108:111]\n\t"
      "ds_read_b64_tr_b16 a[62:63], v24 offset:25376\n\t"
      "v_add_f32 v80, 1.0, v80\n\t"
      "v_add_f32 v81, 1.0, v81\n\t"
      "v_mfma_f32_16x16x32_bf16 v[208:211], a[12:15], a[160:163], v[208:211]\n\t"
      "v_add_f32 v82, 1.0, v82\n\t"
      "v_add_f32 v83, 1.0, v83\n\t"
      "v_mfma_f32_16x16x32_bf16 v[212:215], a[12:15], a[164:167], v[212:215]\n\t"
      "ds_read_b64_tr_b16 a[64:65], v24 offset:29088\n\t"
      "v_rcp_f32 v80, v80\n\t"
      "v_rcp_f32 v81, v81\n\t"
      "v_rcp_f32 v82, v82\n\t"
      "v_mfma_f32_16x16x32_bf16 v[216:219], a[12:15], a[168:171], v[216:219]\n\t"
      "v_rcp_f32 v83, v83\n\t"
      "v_mul_f32 v80, v80, v192\n\t"
      "v_mfma_f32_16x16x32_bf16 v[220:223], a[12:15], a[172:175], v[220:223]\n\t"
      "ds_read_b64_tr_b16 a[66:67], v24 offset:32672\n\t"
      "v_mul_f32 v81, v81, v193\n\t"
      "v_mul_f32 v82, v82, v194\n\t"
      "v_mfma_f32_16x16x32_bf16 v[96:99], a[16:19], a[176:179], v[96:99]\n\t"
      "ds_read_b64_tr_b16 a[68:69], v24 offset:43552\n\t"
      "v_mul_f32 v83, v83, v195\n\t"
      "v_cvt_pk_bf16_f32 v66, v80, v81\n\t"
      "v_cvt_pk_bf16_f32 v67, v82, v83\n\t"
      "v_mfma_f32_16x16x32_bf16 v[100:103], a[16:19], a[180:183], v[100:103]\n\t"
      "global_store_dwordx4 v26, v[64:67], %[hbq] offset:1024 sc1\n\t"
      "v_mul_f32 v84, s40, v84\n\t"
      "v_mfma_f32_16x16x32_bf16 v[104:107], a[16:19], a[184:187], v[104:107]\n\t"
      "ds_read_b64_tr_b16 a[70:71], v24 offset:47136\n\t"
      "v_mul_f32 v85, s40, v85\n\t"
      "v_mul_f32 v86, s40, v86\n\t"
      "v_mfma_f32_16x16x32_bf16 v[108:111], a[16:19], a[188:191], v[108:111]\n\t"
      "v_mul_f32 v87, s40, v87\n\t"
      "v_exp_f32 v84, v84\n\t"
      "v_exp_f32 v85, v85\n\t"
      "v_mfma_f32_16x16x32_bf16 v[208:211], a[20:23], a[176:179], v[208:211]\n\t"
      "ds_read_b64_tr_b16 a[72:73], v24 offset:36256\n\t"
      "v_exp_f32 v86, v86\n\t"
      "v_exp_f32 v87, v87\n\t"
      "v_mfma_f32_16x16x32_bf16 v[212:215], a[20:23], a[180:183], v[212:215]\n\t"
      "ds_read_b64_tr_b16 a[74:75], v24 offset:39840\n\t"
      "v_add_f32 v84, 1.0, v84\n\t"
      "v_add_f32 v85, 1.0, v85\n\t"
      "v_add_f32 v86, 1.0, v86\n\t"
      "v_mfma_f32_16x16x32_bf16 v[216:219], a[20:23], a[184:187], v[216:219]\n\t"
      "v_add_f32 v87, 1.0, v87\n\t"
      "v_rcp_f32 v84, v84\n\t"
      "v_mfma_f32_16x16x32_bf16 v[220:223], a[20:23], a[188:191], v[220:223]\n\t"
      "ds_read_b64_tr_b16 a[76:77], v24 offset:50720\n\t"
      "v_rcp_f32 v85, v85\n\t"
      "v_rcp_f32 v86, v86\n\t"
      "v_mfma_f32_16x16x32_bf16 v[96:99], a[24:27], a[192:195], v[96:99]\n\t"
      "v_rcp_f32 v87, v87\n\t"
      "v_mul_f32 v84, v84, v196\n\t"
      "v_mul_f32 v85, v85, v197\n\t"
      "v_mfma_f32_16x16x32_bf16 v[100:103], a[24:27], a[196:199], v[100:103]\n\t"
      "ds_read_b64_tr_b16 a[78:79], v24 offset:54304\n\t"
      "v_mul_f32 v86, v86, v198\n\t"
      "v_mul_f32 v87, v87, v199\n\t"
      "v_mfma_f32_16x16x32_bf16 v[104:107], a[24:27], a[200:203], v[104:107]\n\t"
      "ds_read_b64_tr_b16 a[80:81], v23 offset:160\n\t"
      "v_cvt_pk_bf16_f32 v70, v84, v85\n\t"
      "v_cvt_pk_bf16_f32 v71, v86, v87\n\t"
      "v_mfma_f32_16x16x32_bf16 v[108:111], a[24:27], a[204:207], v[108:111]\n\t"
      "global_store_dwordx4 v27, v[68:71], %[hbq] offset:1024 sc1\n\t"
      "v_mul_f32 v88, s40, v88\n\t"
      "v_mul_f32 v89, s40, v89\n\t"
      "v_mfma_f32_16x16x32_bf16 v[208:211], a[28:31], a[192:195], v[208:211]\n\t"
      "ds_read_b64_tr_b16 a[82:83], v23 offset:3744\n\t"
      "v_mul_f32 v90, s40, v90\n\t"
      "v_mul_f32 v91, s40, v91\n\t"
      "v_mfma_f32_16x16x32_bf16 v[212:215], a[28:31], a[196:199], v[212:215]\n\t"
      "v_exp_f32 v88, v88\n\t"
      "v_exp_f32 v89, v89\n\t"
      "v_mfma_f32_16x16x32_bf16 v[216:219], a[28:31], a[200:203], v[216:219]\n\t"
      "ds_read_b64_tr_b16 a[84:85], v23 offset:14624\n\t"
      "v_exp_f32 v90, v90\n\t"
      "v_exp_f32 v91, v91\n\t"
      "v_add_f32 v88, 1.0, v88\n\t"
      "v_mfma_f32_16x16x32_bf16 v[220:223], a[28:31], a[204:207], v[220:223]\n\t"
      "ds_read_b64_tr_b16 a[86:87], v23 offset:18208\n\t"
      "v_add_f32 v89, 1.0, v89\n\t"
      "v_add_f32 v90, 1.0, v90\n\t"
      "v_mfma_f32_16x16x32_bf16 v[96:99], a[32:35], a[208:211], v[96:99]\n\t"
      "v_add_f32 v91, 1.0, v91\n\t"
      "v_rcp_f32 v88, v88\n\t"
      "v_mfma_f32_16x16x32_bf16 v[100:103], a[32:35], a[212:215], v[100:103]\n\t"
      "ds_read_b64_tr_b16 a[88:89], v23 offset:7328\n\t"
      "v_rcp_f32 v89, v89\n\t"
      "v_rcp_f32 v90, v90\n\t"
      "v_rcp_f32 v91, v91\n\t"
      "v_mfma_f32_16x16x32_bf16 v[104:107], a[32:35], a[216:219], v[104:107]\n\t"
      "v_mul_f32 v88, v88, v200\n\t"
      "v_mul_f32 v89, v89, v201\n\t"
      "v_mfma_f32_16x16x32_bf16 v[108:111], a[32:35], a[220:223], v[108:111]\n\t"
      "ds_read_b64_tr_b16 a[90:91], v23 offset:10912\n\t"
      "v_mul_f32 v90, v90, v202\n\t"
      "v_mul_f32 v91, v91, v203\n\t"
      "v_cvt_pk_bf16_f32 v74, v88, v89\n\t"
      "v_mfma_f32_16x16x32_bf16 v[208:211], a[36:39], a[208:211], v[208:211]\n\t"
      "ds_read_b64_tr_b16 a[92:93], v23 offset:21792\n\t"
      "v_cvt_pk_bf16_f32 v75, v90, v91\n\t"
      "global_store_dwordx4 v28, v[72:75], %[hbq] offset:1024 sc1\n\t"
      "v_mfma_f32_16x16x32_bf16 v[212:215], a[36:39], a[212:215], v[212:215]\n\t"
      "v_mul_f32 v92, s40, v92\n\t"
      "v_mul_f32 v93, s40, v93\n\t"
      "v_mfma_f32_16x16x32_bf16 v[216:219], a[36:39], a[216:219], v[216:219]\n\t"
      "ds_read_b64_tr_b16 a[94:95], v23 offset:25376\n\t"
      "v_mul_f32 v94, s40, v94\n\t"
      "v_mul_f32 v95, s40, v95\n\t"
      "v_exp_f32 v92, v92\n\t"
      "v_mfma_f32_16x16x32_bf16 v[220:223], a[36:39], a[220:223], v[220:223]\n\t"
      "v_exp_f32 v93, v93\n\t"
      "v_exp_f32 v94, v94\n\t"
      "v_mfma_f32_16x16x32_bf16 v[96:99], a[40:43], a[224:227], v[96:99]\n\t"
      "v_exp_f32 v95, v95\n\t"
      "v_add_f32 v92, 1.0, v92\n\t"
      "v_mfma_f32_16x16x32_bf16 v[100:103], a[40:43], a[228:231], v[100:103]\n\t"
      "v_add_f32 v93, 1.0, v93\n\t"
      "v_add_f32 v94, 1.0, v94\n\t"
      "v_add_f32 v95, 1.0, v95\n\t"
      "v_mfma_f32_16x16x32_bf16 v[104:107], a[40:43], a[232:235], v[104:107]\n\t"
      "v_rcp_f32 v92, v92\n\t"
      "v_rcp_f32 v93, v93\n\t"
      "v_mfma_f32_16x16x32_bf16 v[108:111], a[40:43], a[236:239], v[108:111]\n\t"
      "v_rcp_f32 v94, v94\n\t"
      "v_rcp_f32 v95, v95\n\t"
      "v_mfma_f32_16x16x32_bf16 v[208:211], a[44:47], a[224:227], v[208:211]\n\t"
      "v_mul_f32 v92, v92, v204\n\t"
      "v_mul_f32 v93, v93, v205\n\t"
      "v_mul_f32 v94, v94, v206\n\t"
      "v_mfma_f32_16x16x32_bf16 v[212:215], a[44:47], a[228:231], v[212:215]\n\t"
      "v_mul_f32 v95, v95, v207\n\t"
      "v_cvt_pk_bf16_f32 v78, v92, v93\n\t"
      "v_mfma_f32_16x16x32_bf16 v[216:219], a[44:47], a[232:235], v[216:219]\n\t"
      "v_cvt_pk_bf16_f32 v79, v94, v95\n\t"
      "global_store_dwordx4 v29, v[76:79], %[hbq] offset:1024 sc1\n\t"
      "v_mfma_f32_16x16x32_bf16 v[220:223], a[44:47], a[236:239], v[220:223]\n\t"
      "s_waitcnt lgkmcnt(0)\n\t"
      "v_mfma_f32_16x16x32_bf16 v[112:115], a[48:51], a[144:147], v[112:115]\n\t"
      "ds_read_b64_tr_b16 a[0:1], v24 offset:192\n\t"
      "v_mfma_f32_16x16x32_bf16 v[116:119], a[48:51], a[148:151], v[116:119]\n\t"
      "ds_read_b64_tr_b16 a[2:3], v24 offset:3776\n\t"
      "v_mfma_f32_16x16x32_bf16 v[120:123], a[48:51], a[152:155], v[120:123]\n\t"
      "v_mfma_f32_16x16x32_bf16 v[124:127], a[48:51], a[156:159], v[124:127]\n\t"
      "ds_read_b64_tr_b16 a[4:5], v24 offset:14656\n\t"
      "v_mfma_f32_16x16x32_bf16 v[224:227], a[52:55], a[144:147], v[224:227]\n\t"
      "v_mfma_f32_16x16x32_bf16 v[228:231], a[52:55], a[148:151], v[228:231]\n\t"
      "ds_read_b64_tr_b16 a[6:7], v24 offset:18240\n\t"
      "v_mfma_f32_16x16x32_bf16 v[232:235], a[52:55], a[152:155], v[232:235]\n\t"
      "ds_read_b64_tr_b16 a[8:9], v24 offset:7360\n\t"
      "v_mfma_f32_16x16x32_bf16 v[236:239], a[52:55], a[156:159], v[236:239]\n\t"
      "v_mfma_f32_16x16x32_bf16 v[112:115], a[56:59], a[160:163], v[112:115]\n\t"
      "ds_read_b64_tr_b16 a[10:11], v24 offset:10944\n\t"
      "v_mul_f32 v96, s40, v96\n\t"
      "v_mul_f32 v97, s40, v97\n\t"
      "v_mul_f32 v98, s40, v98\n\t"
      "v_mfma_f32_16x16x32_bf16 v[116:119], a[56:59], a[164:167], v[116:119]\n\t"
      "v_mul_f32 v99, s40, v99\n\t"
      "v_exp_f32 v96, v96\n\t"
      "v_mfma_f32_16x16x32_bf16 v[120:123], a[56:59], a[168:171], v[120:123]\n\t"
      "ds_read_b64_tr_b16 a[12:13], v24 offset:21824\n\t"
      "v_exp_f32 v97, v97\n\t"
      "v_exp_f32 v98, v98\n\t"
      "v_mfma_f32_16x16x32_bf16 v[124:127], a[56:59], a[172:175], v[124:127]\n\t"
      "ds_read_b64_tr_b16 a[14:15], v24 offset:25408\n\t"
      "v_exp_f32 v99, v99\n\t"
      "v_add_f32 v96, 1.0, v96\n\t"
      "v_add_f32 v97, 1.0, v97\n\t"
      "v_mfma_f32_16x16x32_bf16 v[224:227], a[60:63], a[160:163], v[224:227]\n\t"
      "v_add_f32 v98, 1.0, v98\n\t"
      "v_add_f32 v99, 1.0, v99\n\t"
      "v_mfma_f32_16x16x32_bf16 v[228:231], a[60:63], a[164:167], v[228:231]\n\t"
      "ds_read_b64_tr_b16 a[16:17], v24 offset:29120\n\t"
      "v_rcp_f32 v96, v96\n\t"
      "v_rcp_f32 v97, v97\n\t"
      "v_mfma_f32_16x16x32_bf16 v[232:235], a[60:63], a[168:171], v[232:235]\n\t"
      "v_rcp_f32 v98, v98\n\t"
      "v_rcp_f32 v99, v99\n\t"
      "v_mfma_f32_16x16x32_bf16 v[236:239], a[60:63], a[172:175], v[236:239]\n\t"
      "ds_read_b64_tr_b16 a[18:19], v24 offset:32704\n\t"
      "v_mul_f32 v96, v96, v208\n\t"
      "v_mul_f32 v97, v97, v209\n\t"
      "v_mul_f32 v98, v98, v210\n\t"
      "v_mfma_f32_16x16x32_bf16 v[112:115], a[64:67], a[176:179], v[112:115]\n\t"
      "ds_read_b64_tr_b16 a[20:21], v24 offset:43584\n\t"
      "v_mul_f32 v99, v99, v211\n\t"
      "v_cvt_pk_bf16_f32 v96, v96, v97\n\t"
      "v_mfma_f32_16x16x32_bf16 v[116:119], a[64:67], a[180:183], v[116:119]\n\t"
      "v_cvt_pk_bf16_f32 v97, v98, v99\n\t"
      "v_mul_f32 v100, s40, v100\n\t"
      "v_mfma_f32_16x16x32_bf16 v[120:123], a[64:67], a[184:187], v[120:123]\n\t"
      "ds_read_b64_tr_b16 a[22:23], v24 offset:47168\n\t"
      "v_mul_f32 v101, s40, v101\n\t"
      "v_mul_f32 v102, s40, v102\n\t"
      "v_mfma_f32_16x16x32_bf16 v[124:127], a[64:67], a[188:191], v[124:127]\n\t"
      "v_mul_f32 v103, s40, v103\n\t"
      "v_exp_f32 v100, v100\n\t"
      "v_exp_f32 v101, v101\n\t"
      "v_mfma_f32_16x16x32_bf16 v[224:227], a[68:71], a[176:179], v[224:227]\n\t"
      "ds_read_b64_tr_b16 a[24:25], v24 offset:36288\n\t"
      "v_exp_f32 v102, v102\n\t"
      "v_exp_f32 v103, v103\n\t"
      "v_mfma_f32_16x16x32_bf16 v[228:231], a[68:71], a[180:183], v[228:231]\n\t"
      "ds_read_b64_tr_b16 a[26:27], v24 offset:39872\n\t"
      "v_add_f32 v100, 1.0, v100\n\t"
      "v_add_f32 v101, 1.0, v101\n\t"
      "v_mfma_f32_16x16x32_bf16 v[232:235], a[68:71], a[184:187], v[232:235]\n\t"
      "v_add_f32 v102, 1.0, v102\n\t"
      "v_add_f32 v103, 1.0, v103\n\t"
      "v_mfma_f32_16x16x32_bf16 v[236:239], a[68:71], a[188:191], v[236:239]\n\t"
      "ds_read_b64_tr_b16 a[28:29], v24 offset:50752\n\t"
      "v_rcp_f32 v100, v100\n\t"
      "v_rcp_f32 v101, v101\n\t"
      "v_rcp_f32 v102, v102\n\t"
      "v_mfma_f32_16x16x32_bf16 v[112:115], a[72:75], a[192:195], v[112:115]\n\t"
      "v_rcp_f32 v103, v103\n\t"
      "v_mul_f32 v100, v100, v212\n\t"
      "v_mfma_f32_16x16x32_bf16 v[116:119], a[72:75], a[196:199], v[116:119]\n\t"
      "ds_read_b64_tr_b16 a[30:31], v24 offset:54336\n\t"
      "v_mul_f32 v101, v101, v213\n\t"
      "v_mul_f32 v102, v102, v214\n\t"
      "v_mfma_f32_16x16x32_bf16 v[120:123], a[72:75], a[200:203], v[120:123]\n\t"
      "ds_read_b64_tr_b16 a[32:33], v23 offset:192\n\t"
      "v_mul_f32 v103, v103, v215\n\t"
      "v_cvt_pk_bf16_f32 v100, v100, v101\n\t"
      "v_mfma_f32_16x16x32_bf16 v[124:127], a[72:75], a[204:207], v[124:127]\n\t"
      "v_cvt_pk_bf16_f32 v101, v102, v103\n\t"
      "v_mul_f32 v104, s40, v104\n\t"
      "v_mul_f32 v105, s40, v105\n\t"
      "v_mfma_f32_16x16x32_bf16 v[224:227], a[76:79], a[192:195], v[224:227]\n\t"
      "ds_read_b64_tr_b16 a[34:35], v23 offset:3776\n\t"
      "v_mul_f32 v106, s40, v106\n\t"
      "v_mul_f32 v107, s40, v107\n\t"
      "v_mfma_f32_16x16x32_bf16 v[228:231], a[76:79], a[196:199], v[228:231]\n\t"
      "v_exp_f32 v104, v104\n\t"
      "v_exp_f32 v105, v105\n\t"
      "v_mfma_f32_16x16x32_bf16 v[232:235], a[76:79], a[200:203], v[232:235]\n\t"
      "ds_read_b64_tr_b16 a[36:37], v23 offset:14656\n\t"
      "v_exp_f32 v106, v106\n\t"
      "v_exp_f32 v107, v107\n\t"
      "v_mfma_f32_16x16x32_bf16 v[236:239], a[76:79], a[204:207], v[236:239]\n\t"
      "ds_read_b64_tr_b16 a[38:39], v23 offset:18240\n\t"
      "v_add_f32 v104, 1.0, v104\n\t"
      "v_add_f32 v105, 1.0, v105\n\t"
      "v_add_f32 v106, 1.0, v106\n\t"
      "v_mfma_f32_16x16x32_bf16 v[112:115], a[80:83], a[208:211], v[112:115]\n\t"
      "v_add_f32 v107, 1.0, v107\n\t"
      "v_rcp_f32 v104, v104\n\t"
      "v_mfma_f32_16x16x32_bf16 v[116:119], a[80:83], a[212:215], v[116:119]\n\t"
      "ds_read_b64_tr_b16 a[40:41], v23 offset:7360\n\t"
      "v_rcp_f32 v105, v105\n\t"
      "v_rcp_f32 v106, v106\n\t"
      "v_mfma_f32_16x16x32_bf16 v[120:123], a[80:83], a[216:219], v[120:123]\n\t"
      "v_rcp_f32 v107, v107\n\t"
      "v_mul_f32 v104, v104, v216\n\t"
      "v_mfma_f32_16x16x32_bf16 v[124:127], a[80:83], a[220:223], v[124:127]\n\t"
      "ds_read_b64_tr_b16 a[42:43], v23 offset:10944\n\t"
      "v_mul_f32 v105, v105, v217\n\t"
      "v_mul_f32 v106, v106, v218\n\t"
      "v_mul_f32 v107, v107, v219\n\t"
      "v_mfma_f32_16x16x32_bf16 v[224:227], a[84:87], a[208:211], v[224:227]\n\t"
      "ds_read_b64_tr_b16 a[44:45], v23 offset:21824\n\t"
      "v_cvt_pk_bf16_f32 v104, v104, v105\n\t"
      "v_cvt_pk_bf16_f32 v105, v106, v107\n\t"
      "v_mfma_f32_16x16x32_bf16 v[228:231], a[84:87], a[212:215], v[228:231]\n\t"
      "v_mul_f32 v108, s40, v108\n\t"
      "v_mul_f32 v109, s40, v109\n\t"
      "v_mfma_f32_16x16x32_bf16 v[232:235], a[84:87], a[216:219], v[232:235]\n\t"
      "ds_read_b64_tr_b16 a[46:47], v23 offset:25408\n\t"
      "v_mul_f32 v110, s40, v110\n\t"
      "v_mul_f32 v111, s40, v111\n\t"
      "v_mfma_f32_16x16x32_bf16 v[236:239], a[84:87], a[220:223], v[236:239]\n\t"
      "v_exp_f32 v108, v108\n\t"
      "v_exp_f32 v109, v109\n\t"
      "v_exp_f32 v110, v110\n\t"
      "v_mfma_f32_16x16x32_bf16 v[112:115], a[88:91], a[224:227], v[112:115]\n\t"
      "v_exp_f32 v111, v111\n\t"
      "v_add_f32 v108, 1.0, v108\n\t"
      "v_mfma_f32_16x16x32_bf16 v[116:119], a[88:91], a[228:231], v[116:119]\n\t"
      "v_add_f32 v109, 1.0, v109\n\t"
      "v_add_f32 v110, 1.0, v110\n\t"
      "v_mfma_f32_16x16x32_bf16 v[120:123], a[88:91], a[232:235], v[120:123]\n\t"
      "v_add_f32 v111, 1.0, v111\n\t"
      "v_rcp_f32 v108, v108\n\t"
      "v_mfma_f32_16x16x32_bf16 v[124:127], a[88:91], a[236:239], v[124:127]\n\t"
      "v_rcp_f32 v109, v109\n\t"
      "v_rcp_f32 v110, v110\n\t"
      "v_rcp_f32 v111, v111\n\t"
      "v_mfma_f32_16x16x32_bf16 v[224:227], a[92:95], a[224:227], v[224:227]\n\t"
      "v_mul_f32 v108, v108, v220\n\t"
      "v_mul_f32 v109, v109, v221\n\t"
      "v_mfma_f32_16x16x32_bf16 v[228:231], a[92:95], a[228:231], v[228:231]\n\t"
      "v_mul_f32 v110, v110, v222\n\t"
      "v_mul_f32 v111, v111, v223\n\t"
      "v_mfma_f32_16x16x32_bf16 v[232:235], a[92:95], a[232:235], v[232:235]\n\t"
      "v_cvt_pk_bf16_f32 v108, v108, v109\n\t"
      "v_cvt_pk_bf16_f32 v109, v110, v111\n\t"
      "v_mfma_f32_16x16x32_bf16 v[236:239], a[92:95], a[236:239], v[236:239]\n\t"
      "s_waitcnt lgkmcnt(0)\n\t"
      "v_mfma_f32_16x16x32_bf16 v[128:131], a[0:3], a[144:147], v[128:131]\n\t"
      "v_mfma_f32_16x16x32_bf16 v[132:135], a[0:3], a[148:151], v[132:135]\n\t"
      "v_mfma_f32_16x16x32_bf16 v[136:139], a[0:3], a[152:155], v[136:139]\n\t"
      "v_mfma_f32_16x16x32_bf16 v[140:143], a[0:3], a[156:159], v[140:143]\n\t"
      "v_mfma_f32_16x16x32_bf16 v[240:243], a[4:7], a[144:147], v[240:243]\n\t"
      "v_mfma_f32_16x16x32_bf16 v[244:247], a[4:7], a[148:151], v[244:247]\n\t"
      "v_mfma_f32_16x16x32_bf16 v[248:251], a[4:7], a[152:155], v[248:251]\n\t"
      "v_mfma_f32_16x16x32_bf16 v[252:255], a[4:7], a[156:159], v[252:255]\n\t"
      "v_mfma_f32_16x16x32_bf16 v[128:131], a[8:11], a[160:163], v[128:131]\n\t"
      "v_mul_f32 v112, s40, v112\n\t"
      "v_mul_f32 v113, s40, v113\n\t"
      "v_mul_f32 v114, s40, v114\n\t"
      "v_mfma_f32_16x16x32_bf16 v[132:135], a[8:11], a[164:167], v[132:135]\n\t"
      "v_mul_f32 v115, s40, v115\n\t"
      "v_exp_f32 v112, v112\n\t"
      "v_mfma_f32_16x16x32_bf16 v[136:139], a[8:11], a[168:171], v[136:139]\n\t"
      "v_exp_f32 v113, v113\n\t"
      "v_exp_f32 v114, v114\n\t"
      "v_exp_f32 v115, v115\n\t"
      "v_mfma_f32_16x16x32_bf16 v[140:143], a[8:11], a[172:175], v[140:143]\n\t"
      "v_add_f32 v112, 1.0, v112\n\t"
      "v_add_f32 v113, 1.0, v113\n\t"
      "v_mfma_f32_16x16x32_bf16 v[240:243], a[12:15], a[160:163], v[240:243]\n\t"
      "v_add_f32 v114, 1.0, v114\n\t"
      "v_add_f32 v115, 1.0, v115\n\t"
      "v_mfma_f32_16x16x32_bf16 v[244:247], a[12:15], a[164:167], v[244:247]\n\t"
      "v_rcp_f32 v112, v112\n\t"
      "v_rcp_f32 v113, v113\n\t"
      "v_rcp_f32 v114, v114\n\t"
      "v_mfma_f32_16x16x32_bf16 v[248:251], a[12:15], a[168:171], v[248:251]\n\t"
      "v_rcp_f32 v115, v115\n\t"
      "v_mul_f32 v112, v112, v224\n\t"
      "v_mfma_f32_16x16x32_bf16 v[252:255], a[12:15], a[172:175], v[252:255]\n\t"
      "v_mul_f32 v113, v113, v225\n\t"
      "v_mul_f32 v114, v114, v226\n\t"
      "v_mfma_f32_16x16x32_bf16 v[128:131], a[16:19], a[176:179], v[128:131]\n\t"
      "v_mul_f32 v115, v115, v227\n\t"
      "v_cvt_pk_bf16_f32 v98, v112, v113\n\t"
      "v_cvt_pk_bf16_f32 v99, v114, v115\n\t"
      "v_mfma_f32_16x16x32_bf16 v[132:135], a[16:19], a[180:183], v[132:135]\n\t"
      "global_store_dwordx4 v26, v[96:99], %[hbq] offset:2048 sc1\n\t"
      "v_mul_f32 v116, s40, v116\n\t"
      "v_mfma_f32_16x16x32_bf16 v[136:139], a[16:19], a[184:187], v[136:139]\n\t"
      "v_mul_f32 v117, s40, v117\n\t"
      "v_mul_f32 v118, s40, v118\n\t"
      "v_mfma_f32_16x16x32_bf16 v[140:143], a[16:19], a[188:191], v[140:143]\n\t"
      "v_mul_f32 v119, s40, v119\n\t"
      "v_exp_f32 v116, v116\n\t"
      "v_exp_f32 v117, v117\n\t"
      "v_mfma_f32_16x16x32_bf16 v[240:243], a[20:23], a[176:179], v[240:243]\n\t"
      "v_exp_f32 v118, v118\n\t"
      "v_exp_f32 v119, v119\n\t"
      "v_mfma_f32_16x16x32_bf16 v[244:247], a[20:23], a[180:183], v[244:247]\n\t"
      "v_add_f32 v116, 1.0, v116\n\t"
      "v_add_f32 v117, 1.0, v117\n\t"
      "v_add_f32 v118, 1.0, v118\n\t"
      "v_mfma_f32_16x16x32_bf16 v[248:251], a[20:23], a[184:187], v[248:251]\n\t"
      "v_add_f32 v119, 1.0, v119\n\t"
      "v_rcp_f32 v116, v116\n\t"
      "v_mfma_f32_16x16x32_bf16 v[252:255], a[20:23], a[188:191], v[252:255]\n\t"
      "v_rcp_f32 v117, v117\n\t"
      "v_rcp_f32 v118, v118\n\t"
      "v_mfma_f32_16x16x32_bf16 v[128:131], a[24:27], a[192:195], v[128:131]\n\t"
      "v_rcp_f32 v119, v119\n\t"
      "v_mul_f32 v116, v116, v228\n\t"
      "v_mul_f32 v117, v117, v229\n\t"
      "v_mfma_f32_16x16x32_bf16 v[132:135], a[24:27], a[196:199], v[132:135]\n\t"
      "v_mul_f32 v118, v118, v230\n\t"
      "v_mul_f32 v119, v119, v231\n\t"
      "v_mfma_f32_16x16x32_bf16 v[136:139], a[24:27], a[200:203], v[136:139]\n\t"
      "v_cvt_pk_bf16_f32 v102, v116, v117\n\t"
      "v_cvt_pk_bf16_f32 v103, v118, v119\n\t"
      "v_mfma_f32_16x16x32_bf16 v[140:143], a[24:27], a[204:207], v[140:143]\n\t"
      "global_store_dwordx4 v27, v[100:103], %[hbq] offset:2048 sc1\n\t"
      "v_mul_f32 v120, s40, v120\n\t"
      "v_mul_f32 v121, s40, v121\n\t"
      "v_mfma_f32_16x16x32_bf16 v[240:243], a[28:31], a[192:195], v[240:243]\n\t"
      "v_mul_f32 v122, s40, v122\n\t"
      "v_mul_f32 v123, s40, v123\n\t"
      "v_mfma_f32_16x16x32_bf16 v[244:247], a[28:31], a[196:199], v[244:247]\n\t"
      "v_exp_f32 v120, v120\n\t"
      "v_exp_f32 v121, v121\n\t"
      "v_mfma_f32_16x16x32_bf16 v[248:251], a[28:31], a[200:203], v[248:251]\n\t"
      "v_exp_f32 v122, v122\n\t"
      "v_exp_f32 v123, v123\n\t"
      "v_add_f32 v120, 1.0, v120\n\t"
      "v_mfma_f32_16x16x32_bf16 v[252:255], a[28:31], a[204:207], v[252:255]\n\t"
      "v_add_f32 v121, 1.0, v121\n\t"
      "v_add_f32 v122, 1.0, v122\n\t"
      "v_mfma_f32_16x16x32_bf16 v[128:131], a[32:35], a[208:211], v[128:131]\n\t"
      "v_add_f32 v123, 1.0, v123\n\t"
      "v_rcp_f32 v120, v120\n\t"
      "v_mfma_f32_16x16x32_bf16 v[132:135], a[32:35], a[212:215], v[132:135]\n\t"
      "v_rcp_f32 v121, v121\n\t"
      "v_rcp_f32 v122, v122\n\t"
      "v_rcp_f32 v123, v123\n\t"
      "v_mfma_f32_16x16x32_bf16 v[136:139], a[32:35], a[216:219], v[136:139]\n\t"
      "v_mul_f32 v120, v120, v232\n\t"
      "v_mul_f32 v121, v121, v233\n\t"
      "v_mfma_f32_16x16x32_bf16 v[140:143], a[32:35], a[220:223], v[140:143]\n\t"
      "v_mul_f32 v122, v122, v234\n\t"
      "v_mul_f32 v123, v123, v235\n\t"
      "v_cvt_pk_bf16_f32 v106, v120, v121\n\t"
      "v_mfma_f32_16x16x32_bf16 v[240:243], a[36:39], a[208:211], v[240:243]\n\t"
      "v_cvt_pk_bf16_f32 v107, v122, v123\n\t"
      "global_store_dwordx4 v28, v[104:107], %[hbq] offset:2048 sc1\n\t"
      "v_mfma_f32_16x16x32_bf16 v[244:247], a[36:39], a[212:215], v[244:247]\n\t"
      "v_mul_f32 v124, s40, v124\n\t"
      "v_mul_f32 v125, s40, v125\n\t"
      "v_mfma_f32_16x16x32_bf16 v[248:251], a[36:39], a[216:219], v[248:251]\n\t"
      "v_mul_f32 v126, s40, v126\n\t"
      "v_mul_f32 v127, s40, v127\n\t"
      "v_exp_f32 v124, v124\n\t"
      "v_mfma_f32_16x16x32_bf16 v[252:255], a[36:39], a[220:223], v[252:255]\n\t"
      "v_exp_f32 v125, v125\n\t"
      "v_exp_f32 v126, v126\n\t"
      "v_mfma_f32_16x16x32_bf16 v[128:131], a[40:43], a[224:227], v[128:131]\n\t"
      "v_exp_f32 v127, v127\n\t"
      "v_add_f32 v124, 1.0, v124\n\t"
      "v_mfma_f32_16x16x32_bf16 v[132:135], a[40:43], a[228:231], v[132:135]\n\t"
      "v_add_f32 v125, 1.0, v125\n\t"
      "v_add_f32 v126, 1.0, v126\n\t"
      "v_add_f32 v127, 1.0, v127\n\t"
      "v_mfma_f32_16x16x32_bf16 v[136:139], a[40:43], a[232:235], v[136:139]\n\t"
      "v_rcp_f32 v124, v124\n\t"
      "v_rcp_f32 v125, v125\n\t"
      "v_mfma_f32_16x16x32_bf16 v[140:143], a[40:43], a[236:239], v[140:143]\n\t"
      "v_rcp_f32 v126, v126\n\t"
      "v_rcp_f32 v127, v127\n\t"
      "v_mfma_f32_16x16x32_bf16 v[240:243], a[44:47], a[224:227], v[240:243]\n\t"
      "v_mul_f32 v124, v124, v236\n\t"
      "v_mul_f32 v125, v125, v237\n\t"
      "v_mul_f32 v126, v126, v238\n\t"
      "v_mfma_f32_16x16x32_bf16 v[244:247], a[44:47], a[228:231], v[244:247]\n\t"
      "v_mul_f32 v127, v127, v239\n\t"
      "v_cvt_pk_bf16_f32 v110, v124, v125\n\t"
      "v_mfma_f32_16x16x32_bf16 v[248:251], a[44:47], a[232:235], v[248:251]\n\t"
      "v_cvt_pk_bf16_f32 v111, v126, v127\n\t"
      "global_store_dwordx4 v29, v[108:111], %[hbq] offset:2048 sc1\n\t"
      "v_mfma_f32_16x16x32_bf16 v[252:255], a[44:47], a[236:239], v[252:255]\n\t"
      "s_nop 7\n\t"
      "v_mul_f32 v128, s40, v128\n\t"
      "v_mul_f32 v129, s40, v129\n\t"
      "v_mul_f32 v130, s40, v130\n\t"
      "v_mul_f32 v131, s40, v131\n\t"
      "v_exp_f32 v128, v128\n\t"
      "v_exp_f32 v129, v129\n\t"
      "v_exp_f32 v130, v130\n\t"
      "v_exp_f32 v131, v131\n\t"
      "v_add_f32 v128, 1.0, v128\n\t"
      "v_add_f32 v129, 1.0, v129\n\t"
      "v_add_f32 v130, 1.0, v130\n\t"
      "v_add_f32 v131, 1.0, v131\n\t"
      "v_rcp_f32 v128, v128\n\t"
      "v_rcp_f32 v129, v129\n\t"
      "v_rcp_f32 v130, v130\n\t"
      "v_rcp_f32 v131, v131\n\t"
      "v_mul_f32 v128, v128, v240\n\t"
      "v_mul_f32 v129, v129, v241\n\t"
      "v_mul_f32 v130, v130, v242\n\t"
      "v_mul_f32 v131, v131, v243\n\t"
      "v_cvt_pk_bf16_f32 v128, v128, v129\n\t"
      "v_cvt_pk_bf16_f32 v129, v130, v131\n\t"
      "global_store_dwordx2 v26, v[128:129], %[hbs] sc1\n\t"
      "v_mul_f32 v132, s40, v132\n\t"
      "v_mul_f32 v133, s40, v133\n\t"
      "v_mul_f32 v134, s40, v134\n\t"
      "v_mul_f32 v135, s40, v135\n\t"
      "v_exp_f32 v132, v132\n\t"
      "v_exp_f32 v133, v133\n\t"
      "v_exp_f32 v134, v134\n\t"
      "v_exp_f32 v135, v135\n\t"
      "v_add_f32 v132, 1.0, v132\n\t"
      "v_add_f32 v133, 1.0, v133\n\t"
      "v_add_f32 v134, 1.0, v134\n\t"
      "v_add_f32 v135, 1.0, v135\n\t"
      "v_rcp_f32 v132, v132\n\t"
      "v_rcp_f32 v133, v133\n\t"
      "v_rcp_f32 v134, v134\n\t"
      "v_rcp_f32 v135, v135\n\t"
      "v_mul_f32 v132, v132, v244\n\t"
      "v_mul_f32 v133, v133, v245\n\t"
      "v_mul_f32 v134, v134, v246\n\t"
      "v_mul_f32 v135, v135, v247\n\t"
      "v_cvt_pk_bf16_f32 v132, v132, v133\n\t"
      "v_cvt_pk_bf16_f32 v133, v134, v135\n\t"
      "global_store_dwordx2 v27, v[132:133], %[hbs] sc1\n\t"
      "v_mul_f32 v136, s40, v136\n\t"
      "v_mul_f32 v137, s40, v137\n\t"
      "v_mul_f32 v138, s40, v138\n\t"
      "v_mul_f32 v139, s40, v139\n\t"
      "v_exp_f32 v136, v136\n\t"
      "v_exp_f32 v137, v137\n\t"
      "v_exp_f32 v138, v138\n\t"
      "v_exp_f32 v139, v139\n\t"
      "v_add_f32 v136, 1.0, v136\n\t"
      "v_add_f32 v137, 1.0, v137\n\t"
      "v_add_f32 v138, 1.0, v138\n\t"
      "v_add_f32 v139, 1.0, v139\n\t"
      "v_rcp_f32 v136, v136\n\t"
      "v_rcp_f32 v137, v137\n\t"
      "v_rcp_f32 v138, v138\n\t"
      "v_rcp_f32 v139, v139\n\t"
      "v_mul_f32 v136, v136, v248\n\t"
      "v_mul_f32 v137, v137, v249\n\t"
      "v_mul_f32 v138, v138, v250\n\t"
      "v_mul_f32 v139, v139, v251\n\t"
      "v_cvt_pk_bf16_f32 v136, v136, v137\n\t"
      "v_cvt_pk_bf16_f32 v137, v138, v139\n\t"
      "global_store_dwordx2 v28, v[136:137], %[hbs] sc1\n\t"
      "v_mul_f32 v140, s40, v140\n\t"
      "v_mul_f32 v141, s40, v141\n\t"
      "v_mul_f32 v142, s40, v142\n\t"
      "v_mul_f32 v143, s40, v143\n\t"
      "v_exp_f32 v140, v140\n\t"
      "v_exp_f32 v141, v141\n\t"
      "v_exp_f32 v142, v142\n\t"
      "v_exp_f32 v143, v143\n\t"
      "v_add_f32 v140, 1.0, v140\n\t"
      "v_add_f32 v141, 1.0, v141\n\t"
      "v_add_f32 v142, 1.0, v142\n\t"
      "v_add_f32 v143, 1.0, v143\n\t"
      "v_rcp_f32 v140, v140\n\t"
      "v_rcp_f32 v141, v141\n\t"
      "v_rcp_f32 v142, v142\n\t"
      "v_rcp_f32 v143, v143\n\t"
      "v_mul_f32 v140, v140, v252\n\t"
      "v_mul_f32 v141, v141, v253\n\t"
      "v_mul_f32 v142, v142, v254\n\t"
      "v_mul_f32 v143, v143, v255\n\t"
      "v_cvt_pk_bf16_f32 v140, v140, v141\n\t"
      "v_cvt_pk_bf16_f32 v141, v142, v143\n\t"
      "global_store_dwordx2 v29, v[140:141], %[hbs] sc1\n\t"
      "s_waitcnt vmcnt(16)\n\t"
      "s_nop 7\n\t"
      "s_nop 7\n\t"
        : "=&{v[32:47]}"(acc[0]), "=&{v[48:63]}"(acc[1]), "=&{v[64:79]}"(acc[2]), "=&{v[80:95]}"(acc[3]), "=&{v[96:111]}"(acc[4]), "=&{v[112:127]}"(acc[5]), "=&{v[128:143]}"(acc[6]), "=&{v[144:159]}"(acc[7]), "=&{v[160:175]}"(acc[8]), "=&{v[176:191]}"(acc[9]), "=&{v[192:207]}"(acc[10]), "=&{v[208:223]}"(acc[11]), "=&{v[224:239]}"(acc[12]), "=&{v[240:255]}"(acc[13])
        : [wb] "s"(wb + 2 * 0x70000), [xb] "s"(xb), [wr0] "s"(wr0), [ldsb] "s"(ldsb), [lane] "v"(lane), [rot] "s"(2 * (ntile & 1)),
          [mu] "s"(mu), [mc] "s"(mc), [ub] "s"(ub), [srd] "s"(srd), [wsrd] "s"(wsrd), [wpo] "s"(wpo),
          [fu] "s"(fu), [fur] "s"(fur), [eplo] "s"(eplo), [ephi] "s"(ephi), [tmo] "s"(tmo), [hbq] "s"(hbq), [hbs] "s"(hbs),
          [pma] "s"(a.ws + WS_PART + ((size_t)b * 4 + w) * 8192), [sent] "s"(SENTINEL), [kvl] "v"(kvl), [kvo] "v"(kvo), [kvb] "s"(kvb),
          [pfw] "s"((const char*)a.wd + (size_t)(b * 4 + w) * 7168)
        : "vcc", "v12", "v13", "v14", "v15", "v16", "v17", "v18", "v19", "v20", "v21", "v22", "v23", "v24", "v25", "v26", "v27", "v28", "v29", "v30", "v31", "a0", "a1", "a2", "a3", "a4", "a5", "a6", "a7", "a8", "a9", "a10", "a11", "a12", "a13", "a14", "a15", "a16", "a17", "a18", "a19", "a20", "a21", "a22", "a23", "a24", "a25", "a26", "a27", "a28", "a29", "a30", "a31", "a32", "a33", "a34", "a35", "a36", "a37", "a38", "a39", "a40", "a41", "a42", "a43", "a44", "a45", "a46", "a47", "a48", "a49", "a50", "a51", "a52", "a53", "a54", "a55", "a56", "a57", "a58", "a59", "a60", "a61", "a62", "a63", "a64", "a65", "a66", "a67", "a68", "a69", "a70", "a71", "a72", "a73", "a74", "a75", "a76", "a77", "a78", "a79", "a80", "a81", "a82", "a83", "a84", "a85", "a86", "a87", "a88", "a89", "a90", "a91", "a92", "a93", "a94", "a95", "a96", "a97", "a98", "a99", "a100", "a101", "a102", "a103", "a104", "a105", "a106", "a107", "a108", "a109", "a110", "a111", "a112", "a113", "a114", "a115", "a116", "a117", "a118", "a119", "a120", "a121", "a122", "a123", "a124", "a125", "a126", "a127", "a128", "a129", "a130", "a131", "a132", "a133", "a134", "a135", "a136", "a137", "a138", "a139", "a140", "a141", "a142", "a143", "a144", "a145", "a146", "a147", "a148", "a149", "a150", "a151", "a152", "a153", "a154", "a155", "a156", "a157", "a158", "a159", "a160", "a161", "a162", "a163", "a164", "a165", "a166", "a167", "a168", "a169", "a170", "a171", "a172", "a173", "a174", "a175", "a176", "a177", "a178", "a179", "a180", "a181", "a182", "a183", "a184", "a185", "a186", "a187", "a188", "a189", "a190", "a191", "a192", "a193", "a194", "a195", "a196", "a197", "a198", "a199", "a200", "a201", "a202", "a203", "a204", "a205", "a206", "a207", "a208", "a209", "a210", "a211", "a212", "a213", "a214", "a215", "a216", "a217", "a218", "a219", "a220", "a221", "a222", "a223", "a224", "a225", "a226", "a227", "a228", "a229", "a230", "a231", "a232", "a233", "a234", "a235", "a236", "a237", "a238", "a239", "a240", "a241", "a242", "a243", "a244", "a245", "a246", "a247", "a248", "a249", "a250", "a251", "a252", "a253", "a254", "a255", "s40", "s41", "s42", "s43", "s44", "s45", "s46", "s47", "s48", "s49", "s50", "s51", "s52", "s53", "s54", "s55", "s56", "s57", "s58", "s59", "s60", "s61", "s62", "s63", "s64", "s65", "s66", "s67", "s68", "memory", "scc");
    }
    STAMP(1);
  }
  {
    BAR();
    LANE_VARS();
    char* ur = lds + L_UR + w * 4608;
    char* co = lds + L_CO + w * 8448;
    char* ce = lds + L_CE + w * 8448;
    *(u32x2*)(ur + 4096 + lane * 8) = (u32x2){0u, 0u};
    if (lane < 8) *(u32x4*)(((lane & 4) ? ce : co) + ((lane >> 1) & 1) * 4224 + (lane & 1) * 16) = (u32x4){0u, 0u, 0u, 0u};
    {
      const int sg = (lane & 4) ? 3 : 1, ii = lane & 3;
      const unsigned tap = ((ii & 2) ? tp23 : tp01) >> ((ii & 1) * 16);
      if (lane < 8 && ii <= sg) *(u16*)(co + (sg >> 1) * 4224 + (15 - sg + ii) * 2) = (u16)tap;
      asm volatile("s_waitcnt lgkmcnt(0)" ::: "memory");
      u32x4 q[2][5]; unsigned pd[2][5];
#pragma unroll
      for (int n = 0; n < 2; ++n)
#pragma unroll
        for (int i = 0; i < 5; ++i) {
          const int c = i < 4 ? 1 + 64 * i + lane : 257;
          const char* src = co + n * 4224 + c * 16;
          q[n][i] = *(const u32x4*)src; pd[n][i] = *(const unsigned*)(src - 4);
        }
#pragma unroll
      for (int n = 0; n < 2; ++n)
#pragma unroll
        for (int i = 0; i < 5; ++i) {
          const int c = i < 4 ? 1 + 64 * i + lane : 257;
          const u32x4 v = q[n][i];
          *(u32x4*)(ce + n * 4224 + c * 16) = (u32x4){__builtin_amdgcn_alignbyte(v[0], pd[n][i], 2), __builtin_amdgcn_alignbyte(v[1], v[0], 2),
                                                      __builtin_amdgcn_alignbyte(v[2], v[1], 2), __builtin_amdgcn_alignbyte(v[3], v[2], 2)};
        }
    }
    STAMP(2);
    DRAIN();
    BAR();
    if (w == 0 && lane == 0) st_flag(a.ws + WS_FH + (mt * 32 + ntile) * 8, ep);
    const unsigned vg = ldsb + L_UR + w * 4608 + 480 - 32 * i16 + 16 * g;
    const unsigned vf = ldsb + ((i16 & 1) ? L_CO : L_CE) + w * 8448 + ((i16 >> 1) & 1) * 4224 + 16 * g + 2 * (i16 & 12);
    const unsigned l8 = i16 * 8;
    asm volatile(
      "s_waitcnt lgkmcnt(0)\n\t"
      "s_mov_b64 s[44:45], %[wb]\n\t"
      "s_mov_b32 s46, %[m0b]\n\t"
      "v_lshrrev_b32 v14, 4, %[lane]\n\t"
      "v_and_b32 v15, 15, %[lane]\n\t"
      "v_add_u32 v13, 0, v14\n\t"
      "v_lshlrev_b32 v24, 1, v13\n\t"
      "v_and_b32 v24, 14, v24\n\t"
      "v_xor_b32 v24, v24, v15\n\t"
      "v_lshlrev_b32 v24, 4, v24\n\t"
      "v_lshl_add_u32 v24, v13, 11, v24\n\t"
      "v_add_u32 v13, 4, v14\n\t"
      "v_lshlrev_b32 v25, 1, v13\n\t"
      "v_and_b32 v25, 14, v25\n\t"
      "v_xor_b32 v25, v25, v15\n\t"
      "v_lshlrev_b32 v25, 4, v25\n\t"
      "v_lshl_add_u32 v25, v13, 11, v25\n\t"
      "v_add_u32 v13, 8, v14\n\t"
      "v_lshlrev_b32 v26, 1, v13\n\t"
      "v_and_b32 v26, 14, v26\n\t"
      "v_xor_b32 v26, v26, v15\n\t"
      "v_lshlrev_b32 v26, 4, v26\n\t"
      "v_lshl_add_u32 v26, v13, 11, v26\n\t"
      "v_add_u32 v13, 12, v14\n\t"
      "v_lshlrev_b32 v27, 1, v13\n\t"
      "v_and_b32 v27, 14, v27\n\t"
      "v_xor_b32 v27, v27, v15\n\t"
      "v_lshlrev_b32 v27, 4, v27\n\t"
      "v_lshl_add_u32 v27, v13, 11, v27\n\t"
      "v_lshlrev_b32 v16, 4, %[lane]\n\t"
      "v_add_u32 v18, %[o23], v16\n\t"
      "v_add_u32 v16, %[o01], v16\n\t"
      "v_add_u32 v17, 0x1c000, v16\n\t"
      "v_add_u32 v19, 0x1c000, v18\n\t"
      "ds_read_b64 v[64:65], %[vf] offset:0\n\t"
      "ds_read_b64 v[66:67], %[vf] offset:8\n\t"
      "ds_read_b128 a[0:3], %[vg] offset:3584\n\t"
      "ds_read_b128 a[32:35], %[vg] offset:3072\n\t"
      "ds_read_b128 a[64:67], %[vg] offset:2560\n\t"
      "ds_read_b128 a[96:99], %[vg] offset:2048\n\t"
      "ds_read_b128 a[128:131], %[vg] offset:1536\n\t"
      "ds_read_b128 a[160:163], %[vg] offset:1024\n\t"
      "ds_read_b128 a[192:195], %[vg] offset:512\n\t"
      "ds_read_b128 a[224:227], %[vg] offset:0\n\t"
      "ds_read_b64 v[68:69], %[vf] offset:64\n\t"
      "ds_read_b64 v[70:71], %[vf] offset:72\n\t"
      "ds_read_b128 a[4:7], %[vg] offset:3648\n\t"
      "ds_read_b128 a[36:39], %[vg] offset:3136\n\t"
      "ds_read_b128 a[68:71], %[vg] offset:2624\n\t"
      "ds_read_b128 a[100:103], %[vg] offset:2112\n\t"
      "ds_read_b128 a[132:135], %[vg] offset:1600\n\t"
      "ds_read_b128 a[164:167], %[vg] offset:1088\n\t"
      "ds_read_b128 a[196:199], %[vg] offset:576\n\t"
      "ds_read_b128 a[228:231], %[vg] offset:64\n\t"
      "s_waitcnt lgkmcnt(10)\n\t"
      "v_mfma_f32_16x16x32_bf16 v[32:35], v[64:67], a[0:3], 0\n\t"
      "ds_read_b64 v[72:73], %[vf] offset:128\n\t"
      "ds_read_b64 v[74:75], %[vf] offset:136\n\t"
      "v_mfma_f32_16x16x32_bf16 v[36:39], v[64:67], a[32:35], 0\n\t"
      "ds_read_b128 a[8:11], %[vg] offset:3712\n\t"
      "ds_read_b128 a[40:43], %[vg] offset:3200\n\t"
      "v_mfma_f32_16x16x32_bf16 v[40:43], v[64:67], a[64:67], 0\n\t"
      "ds_read_b128 a[72:75], %[vg] offset:2688\n\t"
      "ds_read_b128 a[104:107], %[vg] offset:2176\n\t"
      "v_mfma_f32_16x16x32_bf16 v[44:47], v[64:67], a[96:99], 0\n\t"
      "ds_read_b128 a[136:139], %[vg] offset:1664\n\t"
      "ds_read_b128 a[168:171], %[vg] offset:1152\n\t"
      "v_mfma_f32_16x16x32_bf16 v[48:51], v[64:67], a[128:131], 0\n\t"
      "ds_read_b128 a[200:203], %[vg] offset:640\n\t"
      "v_mfma_f32_16x16x32_bf16 v[52:55], v[64:67], a[160:163], 0\n\t"
      "ds_read_b128 a[232:235], %[vg] offset:128\n\t"
      "v_mfma_f32_16x16x32_bf16 v[56:59], v[64:67], a[192:195], 0\n\t"
      "ds_read_b64 v[96:97], %[vf] offset:512\n\t"
      "v_mfma_f32_16x16x32_bf16 v[60:63], v[64:67], a[224:227], 0\n\t"
      "ds_read_b64 v[98:99], %[vf] offset:520\n\t"
      "s_waitcnt lgkmcnt(12)\n\t"
      "v_mfma_f32_16x16x32_bf16 v[32:35], v[68:71], a[4:7], v[32:35]\n\t"
      "ds_read_b64 v[76:77], %[vf] offset:192\n\t"
      "ds_read_b64 v[78:79], %[vf] offset:200\n\t"
      "v_mfma_f32_16x16x32_bf16 v[36:39], v[68:71], a[36:39], v[36:39]\n\t"
      "ds_read_b128 a[12:15], %[vg] offset:3776\n\t"
      "ds_read_b128 a[44:47], %[vg] offset:3264\n\t"
      "v_mfma_f32_16x16x32_bf16 v[40:43], v[68:71], a[68:71], v[40:43]\n\t"
      "ds_read_b128 a[76:79], %[vg] offset:2752\n\t"
      "ds_read_b128 a[108:111], %[vg] offset:2240\n\t"
      "v_mfma_f32_16x16x32_bf16 v[44:47], v[68:71], a[100:103], v[44:47]\n\t"
      "ds_read_b128 a[140:143], %[vg] offset:1728\n\t"
      "ds_read_b128 a[172:175], %[vg] offset:1216\n\t"
      "v_mfma_f32_16x16x32_bf16 v[48:51], v[68:71], a[132:135], v[48:51]\n\t"
      "ds_read_b128 a[204:207], %[vg] offset:704\n\t"
      "v_mfma_f32_16x16x32_bf16 v[52:55], v[68:71], a[164:167], v[52:55]\n\t"
      "ds_read_b128 a[236:239], %[vg] offset:192\n\t"
      "v_mfma_f32_16x16x32_bf16 v[56:59], v[68:71], a[196:199], v[56:59]\n\t"
      "ds_read_b64 v[100:101], %[vf] offset:576\n\t"
      "v_mfma_f32_16x16x32_bf16 v[60:63], v[68:71], a[228:231], v[60:63]\n\t"
      "ds_read_b64 v[102:103], %[vf] offset:584\n\t"
      "s_waitcnt lgkmcnt(14)\n\t"
      "v_mfma_f32_16x16x32_bf16 v[32:35], v[72:75], a[8:11], v[32:35]\n\t"
      "ds_read_b64 v[80:81], %[vf] offset:256\n\t"
      "ds_read_b64 v[82:83], %[vf] offset:264\n\t"
      "v_mfma_f32_16x16x32_bf16 v[36:39], v[72:75], a[40:43], v[36:39]\n\t"
      "ds_read_b128 a[16:19], %[vg] offset:3840\n\t"
      "ds_read_b128 a[48:51], %[vg] offset:3328\n\t"
      "v_mfma_f32_16x16x32_bf16 v[40:43], v[72:75], a[72:75], v[40:43]\n\t"
      "ds_read_b128 a[80:83], %[vg] offset:2816\n\t"
      "ds_read_b128 a[112:115], %[vg] offset:2304\n\t"
      "v_mfma_f32_16x16x32_bf16 v[44:47], v[72:75], a[104:107], v[44:47]\n\t"
      "ds_read_b128 a[144:147], %[vg] offset:1792\n\t"
      "ds_read_b128 a[176:179], %[vg] offset:1280\n\t"
      "v_mfma_f32_16x16x32_bf16 v[48:51], v[72:75], a[136:139], v[48:51]\n\t"
      "ds_read_b128 a[208:211], %[vg] offset:768\n\t"
      "v_mfma_f32_16x16x32_bf16 v[52:55], v[72:75], a[168:171], v[52:55]\n\t"
      "ds_read_b128 a[240:243], %[vg] offset:256\n\t"
      "v_mfma_f32_16x16x32_bf16 v[56:59], v[72:75], a[200:203], v[56:59]\n\t"
      "ds_read_b64 v[104:105], %[vf] offset:640\n\t"
      "v_mfma_f32_16x16x32_bf16 v[60:63], v[72:75], a[232:235], v[60:63]\n\t"
      "ds_read_b64 v[106:107], %[vf] offset:648\n\t"
      "s_waitcnt lgkmcnt(14)\n\t"
      "v_mfma_f32_16x16x32_bf16 v[32:35], v[76:79], a[12:15], v[32:35]\n\t"
      "ds_read_b64 v[84:85], %[vf] offset:320\n\t"
      "ds_read_b64 v[86:87], %[vf] offset:328\n\t"
      "v_mfma_f32_16x16x32_bf16 v[36:39], v[76:79], a[44:47], v[36:39]\n\t"
      "ds_read_b128 a[20:23], %[vg] offset:3904\n\t"
      "ds_read_b128 a[52:55], %[vg] offset:3392\n\t"
      "v_mfma_f32_16x16x32_bf16 v[40:43], v[76:79], a[76:79], v[40:43]\n\t"
      "ds_read_b128 a[84:87], %[vg] offset:2880\n\t"
      "ds_read_b128 a[116:119], %[vg] offset:2368\n\t"
      "v_mfma_f32_16x16x32_bf16 v[44:47], v[76:79], a[108:111], v[44:47]\n\t"
      "ds_read_b128 a[148:151], %[vg] offset:1856\n\t"
      "ds_read_b128 a[180:183], %[vg] offset:1344\n\t"
      "v_mfma_f32_16x16x32_bf16 v[48:51], v[76:79], a[140:143], v[48:51]\n\t"
      "ds_read_b128 a[212:215], %[vg] offset:832\n\t"
      "v_mfma_f32_16x16x32_bf16 v[52:55], v[76:79], a[172:175], v[52:55]\n\t"
      "ds_read_b128 a[244:247], %[vg] offset:320\n\t"
      "v_mfma_f32_16x16x32_bf16 v[56:59], v[76:79], a[204:207], v[56:59]\n\t"
      "ds_read_b64 v[108:109], %[vf] offset:704\n\t"
      "v_mfma_f32_16x16x32_bf16 v[60:63], v[76:79], a[236:239], v[60:63]\n\t"
      "ds_read_b64 v[110:111], %[vf] offset:712\n\t"
      "s_waitcnt lgkmcnt(14)\n\t"
      "v_mfma_f32_16x16x32_bf16 v[32:35], v[80:83], a[16:19], v[32:35]\n\t"
      "ds_read_b64 v[88:89], %[vf] offset:384\n\t"
      "ds_read_b64 v[90:91], %[vf] offset:392\n\t"
      "v_mfma_f32_16x16x32_bf16 v[36:39], v[80:83], a[48:51], v[36:39]\n\t"
      "ds_read_b128 a[24:27], %[vg] offset:3968\n\t"
      "ds_read_b128 a[56:59], %[vg] offset:3456\n\t"
      "v_mfma_f32_16x16x32_bf16 v[40:43], v[80:83], a[80:83], v[40:43]\n\t"
      "ds_read_b128 a[88:91], %[vg] offset:2944\n\t"
      "ds_read_b128 a[120:123], %[vg] offset:2432\n\t"
      "v_mfma_f32_16x16x32_bf16 v[44:47], v[80:83], a[112:115], v[44:47]\n\t"
      "ds_read_b128 a[152:155], %[vg] offset:1920\n\t"
      "ds_read_b128 a[184:187], %[vg] offset:1408\n\t"
      "v_mfma_f32_16x16x32_bf16 v[48:51], v[80:83], a[144:147], v[48:51]\n\t"
      "ds_read_b128 a[216:219], %[vg] offset:896\n\t"
      "v_mfma_f32_16x16x32_bf16 v[52:55], v[80:83], a[176:179], v[52:55]\n\t"
      "ds_read_b128 a[248:251], %[vg] offset:384\n\t"
      "v_mfma_f32_16x16x32_bf16 v[56:59], v[80:83], a[208:211], v[56:59]\n\t"
      "ds_read_b64 v[112:113], %[vf] offset:768\n\t"
      "v_mfma_f32_16x16x32_bf16 v[60:63], v[80:83], a[240:243], v[60:63]\n\t"
      "ds_read_b64 v[114:115], %[vf] offset:776\n\t"
      "s_waitcnt lgkmcnt(14)\n\t"
      "v_mfma_f32_16x16x32_bf16 v[32:35], v[84:87], a[20:23], v[32:35]\n\t"
      "ds_read_b64 v[92:93], %[vf] offset:448\n\t"
      "ds_read_b64 v[94:95], %[vf] offset:456\n\t"
      "v_mfma_f32_16x16x32_bf16 v[36:39], v[84:87], a[52:55], v[36:39]\n\t"
      "ds_read_b128 a[28:31], %[vg] offset:4032\n\t"
      "ds_read_b128 a[60:63], %[vg] offset:3520\n\t"
      "v_mfma_f32_16x16x32_bf16 v[40:43], v[84:87], a[84:87], v[40:43]\n\t"
      "ds_read_b128 a[92:95], %[vg] offset:3008\n\t"
      "ds_read_b128 a[124:127], %[vg] offset:2496\n\t"
      "v_mfma_f32_16x16x32_bf16 v[44:47], v[84:87], a[116:119], v[44:47]\n\t"
      "ds_read_b128 a[156:159], %[vg] offset:1984\n\t"
      "ds_read_b128 a[188:191], %[vg] offset:1472\n\t"
      "v_mfma_f32_16x16x32_bf16 v[48:51], v[84:87], a[148:151], v[48:51]\n\t"
      "ds_read_b128 a[220:223], %[vg] offset:960\n\t"
      "v_mfma_f32_16x16x32_bf16 v[52:55], v[84:87], a[180:183], v[52:55]\n\t"
      "ds_read_b128 a[252:255], %[vg] offset:448\n\t"
      "v_mfma_f32_16x16x32_bf16 v[56:59], v[84:87], a[212:215], v[56:59]\n\t"
      "ds_read_b64 v[116:117], %[vf] offset:832\n\t"
      "v_mfma_f32_16x16x32_bf16 v[60:63], v[84:87], a[244:247], v[60:63]\n\t"
      "ds_read_b64 v[118:119], %[vf] offset:840\n\t"
      "s_waitcnt lgkmcnt(14)\n\t"
      "v_mfma_f32_16x16x32_bf16 v[32:35], v[88:91], a[24:27], v[32:35]\n\t"
      "ds_read_b64 v[120:121], %[vf] offset:896\n\t"
      "v_mfma_f32_16x16x32_bf16 v[36:39], v[88:91], a[56:59], v[36:39]\n\t"
      "ds_read_b64 v[122:123], %[vf] offset:904\n\t"
      "v_mfma_f32_16x16x32_bf16 v[40:43], v[88:91], a[88:91], v[40:43]\n\t"
      "v_mfma_f32_16x16x32_bf16 v[44:47], v[88:91], a[120:123], v[44:47]\n\t"
      "v_mfma_f32_16x16x32_bf16 v[48:51], v[88:91], a[152:155], v[48:51]\n\t"
      "v_mfma_f32_16x16x32_bf16 v[52:55], v[88:91], a[184:187], v[52:55]\n\t"
      "v_mfma_f32_16x16x32_bf16 v[56:59], v[88:91], a[216:219], v[56:59]\n\t"
      "v_mfma_f32_16x16x32_bf16 v[60:63], v[88:91], a[248:251], v[60:63]\n\t"
      "s_waitcnt lgkmcnt(4)\n\t"
      "v_mfma_f32_16x16x32_bf16 v[32:35], v[92:95], a[28:31], v[32:35]\n\t"
      "ds_read_b64 v[124:125], %[vf] offset:960\n\t"
      "v_mfma_f32_16x16x32_bf16 v[36:39], v[92:95], a[60:63], v[36:39]\n\t"
      "ds_read_b64 v[126:127], %[vf] offset:968\n\t"
      "v_mfma_f32_16x16x32_bf16 v[40:43], v[92:95], a[92:95], v[40:43]\n\t"
      "v_mfma_f32_16x16x32_bf16 v[44:47], v[92:95], a[124:127], v[44:47]\n\t"
      "v_mfma_f32_16x16x32_bf16 v[48:51], v[92:95], a[156:159], v[48:51]\n\t"
      "v_mfma_f32_16x16x32_bf16 v[52:55], v[92:95], a[188:191], v[52:55]\n\t"
      "v_mfma_f32_16x16x32_bf16 v[56:59], v[92:95], a[220:223], v[56:59]\n\t"
      "v_mfma_f32_16x16x32_bf16 v[60:63], v[92:95], a[252:255], v[60:63]\n\t"
      "s_waitcnt lgkmcnt(15)\n\t"
      "v_mfma_f32_16x16x32_bf16 v[36:39], v[96:99], a[0:3], v[36:39]\n\t"
      "ds_read_b64 v[64:65], %[vf] offset:1024\n\t"
      "v_mfma_f32_16x16x32_bf16 v[40:43], v[96:99], a[32:35], v[40:43]\n\t"
      "ds_read_b64 v[66:67], %[vf] offset:1032\n\t"
      "v_mfma_f32_16x16x32_bf16 v[44:47], v[96:99], a[64:67], v[44:47]\n\t"
      "s_add_u32 m0, s46, 0\n\t"
      "s_nop 0\n\t"
      "global_load_lds_dwordx4 v24, s[44:45]\n\t"
      "s_add_u32 m0, m0, 0x400\n\t"
      "v_mfma_f32_16x16x32_bf16 v[48:51], v[96:99], a[96:99], v[48:51]\n\t"
      "v_mfma_f32_16x16x32_bf16 v[52:55], v[96:99], a[128:131], v[52:55]\n\t"
      "v_mfma_f32_16x16x32_bf16 v[56:59], v[96:99], a[160:163], v[56:59]\n\t"
      "v_mfma_f32_16x16x32_bf16 v[60:63], v[96:99], a[192:195], v[60:63]\n\t"
      "s_waitcnt lgkmcnt(15)\n\t"
      "v_mfma_f32_16x16x32_bf16 v[36:39], v[100:103], a[4:7], v[36:39]\n\t"
      "ds_read_b64 v[68:69], %[vf] offset:1088\n\t"
      "v_mfma_f32_16x16x32_bf16 v[40:43], v[100:103], a[36:39], v[40:43]\n\t"
      "ds_read_b64 v[70:71], %[vf] offset:1096\n\t"
      "v_mfma_f32_16x16x32_bf16 v[44:47], v[100:103], a[68:71], v[44:47]\n\t"
      "global_load_lds_dwordx4 v25, s[44:45]\n\t"
      "s_add_u32 m0, m0, 0x400\n\t"
      "v_mfma_f32_16x16x32_bf16 v[48:51], v[100:103], a[100:103], v[48:51]\n\t"
      "v_mfma_f32_16x16x32_bf16 v[52:55], v[100:103], a[132:135], v[52:55]\n\t"
      "v_mfma_f32_16x16x32_bf16 v[56:59], v[100:103], a[164:167], v[56:59]\n\t"
      "v_mfma_f32_16x16x32_bf16 v[60:63], v[100:103], a[196:199], v[60:63]\n\t"
      "s_waitcnt lgkmcnt(15)\n\t"
      "v_mfma_f32_16x16x32_bf16 v[36:39], v[104:107], a[8:11], v[36:39]\n\t"
      "ds_read_b64 v[72:73], %[vf] offset:1152\n\t"
      "v_mfma_f32_16x16x32_bf16 v[40:43], v[104:107], a[40:43], v[40:43]\n\t"
      "ds_read_b64 v[74:75], %[vf] offset:1160\n\t"
      "v_mfma_f32_16x16x32_bf16 v[44:47], v[104:107], a[72:75], v[44:47]\n\t"
      "global_load_lds_dwordx4 v26, s[44:45]\n\t"
      "s_add_u32 m0, m0, 0x400\n\t"
      "v_mfma_f32_16x16x32_bf16 v[48:51], v[104:107], a[104:107], v[48:51]\n\t"
      "v_mfma_f32_16x16x32_bf16 v[52:55], v[104:107], a[136:139], v[52:55]\n\t"
      "v_mfma_f32_16x16x32_bf16 v[56:59], v[104:107], a[168:171], v[56:59]\n\t"
      "v_mfma_f32_16x16x32_bf16 v[60:63], v[104:107], a[200:203], v[60:63]\n\t"
      "s_waitcnt lgkmcnt(15)\n\t"
      "v_mfma_f32_16x16x32_bf16 v[36:39], v[108:111], a[12:15], v[36:39]\n\t"
      "ds_read_b64 v[76:77], %[vf] offset:1216\n\t"
      "v_mfma_f32_16x16x32_bf16 v[40:43], v[108:111], a[44:47], v[40:43]\n\t"
      "ds_read_b64 v[78:79], %[vf] offset:1224\n\t"
      "v_mfma_f32_16x16x32_bf16 v[44:47], v[108:111], a[76:79], v[44:47]\n\t"
      "global_load_lds_dwordx4 v27, s[44:45]\n\t"
      "s_add_u32 s44, s44, 0x20000\n\t"
      "s_addc_u32 s45, s45, 0\n\t"
      "v_mfma_f32_16x16x32_bf16 v[48:51], v[108:111], a[108:111], v[48:51]\n\t"
      "v_mfma_f32_16x16x32_bf16 v[52:55], v[108:111], a[140:143], v[52:55]\n\t"
      "v_mfma_f32_16x16x32_bf16 v[56:59], v[108:111], a[172:175], v[56:59]\n\t"
      "v_mfma_f32_16x16x32_bf16 v[60:63], v[108:111], a[204:207], v[60:63]\n\t"
      "s_waitcnt lgkmcnt(15)\n\t"
      "v_mfma_f32_16x16x32_bf16 v[36:39], v[112:115], a[16:19], v[36:39]\n\t"
      "ds_read_b64 v[80:81], %[vf] offset:1280\n\t"
      "v_mfma_f32_16x16x32_bf16 v[40:43], v[112:115], a[48:51], v[40:43]\n\t"
      "ds_read_b64 v[82:83], %[vf] offset:1288\n\t"
      "v_mfma_f32_16x16x32_bf16 v[44:47], v[112:115], a[80:83], v[44:47]\n\t"
      "s_add_u32 m0, s46, 16384\n\t"
      "s_nop 0\n\t"
      "global_load_lds_dwordx4 v24, s[44:45]\n\t"
      "s_add_u32 m0, m0, 0x400\n\t"
      "v_mfma_f32_16x16x32_bf16 v[48:51], v[112:115], a[112:115], v[48:51]\n\t"
      "v_mfma_f32_16x16x32_bf16 v[52:55], v[112:115], a[144:147], v[52:55]\n\t"
      "v_mfma_f32_16x16x32_bf16 v[56:59], v[112:115], a[176:179], v[56:59]\n\t"
      "v_mfma_f32_16x16x32_bf16 v[60:63], v[112:115], a[208:211], v[60:63]\n\t"
      "s_waitcnt lgkmcnt(14)\n\t"
      "v_mfma_f32_16x16x32_bf16 v[36:39], v[116:119], a[20:23], v[36:39]\n\t"
      "ds_read_b64 v[84:85], %[vf] offset:1344\n\t"
      "v_mfma_f32_16x16x32_bf16 v[40:43], v[116:119], a[52:55], v[40:43]\n\t"
      "ds_read_b64 v[86:87], %[vf] offset:1352\n\t"
      "v_mfma_f32_16x16x32_bf16 v[44:47], v[116:119], a[84:87], v[44:47]\n\t"
      "global_load_lds_dwordx4 v25, s[44:45]\n\t"
      "s_add_u32 m0, m0, 0x400\n\t"
      "v_mfma_f32_16x16x32_bf16 v[48:51], v[116:119], a[116:119], v[48:51]\n\t"
      "v_mfma_f32_16x16x32_bf16 v[52:55], v[116:119], a[148:151], v[52:55]\n\t"
      "v_mfma_f32_16x16x32_bf16 v[56:59], v[116:119], a[180:183], v[56:59]\n\t"
      "v_mfma_f32_16x16x32_bf16 v[60:63], v[116:119], a[212:215], v[60:63]\n\t"
      "s_waitcnt lgkmcnt(14)\n\t"
      "v_mfma_f32_16x16x32_bf16 v[36:39], v[120:123], a[24:27], v[36:39]\n\t"
      "ds_read_b64 v[88:89], %[vf] offset:1408\n\t"
      "v_mfma_f32_16x16x32_bf16 v[40:43], v[120:123], a[56:59], v[40:43]\n\t"
      "ds_read_b64 v[90:91], %[vf] offset:1416\n\t"
      "v_mfma_f32_16x16x32_bf16 v[44:47], v[120:123], a[88:91], v[44:47]\n\t"
      "global_load_lds_dwordx4 v26, s[44:45]\n\t"
      "s_add_u32 m0, m0, 0x400\n\t"
      "v_mfma_f32_16x16x32_bf16 v[48:51], v[120:123], a[120:123], v[48:51]\n\t"
      "v_mfma_f32_16x16x32_bf16 v[52:55], v[120:123], a[152:155], v[52:55]\n\t"
      "v_mfma_f32_16x16x32_bf16 v[56:59], v[120:123], a[184:187], v[56:59]\n\t"
      "v_mfma_f32_16x16x32_bf16 v[60:63], v[120:123], a[216:219], v[60:63]\n\t"
      "s_waitcnt lgkmcnt(14)\n\t"
      "v_mfma_f32_16x16x32_bf16 v[36:39], v[124:127], a[28:31], v[36:39]\n\t"
      "ds_read_b64 v[92:93], %[vf] offset:1472\n\t"
      "v_mfma_f32_16x16x32_bf16 v[40:43], v[124:127], a[60:63], v[40:43]\n\t"
      "ds_read_b64 v[94:95], %[vf] offset:1480\n\t"
      "v_mfma_f32_16x16x32_bf16 v[44:47], v[124:127], a[92:95], v[44:47]\n\t"
      "global_load_lds_dwordx4 v27, s[44:45]\n\t"
      "s_add_u32 s44, s44, 0x20000\n\t"
      "s_addc_u32 s45, s45, 0\n\t"
      "v_mfma_f32_16x16x32_bf16 v[48:51], v[124:127], a[124:127], v[48:51]\n\t"
      "v_mfma_f32_16x16x32_bf16 v[52:55], v[124:127], a[156:159], v[52:55]\n\t"
      "v_mfma_f32_16x16x32_bf16 v[56:59], v[124:127], a[188:191], v[56:59]\n\t"
      "v_mfma_f32_16x16x32_bf16 v[60:63], v[124:127], a[220:223], v[60:63]\n\t"
      "s_waitcnt lgkmcnt(14)\n\t"
      "v_mfma_f32_16x16x32_bf16 v[40:43], v[64:67], a[0:3], v[40:43]\n\t"
      "ds_read_b64 v[96:97], %[vf] offset:1536\n\t"
      "v_mfma_f32_16x16x32_bf16 v[44:47], v[64:67], a[32:35], v[44:47]\n\t"
      "ds_read_b64 v[98:99], %[vf] offset:1544\n\t"
      "v_mfma_f32_16x16x32_bf16 v[48:51], v[64:67], a[64:67], v[48:51]\n\t"
      "s_add_u32 m0, s46, 32768\n\t"
      "s_nop 0\n\t"
      "global_load_lds_dwordx4 v24, s[44:45]\n\t"
      "s_add_u32 m0, m0, 0x400\n\t"
      "v_mfma_f32_16x16x32_bf16 v[52:55], v[64:67], a[96:99], v[52:55]\n\t"
      "v_mfma_f32_16x16x32_bf16 v[56:59], v[64:67], a[128:131], v[56:59]\n\t"
      "v_mfma_f32_16x16x32_bf16 v[60:63], v[64:67], a[160:163], v[60:63]\n\t"
      "s_waitcnt lgkmcnt(14)\n\t"
      "v_mfma_f32_16x16x32_bf16 v[40:43], v[68:71], a[4:7], v[40:43]\n\t"
      "ds_read_b64 v[100:101], %[vf] offset:1600\n\t"
      "v_mfma_f32_16x16x32_bf16 v[44:47], v[68:71], a[36:39], v[44:47]\n\t"
      "ds_read_b64 v[102:103], %[vf] offset:1608\n\t"
      "v_mfma_f32_16x16x32_bf16 v[48:51], v[68:71], a[68:71], v[48:51]\n\t"
      "global_load_lds_dwordx4 v25, s[44:45]\n\t"
      "s_add_u32 m0, m0, 0x400\n\t"
      "v_mfma_f32_16x16x32_bf16 v[52:55], v[68:71], a[100:103], v[52:55]\n\t"
      "v_mfma_f32_16x16x32_bf16 v[56:59], v[68:71], a[132:135], v[56:59]\n\t"
      "v_mfma_f32_16x16x32_bf16 v[60:63], v[68:71], a[164:167], v[60:63]\n\t"
      "s_waitcnt lgkmcnt(14)\n\t"
      "v_mfma_f32_16x16x32_bf16 v[40:43], v[72:75], a[8:11], v[40:43]\n\t"
      "ds_read_b64 v[104:105], %[vf] offset:1664\n\t"
      "v_mfma_f32_16x16x32_bf16 v[44:47], v[72:75], a[40:43], v[44:47]\n\t"
      "ds_read_b64 v[106:107], %[vf] offset:1672\n\t"
      "v_mfma_f32_16x16x32_bf16 v[48:51], v[72:75], a[72:75], v[48:51]\n\t"
      "global_load_lds_dwordx4 v26, s[44:45]\n\t"
      "s_add_u32 m0, m0, 0x400\n\t"
      "v_mfma_f32_16x16x32_bf16 v[52:55], v[72:75], a[104:107], v[52:55]\n\t"
      "v_mfma_f32_16x16x32_bf16 v[56:59], v[72:75], a[136:139], v[56:59]\n\t"
      "v_mfma_f32_16x16x32_bf16 v[60:63], v[72:75], a[168:171], v[60:63]\n\t"
      "s_waitcnt lgkmcnt(14)\n\t"
      "v_mfma_f32_16x16x32_bf16 v[40:43], v[76:79], a[12:15], v[40:43]\n\t"
      "ds_read_b64 v[108:109], %[vf] offset:1728\n\t"
      "v_mfma_f32_16x16x32_bf16 v[44:47], v[76:79], a[44:47], v[44:47]\n\t"
      "ds_read_b64 v[110:111], %[vf] offset:1736\n\t"
      "v_mfma_f32_16x16x32_bf16 v[48:51], v[76:79], a[76:79], v[48:51]\n\t"
      "global_load_lds_dwordx4 v27, s[44:45]\n\t"
      "s_add_u32 s44, s44, 0x20000\n\t"
      "s_addc_u32 s45, s45, 0\n\t"
      "v_mfma_f32_16x16x32_bf16 v[52:55], v[76:79], a[108:111], v[52:55]\n\t"
      "v_mfma_f32_16x16x32_bf16 v[56:59], v[76:79], a[140:143], v[56:59]\n\t"
      "v_mfma_f32_16x16x32_bf16 v[60:63], v[76:79], a[172:175], v[60:63]\n\t"
      "s_waitcnt lgkmcnt(14)\n\t"
      "v_mfma_f32_16x16x32_bf16 v[40:43], v[80:83], a[16:19], v[40:43]\n\t"
      "ds_read_b64 v[112:113], %[vf] offset:1792\n\t"
      "v_mfma_f32_16x16x32_bf16 v[44:47], v[80:83], a[48:51], v[44:47]\n\t"
      "ds_read_b64 v[114:115], %[vf] offset:1800\n\t"
      "v_mfma_f32_16x16x32_bf16 v[48:51], v[80:83], a[80:83], v[48:51]\n\t"
      "s_add_u32 m0, s46, 49152\n\t"
      "s_nop 0\n\t"
      "global_load_lds_dwordx4 v24, s[44:45]\n\t"
      "s_add_u32 m0, m0, 0x400\n\t"
      "v_mfma_f32_16x16x32_bf16 v[52:55], v[80:83], a[112:115], v[52:55]\n\t"
      "v_mfma_f32_16x16x32_bf16 v[56:59], v[80:83], a[144:147], v[56:59]\n\t"
      "v_mfma_f32_16x16x32_bf16 v[60:63], v[80:83], a[176:179], v[60:63]\n\t"
      "s_waitcnt lgkmcnt(14)\n\t"
      "v_mfma_f32_16x16x32_bf16 v[40:43], v[84:87], a[20:23], v[40:43]\n\t"
      "ds_read_b64 v[116:117], %[vf] offset:1856\n\t"
      "v_mfma_f32_16x16x32_bf16 v[44:47], v[84:87], a[52:55], v[44:47]\n\t"
      "ds_read_b64 v[118:119], %[vf] offset:1864\n\t"
      "v_mfma_f32_16x16x32_bf16 v[48:51], v[84:87], a[84:87], v[48:51]\n\t"
      "global_load_lds_dwordx4 v25, s[44:45]\n\t"
      "s_add_u32 m0, m0, 0x400\n\t"
      "v_mfma_f32_16x16x32_bf16 v[52:55], v[84:87], a[116:119], v[52:55]\n\t"
      "v_mfma_f32_16x16x32_bf16 v[56:59], v[84:87], a[148:151], v[56:59]\n\t"
      "v_mfma_f32_16x16x32_bf16 v[60:63], v[84:87], a[180:183], v[60:63]\n\t"
      "s_waitcnt lgkmcnt(14)\n\t"
      "v_mfma_f32_16x16x32_bf16 v[40:43], v[88:91], a[24:27], v[40:43]\n\t"
      "ds_read_b64 v[120:121], %[vf] offset:1920\n\t"
      "v_mfma_f32_16x16x32_bf16 v[44:47], v[88:91], a[56:59], v[44:47]\n\t"
      "ds_read_b64 v[122:123], %[vf] offset:1928\n\t"
      "v_mfma_f32_16x16x32_bf16 v[48:51], v[88:91], a[88:91], v[48:51]\n\t"
      "global_load_lds_dwordx4 v26, s[44:45]\n\t"
      "s_add_u32 m0, m0, 0x400\n\t"
      "v_mfma_f32_16x16x32_bf16 v[52:55], v[88:91], a[120:123], v[52:55]\n\t"
      "v_mfma_f32_16x16x32_bf16 v[56:59], v[88:91], a[152:155], v[56:59]\n\t"
      "v_mfma_f32_16x16x32_bf16 v[60:63], v[88:91], a[184:187], v[60:63]\n\t"
      "s_waitcnt lgkmcnt(14)\n\t"
      "v_mfma_f32_16x16x32_bf16 v[40:43], v[92:95], a[28:31], v[40:43]\n\t"
      "ds_read_b64 v[124:125], %[vf] offset:1984\n\t"
      "v_mfma_f32_16x16x32_bf16 v[44:47], v[92:95], a[60:63], v[44:47]\n\t"
      "ds_read_b64 v[126:127], %[vf] offset:1992\n\t"
      "v_mfma_f32_16x16x32_bf16 v[48:51], v[92:95], a[92:95], v[48:51]\n\t"
      "global_load_lds_dwordx4 v27, s[44:45]\n\t"
      "s_add_u32 s44, s44, 0x20000\n\t"
      "s_addc_u32 s45, s45, 0\n\t"
      "v_mfma_f32_16x16x32_bf16 v[52:55], v[92:95], a[124:127], v[52:55]\n\t"
      "v_mfma_f32_16x16x32_bf16 v[56:59], v[92:95], a[156:159], v[56:59]\n\t"
      "v_mfma_f32_16x16x32_bf16 v[60:63], v[92:95], a[188:191], v[60:63]\n\t"
      "s_waitcnt lgkmcnt(14)\n\t"
      "v_mfma_f32_16x16x32_bf16 v[44:47], v[96:99], a[0:3], v[44:47]\n\t"
      "ds_read_b64 v[64:65], %[vf] offset:2048\n\t"
      "v_mfma_f32_16x16x32_bf16 v[48:51], v[96:99], a[32:35], v[48:51]\n\t"
      "ds_read_b64 v[66:67], %[vf] offset:2056\n\t"
      "v_mfma_f32_16x16x32_bf16 v[52:55], v[96:99], a[64:67], v[52:55]\n\t"
      "v_mfma_f32_16x16x32_bf16 v[56:59], v[96:99], a[96:99], v[56:59]\n\t"
      "v_mfma_f32_16x16x32_bf16 v[60:63], v[96:99], a[128:131], v[60:63]\n\t"
      "s_waitcnt lgkmcnt(14)\n\t"
      "v_mfma_f32_16x16x32_bf16 v[44:47], v[100:103], a[4:7], v[44:47]\n\t"
      "ds_read_b64 v[68:69], %[vf] offset:2112\n\t"
      "v_mfma_f32_16x16x32_bf16 v[48:51], v[100:103], a[36:39], v[48:51]\n\t"
      "ds_read_b64 v[70:71], %[vf] offset:2120\n\t"
      "v_mfma_f32_16x16x32_bf16 v[52:55], v[100:103], a[68:71], v[52:55]\n\t"
      "v_mfma_f32_16x16x32_bf16 v[56:59], v[100:103], a[100:103], v[56:59]\n\t"
      "v_mfma_f32_16x16x32_bf16 v[60:63], v[100:103], a[132:135], v[60:63]\n\t"
      "s_waitcnt lgkmcnt(14)\n\t"
      "v_mfma_f32_16x16x32_bf16 v[44:47], v[104:107], a[8:11], v[44:47]\n\t"
      "ds_read_b64 v[72:73], %[vf] offset:2176\n\t"
      "v_mfma_f32_16x16x32_bf16 v[48:51], v[104:107], a[40:43], v[48:51]\n\t"
      "ds_read_b64 v[74:75], %[vf] offset:2184\n\t"
      "v_mfma_f32_16x16x32_bf16 v[52:55], v[104:107], a[72:75], v[52:55]\n\t"
      "v_mfma_f32_16x16x32_bf16 v[56:59], v[104:107], a[104:107], v[56:59]\n\t"
      "v_mfma_f32_16x16x32_bf16 v[60:63], v[104:107], a[136:139], v[60:63]\n\t"
      "s_waitcnt lgkmcnt(14)\n\t"
      "v_mfma_f32_16x16x32_bf16 v[44:47], v[108:111], a[12:15], v[44:47]\n\t"
      "ds_read_b64 v[76:77], %[vf] offset:2240\n\t"
      "v_mfma_f32_16x16x32_bf16 v[48:51], v[108:111], a[44:47], v[48:51]\n\t"
      "ds_read_b64 v[78:79], %[vf] offset:2248\n\t"
      "v_mfma_f32_16x16x32_bf16 v[52:55], v[108:111], a[76:79], v[52:55]\n\t"
      "v_mfma_f32_16x16x32_bf16 v[56:59], v[108:111], a[108:111], v[56:59]\n\t"
      "v_mfma_f32_16x16x32_bf16 v[60:63], v[108:111], a[140:143], v[60:63]\n\t"
      "s_waitcnt lgkmcnt(14)\n\t"
      "v_mfma_f32_16x16x32_bf16 v[44:47], v[112:115], a[16:19], v[44:47]\n\t"
      "ds_read_b64 v[80:81], %[vf] offset:2304\n\t"
      "v_mfma_f32_16x16x32_bf16 v[48:51], v[112:115], a[48:51], v[48:51]\n\t"
      "ds_read_b64 v[82:83], %[vf] offset:2312\n\t"
      "v_mfma_f32_16x16x32_bf16 v[52:55], v[112:115], a[80:83], v[52:55]\n\t"
      "v_mfma_f32_16x16x32_bf16 v[56:59], v[112:115], a[112:115], v[56:59]\n\t"
      "v_mfma_f32_16x16x32_bf16 v[60:63], v[112:115], a[144:147], v[60:63]\n\t"
      "s_waitcnt lgkmcnt(14)\n\t"
      "v_mfma_f32_16x16x32_bf16 v[44:47], v[116:119], a[20:23], v[44:47]\n\t"
      "ds_read_b64 v[84:85], %[vf] offset:2368\n\t"
      "v_mfma_f32_16x16x32_bf16 v[48:51], v[116:119], a[52:55], v[48:51]\n\t"
      "ds_read_b64 v[86:87], %[vf] offset:2376\n\t"
      "v_mfma_f32_16x16x32_bf16 v[52:55], v[116:119], a[84:87], v[52:55]\n\t"
      "v_mfma_f32_16x16x32_bf16 v[56:59], v[116:119], a[116:119], v[56:59]\n\t"
      "v_mfma_f32_16x16x32_bf16 v[60:63], v[116:119], a[148:151], v[60:63]\n\t"
      "s_waitcnt lgkmcnt(14)\n\t"
      "v_mfma_f32_16x16x32_bf16 v[44:47], v[120:123], a[24:27], v[44:47]\n\t"
      "ds_read_b64 v[88:89], %[vf] offset:2432\n\t"
      "v_mfma_f32_16x16x32_bf16 v[48:51], v[120:123], a[56:59], v[48:51]\n\t"
      "ds_read_b64 v[90:91], %[vf] offset:2440\n\t"
      "v_mfma_f32_16x16x32_bf16 v[52:55], v[120:123], a[88:91], v[52:55]\n\t"
      "v_mfma_f32_16x16x32_bf16 v[56:59], v[120:123], a[120:123], v[56:59]\n\t"
      "v_mfma_f32_16x16x32_bf16 v[60:63], v[120:123], a[152:155], v[60:63]\n\t"
      "s_waitcnt lgkmcnt(14)\n\t"
      "v_mfma_f32_16x16x32_bf16 v[44:47], v[124:127], a[28:31], v[44:47]\n\t"
      "ds_read_b64 v[92:93], %[vf] offset:2496\n\t"
      "v_mfma_f32_16x16x32_bf16 v[48:51], v[124:127], a[60:63], v[48:51]\n\t"
      "ds_read_b64 v[94:95], %[vf] offset:2504\n\t"
      "v_mfma_f32_16x16x32_bf16 v[52:55], v[124:127], a[92:95], v[52:55]\n\t"
      "v_mfma_f32_16x16x32_bf16 v[56:59], v[124:127], a[124:127], v[56:59]\n\t"
      "v_mfma_f32_16x16x32_bf16 v[60:63], v[124:127], a[156:159], v[60:63]\n\t"
      "s_waitcnt lgkmcnt(14)\n\t"
      "v_mfma_f32_16x16x32_bf16 v[48:51], v[64:67], a[0:3], v[48:51]\n\t"
      "ds_read_b64 v[96:97], %[vf] offset:2560\n\t"
      "v_mfma_f32_16x16x32_bf16 v[52:55], v[64:67], a[32:35], v[52:55]\n\t"
      "ds_read_b64 v[98:99], %[vf] offset:2568\n\t"
      "v_mfma_f32_16x16x32_bf16 v[56:59], v[64:67], a[64:67], v[56:59]\n\t"
      "v_mfma_f32_16x16x32_bf16 v[60:63], v[64:67], a[96:99], v[60:63]\n\t"
      "s_waitcnt lgkmcnt(14)\n\t"
      "v_mfma_f32_16x16x32_bf16 v[48:51], v[68:71], a[4:7], v[48:51]\n\t"
      "ds_read_b64 v[100:101], %[vf] offset:2624\n\t"
      "v_mfma_f32_16x16x32_bf16 v[52:55], v[68:71], a[36:39], v[52:55]\n\t"
      "ds_read_b64 v[102:103], %[vf] offset:2632\n\t"
      "v_mfma_f32_16x16x32_bf16 v[56:59], v[68:71], a[68:71], v[56:59]\n\t"
      "v_mfma_f32_16x16x32_bf16 v[60:63], v[68:71], a[100:103], v[60:63]\n\t"
      "s_waitcnt lgkmcnt(14)\n\t"
      "v_mfma_f32_16x16x32_bf16 v[48:51], v[72:75], a[8:11], v[48:51]\n\t"
      "ds_read_b64 v[104:105], %[vf] offset:2688\n\t"
      "v_mfma_f32_16x16x32_bf16 v[52:55], v[72:75], a[40:43], v[52:55]\n\t"
      "ds_read_b64 v[106:107], %[vf] offset:2696\n\t"
      "v_mfma_f32_16x16x32_bf16 v[56:59], v[72:75], a[72:75], v[56:59]\n\t"
      "v_mfma_f32_16x16x32_bf16 v[60:63], v[72:75], a[104:107], v[60:63]\n\t"
      "s_waitcnt lgkmcnt(14)\n\t"
      "v_mfma_f32_16x16x32_bf16 v[48:51], v[76:79], a[12:15], v[48:51]\n\t"
      "ds_read_b64 v[108:109], %[vf] offset:2752\n\t"
      "v_mfma_f32_16x16x32_bf16 v[52:55], v[76:79], a[44:47], v[52:55]\n\t"
      "ds_read_b64 v[110:111], %[vf] offset:2760\n\t"
      "v_mfma_f32_16x16x32_bf16 v[56:59], v[76:79], a[76:79], v[56:59]\n\t"
      "v_mfma_f32_16x16x32_bf16 v[60:63], v[76:79], a[108:111], v[60:63]\n\t"
      "global_load_dwordx2 v[20:21], %[l8], %[chr] sc1\n\t"
      "s_waitcnt lgkmcnt(14)\n\t"
      "v_mfma_f32_16x16x32_bf16 v[48:51], v[80:83], a[16:19], v[48:51]\n\t"
      "ds_read_b64 v[112:113], %[vf] offset:2816\n\t"
      "v_mfma_f32_16x16x32_bf16 v[52:55], v[80:83], a[48:51], v[52:55]\n\t"
      "ds_read_b64 v[114:115], %[vf] offset:2824\n\t"
      "v_mfma_f32_16x16x32_bf16 v[56:59], v[80:83], a[80:83], v[56:59]\n\t"
      "v_mfma_f32_16x16x32_bf16 v[60:63], v[80:83], a[112:115], v[60:63]\n\t"
      "s_waitcnt lgkmcnt(14)\n\t"
      "v_mfma_f32_16x16x32_bf16 v[48:51], v[84:87], a[20:23], v[48:51]\n\t"
      "ds_read_b64 v[116:117], %[vf] offset:2880\n\t"
      "v_mfma_f32_16x16x32_bf16 v[52:55], v[84:87], a[52:55], v[52:55]\n\t"
      "ds_read_b64 v[118:119], %[vf] offset:2888\n\t"
      "v_mfma_f32_16x16x32_bf16 v[56:59], v[84:87], a[84:87], v[56:59]\n\t"
      "v_mfma_f32_16x16x32_bf16 v[60:63], v[84:87], a[116:119], v[60:63]\n\t"
      "s_waitcnt lgkmcnt(14)\n\t"
      "v_mfma_f32_16x16x32_bf16 v[48:51], v[88:91], a[24:27], v[48:51]\n\t"
      "ds_read_b64 v[120:121], %[vf] offset:2944\n\t"
      "v_mfma_f32_16x16x32_bf16 v[52:55], v[88:91], a[56:59], v[52:55]\n\t"
      "ds_read_b64 v[122:123], %[vf] offset:2952\n\t"
      "v_mfma_f32_16x16x32_bf16 v[56:59], v[88:91], a[88:91], v[56:59]\n\t"
      "v_mfma_f32_16x16x32_bf16 v[60:63], v[88:91], a[120:123], v[60:63]\n\t"
      "s_waitcnt lgkmcnt(14)\n\t"
      "v_mfma_f32_16x16x32_bf16 v[48:51], v[92:95], a[28:31], v[48:51]\n\t"
      "ds_read_b64 v[124:125], %[vf] offset:3008\n\t"
      "v_mfma_f32_16x16x32_bf16 v[52:55], v[92:95], a[60:63], v[52:55]\n\t"
      "ds_read_b64 v[126:127], %[vf] offset:3016\n\t"
      "v_mfma_f32_16x16x32_bf16 v[56:59], v[92:95], a[92:95], v[56:59]\n\t"
      "v_mfma_f32_16x16x32_bf16 v[60:63], v[92:95], a[124:127], v[60:63]\n\t"
      "s_waitcnt lgkmcnt(14)\n\t"
      "v_mfma_f32_16x16x32_bf16 v[52:55], v[96:99], a[0:3], v[52:55]\n\t"
      "ds_read_b64 v[64:65], %[vf] offset:3072\n\t"
      "v_mfma_f32_16x16x32_bf16 v[56:59], v[96:99], a[32:35], v[56:59]\n\t"
      "ds_read_b64 v[66:67], %[vf] offset:3080\n\t"
      "v_mfma_f32_16x16x32_bf16 v[60:63], v[96:99], a[64:67], v[60:63]\n\t"
      "s_waitcnt lgkmcnt(14)\n\t"
      "v_mfma_f32_16x16x32_bf16 v[52:55], v[100:103], a[4:7], v[52:55]\n\t"
      "ds_read_b64 v[68:69], %[vf] offset:3136\n\t"
      "v_mfma_f32_16x16x32_bf16 v[56:59], v[100:103], a[36:39], v[56:59]\n\t"
      "ds_read_b64 v[70:71], %[vf] offset:3144\n\t"
      "v_mfma_f32_16x16x32_bf16 v[60:63], v[100:103], a[68:71], v[60:63]\n\t"
      "s_waitcnt lgkmcnt(14)\n\t"
      "v_mfma_f32_16x16x32_bf16 v[52:55], v[104:107], a[8:11], v[52:55]\n\t"
      "ds_read_b64 v[72:73], %[vf] offset:3200\n\t"
      "v_mfma_f32_16x16x32_bf16 v[56:59], v[104:107], a[40:43], v[56:59]\n\t"
      "ds_read_b64 v[74:75], %[vf] offset:3208\n\t"
      "v_mfma_f32_16x16x32_bf16 v[60:63], v[104:107], a[72:75], v[60:63]\n\t"
      "s_waitcnt lgkmcnt(14)\n\t"
      "v_mfma_f32_16x16x32_bf16 v[52:55], v[108:111], a[12:15], v[52:55]\n\t"
      "ds_read_b64 v[76:77], %[vf] offset:3264\n\t"
      "v_mfma_f32_16x16x32_bf16 v[56:59], v[108:111], a[44:47], v[56:59]\n\t"
      "ds_read_b64 v[78:79], %[vf] offset:3272\n\t"
      "v_mfma_f32_16x16x32_bf16 v[60:63], v[108:111], a[76:79], v[60:63]\n\t"
      "s_waitcnt lgkmcnt(14)\n\t"
      "v_mfma_f32_16x16x32_bf16 v[52:55], v[112:115], a[16:19], v[52:55]\n\t"
      "ds_read_b64 v[80:81], %[vf] offset:3328\n\t"
      "v_mfma_f32_16x16x32_bf16 v[56:59], v[112:115], a[48:51], v[56:59]\n\t"
      "ds_read_b64 v[82:83], %[vf] offset:3336\n\t"
      "v_mfma_f32_16x16x32_bf16 v[60:63], v[112:115], a[80:83], v[60:63]\n\t"
      "s_waitcnt lgkmcnt(14)\n\t"
      "v_mfma_f32_16x16x32_bf16 v[52:55], v[116:119], a[20:23], v[52:55]\n\t"
      "ds_read_b64 v[84:85], %[vf] offset:3392\n\t"
      "v_mfma_f32_16x16x32_bf16 v[56:59], v[116:119], a[52:55], v[56:59]\n\t"
      "ds_read_b64 v[86:87], %[vf] offset:3400\n\t"
      "v_mfma_f32_16x16x32_bf16 v[60:63], v[116:119], a[84:87], v[60:63]\n\t"
      "s_waitcnt lgkmcnt(14)\n\t"
      "v_mfma_f32_16x16x32_bf16 v[52:55], v[120:123], a[24:27], v[52:55]\n\t"
      "ds_read_b64 v[88:89], %[vf] offset:3456\n\t"
      "v_mfma_f32_16x16x32_bf16 v[56:59], v[120:123], a[56:59], v[56:59]\n\t"
      "ds_read_b64 v[90:91], %[vf] offset:3464\n\t"
      "v_mfma_f32_16x16x32_bf16 v[60:63], v[120:123], a[88:91], v[60:63]\n\t"
      "s_waitcnt lgkmcnt(14)\n\t"
      "v_mfma_f32_16x16x32_bf16 v[52:55], v[124:127], a[28:31], v[52:55]\n\t"
      "ds_read_b64 v[92:93], %[vf] offset:3520\n\t"
      "v_mfma_f32_16x16x32_bf16 v[56:59], v[124:127], a[60:63], v[56:59]\n\t"
      "ds_read_b64 v[94:95], %[vf] offset:3528\n\t"
      "v_mfma_f32_16x16x32_bf16 v[60:63], v[124:127], a[92:95], v[60:63]\n\t"
      "s_waitcnt vmcnt(0)\n\t"
      "v_cmp_eq_u32 vcc, %[eplo], v20\n\t"
      "v_cmp_eq_u32 s[50:51], %[ephi], v21\n\t"
      "s_nop 1\n\t"
      "s_and_b64 vcc, vcc, s[50:51]\n\t"
      "s_cmp_eq_u64 vcc, exec\n\t"
      "s_cbranch_scc1 CHOK_%=\n\t"
      "s_mov_b32 s52, 0\n\t"
      "CHLOOP_%=:\n\t"
      "s_sleep 1\n\t"
      "global_load_dwordx2 v[20:21], %[l8], %[chr] sc1\n\t"
      "s_waitcnt vmcnt(0)\n\t"
      "v_cmp_eq_u32 vcc, %[eplo], v20\n\t"
      "v_cmp_eq_u32 s[50:51], %[ephi], v21\n\t"
      "s_nop 1\n\t"
      "s_and_b64 vcc, vcc, s[50:51]\n\t"
      "s_cmp_eq_u64 vcc, exec\n\t"
      "s_cbranch_scc1 CHOK_%=\n\t"
      "s_add_u32 s52, s52, 1\n\t"
      "s_cmp_lt_u32 s52, 0x100000\n\t"
      "s_cbranch_scc1 CHLOOP_%=\n\t"
      "v_mov_b32 v20, %[eplo]\n\t"
      "v_mov_b32 v21, %[ephi]\n\t"
      "v_mov_b32 v13, 0\n\t"
      "global_store_dwordx2 v13, v[20:21], %[tmo] sc1\n\t"
      "s_waitcnt vmcnt(0)\n\t"
      "CHOK_%=:\n\t"
      "s_waitcnt lgkmcnt(14)\n\t"
      "v_mfma_f32_16x16x32_bf16 v[56:59], v[64:67], a[0:3], v[56:59]\n\t"
      "ds_read_b64 v[96:97], %[vf] offset:3584\n\t"
      "v_mfma_f32_16x16x32_bf16 v[60:63], v[64:67], a[32:35], v[60:63]\n\t"
      "ds_read_b64 v[98:99], %[vf] offset:3592\n\t"
      "global_load_dwordx4 a[64:67], v16, %[hbp] offset:-2048 sc1\n\t"
      "s_waitcnt lgkmcnt(14)\n\t"
      "v_mfma_f32_16x16x32_bf16 v[56:59], v[68:71], a[4:7], v[56:59]\n\t"
      "ds_read_b64 v[100:101], %[vf] offset:3648\n\t"
      "v_mfma_f32_16x16x32_bf16 v[60:63], v[68:71], a[36:39], v[60:63]\n\t"
      "ds_read_b64 v[102:103], %[vf] offset:3656\n\t"
      "global_load_dwordx4 a[68:71], v17, %[hbp] offset:-2048 sc1\n\t"
      "s_waitcnt lgkmcnt(14)\n\t"
      "v_mfma_f32_16x16x32_bf16 v[56:59], v[72:75], a[8:11], v[56:59]\n\t"
      "ds_read_b64 v[104:105], %[vf] offset:3712\n\t"
      "v_mfma_f32_16x16x32_bf16 v[60:63], v[72:75], a[40:43], v[60:63]\n\t"
      "ds_read_b64 v[106:107], %[vf] offset:3720\n\t"
      "global_load_dwordx4 a[72:75], v18, %[hbp] offset:-2048 sc1\n\t"
      "s_waitcnt lgkmcnt(14)\n\t"
      "v_mfma_f32_16x16x32_bf16 v[56:59], v[76:79], a[12:15], v[56:59]\n\t"
      "ds_read_b64 v[108:109], %[vf] offset:3776\n\t"
      "v_mfma_f32_16x16x32_bf16 v[60:63], v[76:79], a[44:47], v[60:63]\n\t"
      "ds_read_b64 v[110:111], %[vf] offset:3784\n\t"
      "global_load_dwordx4 a[76:79], v19, %[hbp] offset:-2048 sc1\n\t"
      "s_waitcnt lgkmcnt(14)\n\t"
      "v_mfma_f32_16x16x32_bf16 v[56:59], v[80:83], a[16:19], v[56:59]\n\t"
      "ds_read_b64 v[112:113], %[vf] offset:3840\n\t"
      "v_mfma_f32_16x16x32_bf16 v[60:63], v[80:83], a[48:51], v[60:63]\n\t"
      "ds_read_b64 v[114:115], %[vf] offset:3848\n\t"
      "global_load_dwordx4 a[80:83], v16, %[hbp] offset:0 sc1\n\t"
      "s_waitcnt lgkmcnt(14)\n\t"
      "v_mfma_f32_16x16x32_bf16 v[56:59], v[84:87], a[20:23], v[56:59]\n\t"
      "ds_read_b64 v[116:117], %[vf] offset:3904\n\t"
      "v_mfma_f32_16x16x32_bf16 v[60:63], v[84:87], a[52:55], v[60:63]\n\t"
      "ds_read_b64 v[118:119], %[vf] offset:3912\n\t"
      "global_load_dwordx4 a[84:87], v17, %[hbp] offset:0 sc1\n\t"
      "s_waitcnt lgkmcnt(14)\n\t"
      "v_mfma_f32_16x16x32_bf16 v[56:59], v[88:91], a[24:27], v[56:59]\n\t"
      "ds_read_b64 v[120:121], %[vf] offset:3968\n\t"
      "v_mfma_f32_16x16x32_bf16 v[60:63], v[88:91], a[56:59], v[60:63]\n\t"
      "ds_read_b64 v[122:123], %[vf] offset:3976\n\t"
      "global_load_dwordx4 a[88:91], v18, %[hbp] offset:0 sc1\n\t"
      "s_waitcnt lgkmcnt(14)\n\t"
      "v_mfma_f32_16x16x32_bf16 v[56:59], v[92:95], a[28:31], v[56:59]\n\t"
      "ds_read_b64 v[124:125], %[vf] offset:4032\n\t"
      "v_mfma_f32_16x16x32_bf16 v[60:63], v[92:95], a[60:63], v[60:63]\n\t"
      "ds_read_b64 v[126:127], %[vf] offset:4040\n\t"
      "global_load_dwordx4 a[92:95], v19, %[hbp] offset:0 sc1\n\t"
      "s_waitcnt lgkmcnt(14)\n\t"
      "v_mfma_f32_16x16x32_bf16 v[60:63], v[96:99], a[0:3], v[60:63]\n\t"
      "global_load_dwordx4 a[96:99], v16, %[hbp] offset:2048 sc1\n\t"
      "s_waitcnt lgkmcnt(12)\n\t"
      "v_mfma_f32_16x16x32_bf16 v[60:63], v[100:103], a[4:7], v[60:63]\n\t"
      "global_load_dwordx4 a[100:103], v17, %[hbp] offset:2048 sc1\n\t"
      "s_waitcnt lgkmcnt(10)\n\t"
      "v_mfma_f32_16x16x32_bf16 v[60:63], v[104:107], a[8:11], v[60:63]\n\t"
      "global_load_dwordx4 a[104:107], v18, %[hbp] offset:2048 sc1\n\t"
      "s_waitcnt lgkmcnt(8)\n\t"
      "v_mfma_f32_16x16x32_bf16 v[60:63], v[108:111], a[12:15], v[60:63]\n\t"
      "global_load_dwordx4 a[108:111], v19, %[hbp] offset:2048 sc1\n\t"
      "s_waitcnt lgkmcnt(6)\n\t"
      "v_mfma_f32_16x16x32_bf16 v[60:63], v[112:115], a[16:19], v[60:63]\n\t"
      "s_waitcnt lgkmcnt(4)\n\t"
      "v_mfma_f32_16x16x32_bf16 v[60:63], v[116:119], a[20:23], v[60:63]\n\t"
      "s_waitcnt lgkmcnt(2)\n\t"
      "v_mfma_f32_16x16x32_bf16 v[60:63], v[120:123], a[24:27], v[60:63]\n\t"
      "s_waitcnt lgkmcnt(0)\n\t"
      "v_mfma_f32_16x16x32_bf16 v[60:63], v[124:127], a[28:31], v[60:63]\n\t"
      "s_nop 7\n\t"
      "s_nop 7\n\t"
      "s_waitcnt vmcnt(12)\n\t"
      : "=&{v[32:47]}"(y0), "=&{v[48:63]}"(y1)
      : [vg] "v"(vg), [vf] "v"(vf), [l8] "v"(l8), [chr] "s"(chr), [eplo] "s"(eplo), [ephi] "s"(ephi), [tmo] "s"(a.ws + WS_TMO),
        [hbp] "s"(hb2 + 2048), [o01] "s"(kh * 229376), [o23] "s"(229376 - kh * 229376),
        [lane] "v"(lane), [wb] "s"(wdb), [m0b] "s"(m0b)
      : "v64", "v65", "v66", "v67", "v68", "v69", "v70", "v71", "v72", "v73", "v74", "v75", "v76", "v77", "v78", "v79", "v80", "v81", "v82", "v83", "v84", "v85", "v86", "v87", "v88", "v89", "v90", "v91", "v92", "v93", "v94", "v95", "v96", "v97", "v98", "v99", "v100", "v101", "v102", "v103", "v104", "v105", "v106", "v107", "v108", "v109", "v110", "v111", "v112", "v113", "v114", "v115", "v116", "v117", "v118", "v119", "v120", "v121", "v122", "v123", "v124", "v125", "v126", "v127", "a0", "a1", "a2", "a3", "a4", "a5", "a6", "a7", "a8", "a9", "a10", "a11", "a12", "a13", "a14", "a15", "a16", "a17", "a18", "a19", "a20", "a21", "a22", "a23", "a24", "a25", "a26", "a27", "a28", "a29", "a30", "a31", "a32", "a33", "a34", "a35", "a36", "a37", "a38", "a39", "a40", "a41", "a42", "a43", "a44", "a45", "a46", "a47", "a48", "a49", "a50", "a51", "a52", "a53", "a54", "a55", "a56", "a57", "a58", "a59", "a60", "a61", "a62", "a63", "a64", "a65", "a66", "a67", "a68", "a69", "a70", "a71", "a72", "a73", "a74", "a75", "a76", "a77", "a78", "a79", "a80", "a81", "a82", "a83", "a84", "a85", "a86", "a87", "a88", "a89", "a90", "a91", "a92", "a93", "a94", "a95", "a96", "a97", "a98", "a99", "a100", "a101", "a102", "a103", "a104", "a105", "a106", "a107", "a108", "a109", "a110", "a111", "a112", "a113", "a114", "a115", "a116", "a117", "a118", "a119", "a120", "a121", "a122", "a123", "a124", "a125", "a126", "a127", "a128", "a129", "a130", "a131", "a132", "a133", "a134", "a135", "a136", "a137", "a138", "a139", "a140", "a141", "a142", "a143", "a144", "a145", "a146", "a147", "a148", "a149", "a150", "a151", "a152", "a153", "a154", "a155", "a156", "a157", "a158", "a159", "a160", "a161", "a162", "a163", "a164", "a165", "a166", "a167", "a168", "a169", "a170", "a171", "a172", "a173", "a174", "a175", "a176", "a177", "a178", "a179", "a180", "a181", "a182", "a183", "a184", "a185", "a186", "a187", "a188", "a189", "a190", "a191", "a192", "a193", "a194", "a195", "a196", "a197", "a198", "a199", "a200", "a201", "a202", "a203", "a204", "a205", "a206", "a207", "a208", "a209", "a210", "a211", "a212", "a213", "a214", "a215", "a216", "a217", "a218", "a219", "a220", "a221", "a222", "a223", "a224", "a225", "a226", "a227", "a228", "a229", "a230", "a231", "a232", "a233", "a234", "a235", "a236", "a237", "a238", "a239", "a240", "a241", "a242", "a243", "a244", "a245", "a246", "a247", "a248", "a249", "a250", "a251", "a252", "a253", "a254", "a255", "memory", "vcc", "v13", "v14", "v15", "v16", "v17", "v18", "v19", "v20", "v21", "v22", "v23", "v24", "v25", "v26", "v27", "v28", "v29", "v30", "s44", "s45", "s46", "s50", "s51", "s52", "scc");
  }
  STAMP(3);
  STAMP(4);
  f32x16 acc2[8];
  const int tok0 = 128 * tm + 64 * th + 32 * kh, d0 = 128 * dn + 64 * kg;
  u32x16 zq, xq2; u32x4 sq;
  u32x2 cpv;
  const char* cpr = a.ws + WS_FH + (mt * 32 + (ntile ^ 4)) * 8;
  const char* czr = a.ws + WS_FZ + dn * 256;
  {
    LANE_VARS();
    const char* hb = hb2 + 3 * 2048;
    const int r7 = (4 * g + q4) & 7;
    const unsigned va = (ldsb + L_RING + (32 * kg + 4 * g + q4) * 256 + (r7 << 5) + p4 * 8) ^ (kg << 7);
    const char* wdb2 = wdb + 4 * 0x20000;
    const char* fz = a.ws + WS_FZ + (xcd * 32 + j) * 8;
    const unsigned l8 = (lane & 31) * 8;
    const unsigned vu = ldsb + L_UR + w * 4608 + 504 - 32 * i16 - 8 * g, vz = 32 * i16 + 8 * g;
    asm volatile(
      "s_mov_b64 s[40:41], %[wb]\n\t"
      "s_mov_b64 s[42:43], %[hb]\n\t"
      "s_mov_b32 s46, %[m0b]\n\t"
      "s_add_u32 m0, s46, 0\n\t"
      "v_xor_b32 v116, 0, %[va]\n\t"
      "v_xor_b32 v117, 32, %[va]\n\t"
      "v_xor_b32 v118, 64, %[va]\n\t"
      "v_xor_b32 v119, 96, %[va]\n\t"
      "v_xor_b32 v120, 128, %[va]\n\t"
      "v_xor_b32 v121, 160, %[va]\n\t"
      "v_xor_b32 v122, 192, %[va]\n\t"
      "v_xor_b32 v123, 224, %[va]\n\t"
      "v_lshrrev_b32 v14, 4, %[lane]\n\t"
      "v_and_b32 v15, 15, %[lane]\n\t"
      "v_add_u32 v13, 0, v14\n\t"
      "v_lshlrev_b32 v24, 1, v13\n\t"
      "v_and_b32 v24, 14, v24\n\t"
      "v_xor_b32 v24, v24, v15\n\t"
      "v_lshlrev_b32 v24, 4, v24\n\t"
      "v_lshl_add_u32 v24, v13, 11, v24\n\t"
      "v_add_u32 v13, 4, v14\n\t"
      "v_lshlrev_b32 v25, 1, v13\n\t"
      "v_and_b32 v25, 14, v25\n\t"
      "v_xor_b32 v25, v25, v15\n\t"
      "v_lshlrev_b32 v25, 4, v25\n\t"
      "v_lshl_add_u32 v25, v13, 11, v25\n\t"
      "v_add_u32 v13, 8, v14\n\t"
      "v_lshlrev_b32 v26, 1, v13\n\t"
      "v_and_b32 v26, 14, v26\n\t"
      "v_xor_b32 v26, v26, v15\n\t"
      "v_lshlrev_b32 v26, 4, v26\n\t"
      "v_lshl_add_u32 v26, v13, 11, v26\n\t"
      "v_add_u32 v13, 12, v14\n\t"
      "v_lshlrev_b32 v27, 1, v13\n\t"
      "v_and_b32 v27, 14, v27\n\t"
      "v_xor_b32 v27, v27, v15\n\t"
      "v_lshlrev_b32 v27, 4, v27\n\t"
      "v_lshl_add_u32 v27, v13, 11, v27\n\t"
      "v_add_u32 v28, %[o01], %[l16]\n\t"
      "v_add_u32 v29, 0x1c000, v28\n\t"
      "v_add_u32 v30, %[o23], %[l16]\n\t"
      "v_add_u32 v31, 0x1c000, v30\n\t"
      "v_lshrrev_b32 v104, 2, %[lane]\n\t"
      "v_lshlrev_b32 v104, 12, v104\n\t"
      "v_and_b32 v105, 3, %[lane]\n\t"
      "v_lshl_add_u32 v104, v105, 4, v104\n\t"
      "v_add_u32 v105, 0x10000, v104\n\t"
      "v_add_u32 v106, 0x20000, v104\n\t"
      "v_add_u32 v107, 0x30000, v104\n\t"
      "v_mov_b32 v108, %[vx]\n\t"
      "v_add_u32 v109, 0x8000, v108\n\t"
      "v_and_b32 v110, 15, %[lane]\n\t"
      "v_lshlrev_b32 v110, 3, v110\n\t"
      "s_nop 4\n\t"
      "s_barrier\n\t"
      "ds_read_b64 v[64:65], %[vu] offset:3584\n\t"
      "ds_read_b64 v[66:67], %[vu] offset:3072\n\t"
      "ds_read_b64 v[68:69], %[vu] offset:2560\n\t"
      "ds_read_b64 v[70:71], %[vu] offset:2048\n\t"
      "ds_read_b64 v[72:73], %[vu] offset:1536\n\t"
      "ds_read_b64 v[74:75], %[vu] offset:1024\n\t"
      "ds_read_b64 v[76:77], %[vu] offset:512\n\t"
      "ds_read_b64 v[78:79], %[vu] offset:0\n\t"
      "ds_read_b64_tr_b16 a[0:1], v116 offset:0\n\t"
      "ds_read_b64_tr_b16 a[2:3], v116 offset:4096\n\t"
      "ds_read_b64_tr_b16 a[4:5], v117 offset:0\n\t"
      "ds_read_b64_tr_b16 a[6:7], v117 offset:4096\n\t"
      "ds_read_b64_tr_b16 a[8:9], v118 offset:0\n\t"
      "ds_read_b64_tr_b16 a[10:11], v118 offset:4096\n\t"
      "ds_read_b64_tr_b16 a[12:13], v119 offset:0\n\t"
      "ds_read_b64_tr_b16 a[14:15], v119 offset:4096\n\t"
      "ds_read_b64_tr_b16 a[16:17], v120 offset:0\n\t"
      "ds_read_b64_tr_b16 a[18:19], v120 offset:4096\n\t"
      "ds_read_b64_tr_b16 a[20:21], v121 offset:0\n\t"
      "ds_read_b64_tr_b16 a[22:23], v121 offset:4096\n\t"
      "ds_read_b64_tr_b16 a[24:25], v122 offset:0\n\t"
      "ds_read_b64_tr_b16 a[26:27], v122 offset:4096\n\t"
      "ds_read_b64_tr_b16 a[28:29], v123 offset:0\n\t"
      "ds_read_b64_tr_b16 a[30:31], v123 offset:4096\n\t"
      "s_waitcnt lgkmcnt(0)\n\t"
      "s_waitcnt vmcnt(8)\n\t"
      "v_mfma_f32_16x16x32_bf16 v[128:131], a[0:3], a[64:67], 0\n\t"
      "v_mfma_f32_16x16x32_bf16 v[132:135], a[0:3], a[68:71], 0\n\t"
      "v_and_b32 v80, 0xffff0000, v65\n\t"
      "v_lshlrev_b32 v81, 16, v65\n\t"
      "v_mfma_f32_16x16x32_bf16 v[136:139], a[0:3], a[72:75], 0\n\t"
      "s_waitcnt vmcnt(20)\n\t"
      "s_barrier\n\t"
      "v_and_b32 v82, 0xffff0000, v64\n\t"
      "global_load_dwordx4 a[112:115], v28, s[42:43] sc1\n\t"
      "v_mfma_f32_16x16x32_bf16 v[140:143], a[0:3], a[76:79], 0\n\t"
      "v_lshlrev_b32 v83, 16, v64\n\t"
      "v_fmac_f32 v32, %[dsc], v80\n\t"
      "ds_read_b64_tr_b16 a[32:33], v116 offset:16384\n\t"
      "v_mfma_f32_16x16x32_bf16 v[144:147], a[4:7], a[64:67], 0\n\t"
      "v_fmac_f32 v33, %[dsc], v81\n\t"
      "ds_read_b64_tr_b16 a[34:35], v116 offset:20480\n\t"
      "v_mfma_f32_16x16x32_bf16 v[148:151], a[4:7], a[68:71], 0\n\t"
      "v_fmac_f32 v34, %[dsc], v82\n\t"
      "v_fmac_f32 v35, %[dsc], v83\n\t"
      "global_load_lds_dwordx4 v24, s[40:41]\n\t"
      "s_add_u32 m0, m0, 0x400\n\t"
      "v_mfma_f32_16x16x32_bf16 v[152:155], a[4:7], a[72:75], 0\n\t"
      "v_cvt_pk_bf16_f32 v32, v32, v33\n\t"
      "ds_read_b64_tr_b16 a[36:37], v117 offset:16384\n\t"
      "v_mfma_f32_16x16x32_bf16 v[156:159], a[4:7], a[76:79], 0\n\t"
      "v_cvt_pk_bf16_f32 v33, v34, v35\n\t"
      "global_store_dwordx2 %[vz], v[32:33], %[zt] offset:0 sc1\n\t"
      "ds_read_b64_tr_b16 a[38:39], v117 offset:20480\n\t"
      "v_mfma_f32_16x16x32_bf16 v[160:163], a[8:11], a[64:67], 0\n\t"
      "v_and_b32 v80, 0xffff0000, v67\n\t"
      "global_load_dwordx4 a[116:119], v29, s[42:43] sc1\n\t"
      "v_mfma_f32_16x16x32_bf16 v[164:167], a[8:11], a[68:71], 0\n\t"
      "v_lshlrev_b32 v81, 16, v67\n\t"
      "v_and_b32 v82, 0xffff0000, v66\n\t"
      "ds_read_b64_tr_b16 a[40:41], v118 offset:16384\n\t"
      "v_mfma_f32_16x16x32_bf16 v[168:171], a[8:11], a[72:75], 0\n\t"
      "v_lshlrev_b32 v83, 16, v66\n\t"
      "ds_read_b64_tr_b16 a[42:43], v118 offset:20480\n\t"
      "v_mfma_f32_16x16x32_bf16 v[172:175], a[8:11], a[76:79], 0\n\t"
      "v_fmac_f32 v36, %[dsc], v80\n\t"
      "v_fmac_f32 v37, %[dsc], v81\n\t"
      "global_load_lds_dwordx4 v25, s[40:41]\n\t"
      "s_add_u32 m0, m0, 0x400\n\t"
      "v_mfma_f32_16x16x32_bf16 v[176:179], a[12:15], a[64:67], 0\n\t"
      "v_fmac_f32 v38, %[dsc], v82\n\t"
      "ds_read_b64_tr_b16 a[44:45], v119 offset:16384\n\t"
      "v_mfma_f32_16x16x32_bf16 v[180:183], a[12:15], a[68:71], 0\n\t"
      "v_fmac_f32 v39, %[dsc], v83\n\t"
      "v_cvt_pk_bf16_f32 v36, v36, v37\n\t"
      "ds_read_b64_tr_b16 a[46:47], v119 offset:20480\n\t"
      "v_mfma_f32_16x16x32_bf16 v[184:187], a[12:15], a[72:75], 0\n\t"
      "v_cvt_pk_bf16_f32 v37, v38, v39\n\t"
      "global_load_dwordx4 a[120:123], v30, s[42:43] sc1\n\t"
      "v_mfma_f32_16x16x32_bf16 v[188:191], a[12:15], a[76:79], 0\n\t"
      "global_store_dwordx2 %[vz], v[36:37], %[zt] offset:512 sc1\n\t"
      "ds_read_b64_tr_b16 a[48:49], v120 offset:16384\n\t"
      "v_mfma_f32_16x16x32_bf16 v[192:195], a[16:19], a[64:67], 0\n\t"
      "v_and_b32 v80, 0xffff0000, v69\n\t"
      "v_lshlrev_b32 v81, 16, v69\n\t"
      "v_mfma_f32_16x16x32_bf16 v[196:199], a[16:19], a[68:71], 0\n\t"
      "v_and_b32 v82, 0xffff0000, v68\n\t"
      "ds_read_b64_tr_b16 a[50:51], v120 offset:20480\n\t"
      "global_load_lds_dwordx4 v26, s[40:41]\n\t"
      "s_add_u32 m0, m0, 0x400\n\t"
      "v_mfma_f32_16x16x32_bf16 v[200:203], a[16:19], a[72:75], 0\n\t"
      "v_lshlrev_b32 v83, 16, v68\n\t"
      "v_fmac_f32 v40, %[dsc], v80\n\t"
      "ds_read_b64_tr_b16 a[52:53], v121 offset:16384\n\t"
      "v_mfma_f32_16x16x32_bf16 v[204:207], a[16:19], a[76:79], 0\n\t"
      "v_fmac_f32 v41, %[dsc], v81\n\t"
      "v_mfma_f32_16x16x32_bf16 v[208:211], a[20:23], a[64:67], 0\n\t"
      "v_fmac_f32 v42, %[dsc], v82\n\t"
      "v_fmac_f32 v43, %[dsc], v83\n\t"
      "ds_read_b64_tr_b16 a[54:55], v121 offset:20480\n\t"
      "global_load_dwordx4 a[124:127], v31, s[42:43] sc1\n\t"
      "v_mfma_f32_16x16x32_bf16 v[212:215], a[20:23], a[68:71], 0\n\t"
      "v_cvt_pk_bf16_f32 v40, v40, v41\n\t"
      "ds_read_b64_tr_b16 a[56:57], v122 offset:16384\n\t"
      "v_mfma_f32_16x16x32_bf16 v[216:219], a[20:23], a[72:75], 0\n\t"
      "v_cvt_pk_bf16_f32 v41, v42, v43\n\t"
      "global_store_dwordx2 %[vz], v[40:41], %[zt] offset:1024 sc1\n\t"
      "v_mfma_f32_16x16x32_bf16 v[220:223], a[20:23], a[76:79], 0\n\t"
      "v_and_b32 v80, 0xffff0000, v71\n\t"
      "ds_read_b64_tr_b16 a[58:59], v122 offset:20480\n\t"
      "global_load_lds_dwordx4 v27, s[40:41]\n\t"
      "s_add_u32 m0, s46, 16384\n\t"
      "v_mfma_f32_16x16x32_bf16 v[224:227], a[24:27], a[64:67], 0\n\t"
      "v_lshlrev_b32 v81, 16, v71\n\t"
      "v_and_b32 v82, 0xffff0000, v70\n\t"
      "ds_read_b64_tr_b16 a[60:61], v123 offset:16384\n\t"
      "v_mfma_f32_16x16x32_bf16 v[228:231], a[24:27], a[68:71], 0\n\t"
      "v_lshlrev_b32 v83, 16, v70\n\t"
      "v_mfma_f32_16x16x32_bf16 v[232:235], a[24:27], a[72:75], 0\n\t"
      "v_fmac_f32 v44, %[dsc], v80\n\t"
      "v_fmac_f32 v45, %[dsc], v81\n\t"
      "ds_read_b64_tr_b16 a[62:63], v123 offset:20480\n\t"
      "v_mfma_f32_16x16x32_bf16 v[236:239], a[24:27], a[76:79], 0\n\t"
      "v_fmac_f32 v46, %[dsc], v82\n\t"
      "v_mfma_f32_16x16x32_bf16 v[240:243], a[28:31], a[64:67], 0\n\t"
      "v_fmac_f32 v47, %[dsc], v83\n\t"
      "v_cvt_pk_bf16_f32 v44, v44, v45\n\t"
      "v_mfma_f32_16x16x32_bf16 v[244:247], a[28:31], a[68:71], 0\n\t"
      "v_cvt_pk_bf16_f32 v45, v46, v47\n\t"
      "v_mfma_f32_16x16x32_bf16 v[248:251], a[28:31], a[72:75], 0\n\t"
      "global_store_dwordx2 %[vz], v[44:45], %[zt] offset:1536 sc1\n\t"
      "v_mfma_f32_16x16x32_bf16 v[252:255], a[28:31], a[76:79], 0\n\t"
      "s_add_u32 s42, s42, 0x800\n\t"
      "s_addc_u32 s43, s43, 0\n\t"
      "s_add_u32 s40, s40, 0x20000\n\t"
      "s_addc_u32 s41, s41, 0\n\t"
      "s_waitcnt lgkmcnt(0)\n\t"
      "s_waitcnt vmcnt(16)\n\t"
      "v_mfma_f32_16x16x32_bf16 v[128:131], a[32:35], a[80:83], v[128:131]\n\t"
      "v_mfma_f32_16x16x32_bf16 v[132:135], a[32:35], a[84:87], v[132:135]\n\t"
      "v_and_b32 v80, 0xffff0000, v73\n\t"
      "v_lshlrev_b32 v81, 16, v73\n\t"
      "v_mfma_f32_16x16x32_bf16 v[136:139], a[32:35], a[88:91], v[136:139]\n\t"
      "s_waitcnt vmcnt(28)\n\t"
      "s_barrier\n\t"
      "v_and_b32 v82, 0xffff0000, v72\n\t"
      "global_load_dwordx4 a[64:67], v28, s[42:43] sc1\n\t"
      "v_mfma_f32_16x16x32_bf16 v[140:143], a[32:35], a[92:95], v[140:143]\n\t"
      "v_lshlrev_b32 v83, 16, v72\n\t"
      "v_fmac_f32 v48, %[dsc], v80\n\t"
      "ds_read_b64_tr_b16 a[0:1], v116 offset:32768\n\t"
      "v_mfma_f32_16x16x32_bf16 v[144:147], a[36:39], a[80:83], v[144:147]\n\t"
      "v_fmac_f32 v49, %[dsc], v81\n\t"
      "ds_read_b64_tr_b16 a[2:3], v116 offset:36864\n\t"
      "v_mfma_f32_16x16x32_bf16 v[148:151], a[36:39], a[84:87], v[148:151]\n\t"
      "v_fmac_f32 v50, %[dsc], v82\n\t"
      "v_fmac_f32 v51, %[dsc], v83\n\t"
      "global_load_lds_dwordx4 v24, s[40:41]\n\t"
      "s_add_u32 m0, m0, 0x400\n\t"
      "v_mfma_f32_16x16x32_bf16 v[152:155], a[36:39], a[88:91], v[152:155]\n\t"
      "v_cvt_pk_bf16_f32 v48, v48, v49\n\t"
      "ds_read_b64_tr_b16 a[4:5], v117 offset:32768\n\t"
      "v_mfma_f32_16x16x32_bf16 v[156:159], a[36:39], a[92:95], v[156:159]\n\t"
      "v_cvt_pk_bf16_f32 v49, v50, v51\n\t"
      "global_store_dwordx2 %[vz], v[48:49], %[zt] offset:2048 sc1\n\t"
      "ds_read_b64_tr_b16 a[6:7], v117 offset:36864\n\t"
      "v_mfma_f32_16x16x32_bf16 v[160:163], a[40:43], a[80:83], v[160:163]\n\t"
      "v_and_b32 v80, 0xffff0000, v75\n\t"
      "global_load_dwordx4 a[68:71], v29, s[42:43] sc1\n\t"
      "v_mfma_f32_16x16x32_bf16 v[164:167], a[40:43], a[84:87], v[164:167]\n\t"
      "v_lshlrev_b32 v81, 16, v75\n\t"
      "v_and_b32 v82, 0xffff0000, v74\n\t"
      "ds_read_b64_tr_b16 a[8:9], v118 offset:32768\n\t"
      "v_mfma_f32_16x16x32_bf16 v[168:171], a[40:43], a[88:91], v[168:171]\n\t"
      "v_lshlrev_b32 v83, 16, v74\n\t"
      "ds_read_b64_tr_b16 a[10:11], v118 offset:36864\n\t"
      "v_mfma_f32_16x16x32_bf16 v[172:175], a[40:43], a[92:95], v[172:175]\n\t"
      "v_fmac_f32 v52, %[dsc], v80\n\t"
      "v_fmac_f32 v53, %[dsc], v81\n\t"
      "global_load_lds_dwordx4 v25, s[40:41]\n\t"
      "s_add_u32 m0, m0, 0x400\n\t"
      "v_mfma_f32_16x16x32_bf16 v[176:179], a[44:47], a[80:83], v[176:179]\n\t"
      "v_fmac_f32 v54, %[dsc], v82\n\t"
      "ds_read_b64_tr_b16 a[12:13], v119 offset:32768\n\t"
      "v_mfma_f32_16x16x32_bf16 v[180:183], a[44:47], a[84:87], v[180:183]\n\t"
      "v_fmac_f32 v55, %[dsc], v83\n\t"
      "v_cvt_pk_bf16_f32 v52, v52, v53\n\t"
      "ds_read_b64_tr_b16 a[14:15], v119 offset:36864\n\t"
      "v_mfma_f32_16x16x32_bf16 v[184:187], a[44:47], a[88:91], v[184:187]\n\t"
      "v_cvt_pk_bf16_f32 v53, v54, v55\n\t"
      "global_load_dwordx4 a[72:75], v30, s[42:43] sc1\n\t"
      "v_mfma_f32_16x16x32_bf16 v[188:191], a[44:47], a[92:95], v[188:191]\n\t"
      "global_store_dwordx2 %[vz], v[52:53], %[zt] offset:2560 sc1\n\t"
      "ds_read_b64_tr_b16 a[16:17], v120 offset:32768\n\t"
      "v_mfma_f32_16x16x32_bf16 v[192:195], a[48:51], a[80:83], v[192:195]\n\t"
      "v_and_b32 v80, 0xffff0000, v77\n\t"
      "v_lshlrev_b32 v81, 16, v77\n\t"
      "v_mfma_f32_16x16x32_bf16 v[196:199], a[48:51], a[84:87], v[196:199]\n\t"
      "v_and_b32 v82, 0xffff0000, v76\n\t"
      "ds_read_b64_tr_b16 a[18:19], v120 offset:36864\n\t"
      "global_load_lds_dwordx4 v26, s[40:41]\n\t"
      "s_add_u32 m0, m0, 0x400\n\t"
      "v_mfma_f32_16x16x32_bf16 v[200:203], a[48:51], a[88:91], v[200:203]\n\t"
      "v_lshlrev_b32 v83, 16, v76\n\t"
      "v_fmac_f32 v56, %[dsc], v80\n\t"
      "ds_read_b64_tr_b16 a[20:21], v121 offset:32768\n\t"
      "v_mfma_f32_16x16x32_bf16 v[204:207], a[48:51], a[92:95], v[204:207]\n\t"
      "v_fmac_f32 v57, %[dsc], v81\n\t"
      "v_mfma_f32_16x16x32_bf16 v[208:211], a[52:55], a[80:83], v[208:211]\n\t"
      "v_fmac_f32 v58, %[dsc], v82\n\t"
      "v_fmac_f32 v59, %[dsc], v83\n\t"
      "ds_read_b64_tr_b16 a[22:23], v121 offset:36864\n\t"
      "global_load_dwordx4 a[76:79], v31, s[42:43] sc1\n\t"
      "v_mfma_f32_16x16x32_bf16 v[212:215], a[52:55], a[84:87], v[212:215]\n\t"
      "v_cvt_pk_bf16_f32 v56, v56, v57\n\t"
      "ds_read_b64_tr_b16 a[24:25], v122 offset:32768\n\t"
      "v_mfma_f32_16x16x32_bf16 v[216:219], a[52:55], a[88:91], v[216:219]\n\t"
      "v_cvt_pk_bf16_f32 v57, v58, v59\n\t"
      "global_store_dwordx2 %[vz], v[56:57], %[zt] offset:3072 sc1\n\t"
      "v_mfma_f32_16x16x32_bf16 v[220:223], a[52:55], a[92:95], v[220:223]\n\t"
      "v_and_b32 v80, 0xffff0000, v79\n\t"
      "ds_read_b64_tr_b16 a[26:27], v122 offset:36864\n\t"
      "global_load_lds_dwordx4 v27, s[40:41]\n\t"
      "s_add_u32 m0, s46, 32768\n\t"
      "v_mfma_f32_16x16x32_bf16 v[224:227], a[56:59], a[80:83], v[224:227]\n\t"
      "v_lshlrev_b32 v81, 16, v79\n\t"
      "v_and_b32 v82, 0xffff0000, v78\n\t"
      "ds_read_b64_tr_b16 a[28:29], v123 offset:32768\n\t"
      "v_mfma_f32_16x16x32_bf16 v[228:231], a[56:59], a[84:87], v[228:231]\n\t"
      "v_lshlrev_b32 v83, 16, v78\n\t"
      "v_mfma_f32_16x16x32_bf16 v[232:235], a[56:59], a[88:91], v[232:235]\n\t"
      "v_fmac_f32 v60, %[dsc], v80\n\t"
      "v_fmac_f32 v61, %[dsc], v81\n\t"
      "ds_read_b64_tr_b16 a[30:31], v123 offset:36864\n\t"
      "v_mfma_f32_16x16x32_bf16 v[236:239], a[56:59], a[92:95], v[236:239]\n\t"
      "v_fmac_f32 v62, %[dsc], v82\n\t"
      "v_mfma_f32_16x16x32_bf16 v[240:243], a[60:63], a[80:83], v[240:243]\n\t"
      "v_fmac_f32 v63, %[dsc], v83\n\t"
      "v_cvt_pk_bf16_f32 v60, v60, v61\n\t"
      "v_mfma_f32_16x16x32_bf16 v[244:247], a[60:63], a[84:87], v[244:247]\n\t"
      "v_cvt_pk_bf16_f32 v61, v62, v63\n\t"
      "v_mfma_f32_16x16x32_bf16 v[248:251], a[60:63], a[88:91], v[248:251]\n\t"
      "global_store_dwordx2 %[vz], v[60:61], %[zt] offset:3584 sc1\n\t"
      "v_mfma_f32_16x16x32_bf16 v[252:255], a[60:63], a[92:95], v[252:255]\n\t"
      "s_add_u32 s42, s42, 0x800\n\t"
      "s_addc_u32 s43, s43, 0\n\t"
      "s_add_u32 s40, s40, 0x20000\n\t"
      "s_addc_u32 s41, s41, 0\n\t"
      "s_waitcnt lgkmcnt(0)\n\t"
      "s_waitcnt vmcnt(24)\n\t"
      "v_mfma_f32_16x16x32_bf16 v[128:131], a[0:3], a[96:99], v[128:131]\n\t"
      "v_mfma_f32_16x16x32_bf16 v[132:135], a[0:3], a[100:103], v[132:135]\n\t"
      "v_mfma_f32_16x16x32_bf16 v[136:139], a[0:3], a[104:107], v[136:139]\n\t"
      "s_waitcnt vmcnt(36)\n\t"
      "s_barrier\n\t"
      "global_load_dwordx4 a[80:83], v28, s[42:43] sc1\n\t"
      "v_mfma_f32_16x16x32_bf16 v[140:143], a[0:3], a[108:111], v[140:143]\n\t"
      "ds_read_b64_tr_b16 a[32:33], v116 offset:49152\n\t"
      "v_mfma_f32_16x16x32_bf16 v[144:147], a[4:7], a[96:99], v[144:147]\n\t"
      "ds_read_b64_tr_b16 a[34:35], v116 offset:53248\n\t"
      "v_mfma_f32_16x16x32_bf16 v[148:151], a[4:7], a[100:103], v[148:151]\n\t"
      "global_load_lds_dwordx4 v24, s[40:41]\n\t"
      "s_add_u32 m0, m0, 0x400\n\t"
      "v_mfma_f32_16x16x32_bf16 v[152:155], a[4:7], a[104:107], v[152:155]\n\t"
      "ds_read_b64_tr_b16 a[36:37], v117 offset:49152\n\t"
      "v_mfma_f32_16x16x32_bf16 v[156:159], a[4:7], a[108:111], v[156:159]\n\t"
      "ds_read_b64_tr_b16 a[38:39], v117 offset:53248\n\t"
      "v_mfma_f32_16x16x32_bf16 v[160:163], a[8:11], a[96:99], v[160:163]\n\t"
      "global_load_dwordx4 a[84:87], v29, s[42:43] sc1\n\t"
      "v_mfma_f32_16x16x32_bf16 v[164:167], a[8:11], a[100:103], v[164:167]\n\t"
      "ds_read_b64_tr_b16 a[40:41], v118 offset:49152\n\t"
      "v_mfma_f32_16x16x32_bf16 v[168:171], a[8:11], a[104:107], v[168:171]\n\t"
      "ds_read_b64_tr_b16 a[42:43], v118 offset:53248\n\t"
      "v_mfma_f32_16x16x32_bf16 v[172:175], a[8:11], a[108:111], v[172:175]\n\t"
      "global_load_lds_dwordx4 v25, s[40:41]\n\t"
      "s_add_u32 m0, m0, 0x400\n\t"
      "v_mfma_f32_16x16x32_bf16 v[176:179], a[12:15], a[96:99], v[176:179]\n\t"
      "ds_read_b64_tr_b16 a[44:45], v119 offset:49152\n\t"
      "v_mfma_f32_16x16x32_bf16 v[180:183], a[12:15], a[100:103], v[180:183]\n\t"
      "ds_read_b64_tr_b16 a[46:47], v119 offset:53248\n\t"
      "v_mfma_f32_16x16x32_bf16 v[184:187], a[12:15], a[104:107], v[184:187]\n\t"
      "global_load_dwordx4 a[88:91], v30, s[42:43] sc1\n\t"
      "v_mfma_f32_16x16x32_bf16 v[188:191], a[12:15], a[108:111], v[188:191]\n\t"
      "ds_read_b64_tr_b16 a[48:49], v120 offset:49152\n\t"
      "v_mfma_f32_16x16x32_bf16 v[192:195], a[16:19], a[96:99], v[192:195]\n\t"
      "v_mfma_f32_16x16x32_bf16 v[196:199], a[16:19], a[100:103], v[196:199]\n\t"
      "ds_read_b64_tr_b16 a[50:51], v120 offset:53248\n\t"
      "global_load_lds_dwordx4 v26, s[40:41]\n\t"
      "s_add_u32 m0, m0, 0x400\n\t"
      "v_mfma_f32_16x16x32_bf16 v[200:203], a[16:19], a[104:107], v[200:203]\n\t"
      "ds_read_b64_tr_b16 a[52:53], v121 offset:49152\n\t"
      "v_mfma_f32_16x16x32_bf16 v[204:207], a[16:19], a[108:111], v[204:207]\n\t"
      "v_mfma_f32_16x16x32_bf16 v[208:211], a[20:23], a[96:99], v[208:211]\n\t"
      "ds_read_b64_tr_b16 a[54:55], v121 offset:53248\n\t"
      "global_load_dwordx4 a[92:95], v31, s[42:43] sc1\n\t"
      "v_mfma_f32_16x16x32_bf16 v[212:215], a[20:23], a[100:103], v[212:215]\n\t"
      "ds_read_b64_tr_b16 a[56:57], v122 offset:49152\n\t"
      "v_mfma_f32_16x16x32_bf16 v[216:219], a[20:23], a[104:107], v[216:219]\n\t"
      "s_add_u32 s42, s42, 0x800\n\t"
      "s_addc_u32 s43, s43, 0\n\t"
      "v_mfma_f32_16x16x32_bf16 v[220:223], a[20:23], a[108:111], v[220:223]\n\t"
      "ds_read_b64_tr_b16 a[58:59], v122 offset:53248\n\t"
      "global_load_lds_dwordx4 v27, s[40:41]\n\t"
      "s_add_u32 m0, s46, 49152\n\t"
      "v_mfma_f32_16x16x32_bf16 v[224:227], a[24:27], a[96:99], v[224:227]\n\t"
      "ds_read_b64_tr_b16 a[60:61], v123 offset:49152\n\t"
      "v_mfma_f32_16x16x32_bf16 v[228:231], a[24:27], a[100:103], v[228:231]\n\t"
      "s_add_u32 s40, s40, 0x20000\n\t"
      "s_addc_u32 s41, s41, 0\n\t"
      "v_mfma_f32_16x16x32_bf16 v[232:235], a[24:27], a[104:107], v[232:235]\n\t"
      "ds_read_b64_tr_b16 a[62:63], v123 offset:53248\n\t"
      "v_mfma_f32_16x16x32_bf16 v[236:239], a[24:27], a[108:111], v[236:239]\n\t"
      "v_mfma_f32_16x16x32_bf16 v[240:243], a[28:31], a[96:99], v[240:243]\n\t"
      "v_mfma_f32_16x16x32_bf16 v[244:247], a[28:31], a[100:103], v[244:247]\n\t"
      "v_mfma_f32_16x16x32_bf16 v[248:251], a[28:31], a[104:107], v[248:251]\n\t"
      "v_mfma_f32_16x16x32_bf16 v[252:255], a[28:31], a[108:111], v[252:255]\n\t"
      "s_waitcnt lgkmcnt(0)\n\t"
      "s_waitcnt vmcnt(23)\n\t"
      "v_mfma_f32_16x16x32_bf16 v[128:131], a[32:35], a[112:115], v[128:131]\n\t"
      "v_mfma_f32_16x16x32_bf16 v[132:135], a[32:35], a[116:119], v[132:135]\n\t"
      "v_mfma_f32_16x16x32_bf16 v[136:139], a[32:35], a[120:123], v[136:139]\n\t"
      "s_waitcnt vmcnt(21)\n\t"
      "s_barrier\n\t"
      "global_load_dwordx4 a[96:99], v28, s[42:43] sc1\n\t"
      "v_mfma_f32_16x16x32_bf16 v[140:143], a[32:35], a[124:127], v[140:143]\n\t"
      "ds_read_b64_tr_b16 a[0:1], v116 offset:0\n\t"
      "v_mfma_f32_16x16x32_bf16 v[144:147], a[36:39], a[112:115], v[144:147]\n\t"
      "ds_read_b64_tr_b16 a[2:3], v116 offset:4096\n\t"
      "v_mfma_f32_16x16x32_bf16 v[148:151], a[36:39], a[116:119], v[148:151]\n\t"
      "global_load_lds_dwordx4 v24, s[40:41]\n\t"
      "s_add_u32 m0, m0, 0x400\n\t"
      "v_mfma_f32_16x16x32_bf16 v[152:155], a[36:39], a[120:123], v[152:155]\n\t"
      "ds_read_b64_tr_b16 a[4:5], v117 offset:0\n\t"
      "v_mfma_f32_16x16x32_bf16 v[156:159], a[36:39], a[124:127], v[156:159]\n\t"
      "ds_read_b64_tr_b16 a[6:7], v117 offset:4096\n\t"
      "v_mfma_f32_16x16x32_bf16 v[160:163], a[40:43], a[112:115], v[160:163]\n\t"
      "global_load_dwordx4 a[100:103], v29, s[42:43] sc1\n\t"
      "v_mfma_f32_16x16x32_bf16 v[164:167], a[40:43], a[116:119], v[164:167]\n\t"
      "ds_read_b64_tr_b16 a[8:9], v118 offset:0\n\t"
      "v_mfma_f32_16x16x32_bf16 v[168:171], a[40:43], a[120:123], v[168:171]\n\t"
      "ds_read_b64_tr_b16 a[10:11], v118 offset:4096\n\t"
      "v_mfma_f32_16x16x32_bf16 v[172:175], a[40:43], a[124:127], v[172:175]\n\t"
      "global_load_lds_dwordx4 v25, s[40:41]\n\t"
      "s_add_u32 m0, m0, 0x400\n\t"
      "v_mfma_f32_16x16x32_bf16 v[176:179], a[44:47], a[112:115], v[176:179]\n\t"
      "ds_read_b64_tr_b16 a[12:13], v119 offset:0\n\t"
      "v_mfma_f32_16x16x32_bf16 v[180:183], a[44:47], a[116:119], v[180:183]\n\t"
      "ds_read_b64_tr_b16 a[14:15], v119 offset:4096\n\t"
      "v_mfma_f32_16x16x32_bf16 v[184:187], a[44:47], a[120:123], v[184:187]\n\t"
      "global_load_dwordx4 a[104:107], v30, s[42:43] sc1\n\t"
      "v_mfma_f32_16x16x32_bf16 v[188:191], a[44:47], a[124:127], v[188:191]\n\t"
      "ds_read_b64_tr_b16 a[16:17], v120 offset:0\n\t"
      "v_mfma_f32_16x16x32_bf16 v[192:195], a[48:51], a[112:115], v[192:195]\n\t"
      "v_mfma_f32_16x16x32_bf16 v[196:199], a[48:51], a[116:119], v[196:199]\n\t"
      "ds_read_b64_tr_b16 a[18:19], v120 offset:4096\n\t"
      "global_load_lds_dwordx4 v26, s[40:41]\n\t"
      "s_add_u32 m0, m0, 0x400\n\t"
      "v_mfma_f32_16x16x32_bf16 v[200:203], a[48:51], a[120:123], v[200:203]\n\t"
      "ds_read_b64_tr_b16 a[20:21], v121 offset:0\n\t"
      "v_mfma_f32_16x16x32_bf16 v[204:207], a[48:51], a[124:127], v[204:207]\n\t"
      "v_mfma_f32_16x16x32_bf16 v[208:211], a[52:55], a[112:115], v[208:211]\n\t"
      "ds_read_b64_tr_b16 a[22:23], v121 offset:4096\n\t"
      "global_load_dwordx4 a[108:111], v31, s[42:43] sc1\n\t"
      "v_mfma_f32_16x16x32_bf16 v[212:215], a[52:55], a[116:119], v[212:215]\n\t"
      "ds_read_b64_tr_b16 a[24:25], v122 offset:0\n\t"
      "v_mfma_f32_16x16x32_bf16 v[216:219], a[52:55], a[120:123], v[216:219]\n\t"
      "s_add_u32 s42, s42, 0x800\n\t"
      "s_addc_u32 s43, s43, 0\n\t"
      "v_mfma_f32_16x16x32_bf16 v[220:223], a[52:55], a[124:127], v[220:223]\n\t"
      "ds_read_b64_tr_b16 a[26:27], v122 offset:4096\n\t"
      "global_load_lds_dwordx4 v27, s[40:41]\n\t"
      "s_add_u32 m0, s46, 0\n\t"
      "v_mfma_f32_16x16x32_bf16 v[224:227], a[56:59], a[112:115], v[224:227]\n\t"
      "ds_read_b64_tr_b16 a[28:29], v123 offset:0\n\t"
      "v_mfma_f32_16x16x32_bf16 v[228:231], a[56:59], a[116:119], v[228:231]\n\t"
      "s_add_u32 s40, s40, 0x20000\n\t"
      "s_addc_u32 s41, s41, 0\n\t"
      "v_mfma_f32_16x16x32_bf16 v[232:235], a[56:59], a[120:123], v[232:235]\n\t"
      "ds_read_b64_tr_b16 a[30:31], v123 offset:4096\n\t"
      "v_mfma_f32_16x16x32_bf16 v[236:239], a[56:59], a[124:127], v[236:239]\n\t"
      "v_mfma_f32_16x16x32_bf16 v[240:243], a[60:63], a[112:115], v[240:243]\n\t"
      "v_mfma_f32_16x16x32_bf16 v[244:247], a[60:63], a[116:119], v[244:247]\n\t"
      "v_mfma_f32_16x16x32_bf16 v[248:251], a[60:63], a[120:123], v[248:251]\n\t"
      "v_mfma_f32_16x16x32_bf16 v[252:255], a[60:63], a[124:127], v[252:255]\n\t"
      "s_waitcnt lgkmcnt(0)\n\t"
      "s_waitcnt vmcnt(19)\n\t"
      "v_mfma_f32_16x16x32_bf16 v[128:131], a[0:3], a[64:67], v[128:131]\n\t"
      "v_mfma_f32_16x16x32_bf16 v[132:135], a[0:3], a[68:71], v[132:135]\n\t"
      "v_mfma_f32_16x16x32_bf16 v[136:139], a[0:3], a[72:75], v[136:139]\n\t"
      "s_waitcnt vmcnt(16)\n\t"
      "s_barrier\n\t"
      "s_mov_b64 exec, 1\n\t"
      "v_mov_b32 v14, %[eplo]\n\t"
      "v_mov_b32 v15, %[ephi]\n\t"
      "v_mov_b32 v13, 0\n\t"
      "global_store_dwordx2 v13, v[14:15], %[fz] sc1\n\t"
      "s_mov_b64 exec, -1\n\t"
      "global_load_dwordx4 a[112:115], v28, s[42:43] sc1\n\t"
      "v_mfma_f32_16x16x32_bf16 v[140:143], a[0:3], a[76:79], v[140:143]\n\t"
      "ds_read_b64_tr_b16 a[32:33], v116 offset:16384\n\t"
      "v_mfma_f32_16x16x32_bf16 v[144:147], a[4:7], a[64:67], v[144:147]\n\t"
      "ds_read_b64_tr_b16 a[34:35], v116 offset:20480\n\t"
      "v_mfma_f32_16x16x32_bf16 v[148:151], a[4:7], a[68:71], v[148:151]\n\t"
      "global_load_lds_dwordx4 v24, s[40:41]\n\t"
      "s_add_u32 m0, m0, 0x400\n\t"
      "v_mfma_f32_16x16x32_bf16 v[152:155], a[4:7], a[72:75], v[152:155]\n\t"
      "ds_read_b64_tr_b16 a[36:37], v117 offset:16384\n\t"
      "v_mfma_f32_16x16x32_bf16 v[156:159], a[4:7], a[76:79], v[156:159]\n\t"
      "ds_read_b64_tr_b16 a[38:39], v117 offset:20480\n\t"
      "v_mfma_f32_16x16x32_bf16 v[160:163], a[8:11], a[64:67], v[160:163]\n\t"
      "global_load_dwordx4 a[116:119], v29, s[42:43] sc1\n\t"
      "v_mfma_f32_16x16x32_bf16 v[164:167], a[8:11], a[68:71], v[164:167]\n\t"
      "ds_read_b64_tr_b16 a[40:41], v118 offset:16384\n\t"
      "v_mfma_f32_16x16x32_bf16 v[168:171], a[8:11], a[72:75], v[168:171]\n\t"
      "ds_read_b64_tr_b16 a[42:43], v118 offset:20480\n\t"
      "v_mfma_f32_16x16x32_bf16 v[172:175], a[8:11], a[76:79], v[172:175]\n\t"
      "global_load_lds_dwordx4 v25, s[40:41]\n\t"
      "s_add_u32 m0, m0, 0x400\n\t"
      "v_mfma_f32_16x16x32_bf16 v[176:179], a[12:15], a[64:67], v[176:179]\n\t"
      "ds_read_b64_tr_b16 a[44:45], v119 offset:16384\n\t"
      "v_mfma_f32_16x16x32_bf16 v[180:183], a[12:15], a[68:71], v[180:183]\n\t"
      "ds_read_b64_tr_b16 a[46:47], v119 offset:20480\n\t"
      "v_mfma_f32_16x16x32_bf16 v[184:187], a[12:15], a[72:75], v[184:187]\n\t"
      "global_load_dwordx4 a[120:123], v30, s[42:43] sc1\n\t"
      "v_mfma_f32_16x16x32_bf16 v[188:191], a[12:15], a[76:79], v[188:191]\n\t"
      "ds_read_b64_tr_b16 a[48:49], v120 offset:16384\n\t"
      "v_mfma_f32_16x16x32_bf16 v[192:195], a[16:19], a[64:67], v[192:195]\n\t"
      "v_mfma_f32_16x16x32_bf16 v[196:199], a[16:19], a[68:71], v[196:199]\n\t"
      "ds_read_b64_tr_b16 a[50:51], v120 offset:20480\n\t"
      "global_load_lds_dwordx4 v26, s[40:41]\n\t"
      "s_add_u32 m0, m0, 0x400\n\t"
      "v_mfma_f32_16x16x32_bf16 v[200:203], a[16:19], a[72:75], v[200:203]\n\t"
      "ds_read_b64_tr_b16 a[52:53], v121 offset:16384\n\t"
      "v_mfma_f32_16x16x32_bf16 v[204:207], a[16:19], a[76:79], v[204:207]\n\t"
      "v_mfma_f32_16x16x32_bf16 v[208:211], a[20:23], a[64:67], v[208:211]\n\t"
      "ds_read_b64_tr_b16 a[54:55], v121 offset:20480\n\t"
      "global_load_dwordx4 a[124:127], v31, s[42:43] sc1\n\t"
      "v_mfma_f32_16x16x32_bf16 v[212:215], a[20:23], a[68:71], v[212:215]\n\t"
      "ds_read_b64_tr_b16 a[56:57], v122 offset:16384\n\t"
      "v_mfma_f32_16x16x32_bf16 v[216:219], a[20:23], a[72:75], v[216:219]\n\t"
      "s_add_u32 s42, s42, 0x800\n\t"
      "s_addc_u32 s43, s43, 0\n\t"
      "v_mfma_f32_16x16x32_bf16 v[220:223], a[20:23], a[76:79], v[220:223]\n\t"
      "ds_read_b64_tr_b16 a[58:59], v122 offset:20480\n\t"
      "global_load_lds_dwordx4 v27, s[40:41]\n\t"
      "s_add_u32 m0, s46, 16384\n\t"
      "v_mfma_f32_16x16x32_bf16 v[224:227], a[24:27], a[64:67], v[224:227]\n\t"
      "ds_read_b64_tr_b16 a[60:61], v123 offset:16384\n\t"
      "v_mfma_f32_16x16x32_bf16 v[228:231], a[24:27], a[68:71], v[228:231]\n\t"
      "s_add_u32 s40, s40, 0x20000\n\t"
      "s_addc_u32 s41, s41, 0\n\t"
      "v_mfma_f32_16x16x32_bf16 v[232:235], a[24:27], a[72:75], v[232:235]\n\t"
      "ds_read_b64_tr_b16 a[62:63], v123 offset:20480\n\t"
      "v_mfma_f32_16x16x32_bf16 v[236:239], a[24:27], a[76:79], v[236:239]\n\t"
      "v_mfma_f32_16x16x32_bf16 v[240:243], a[28:31], a[64:67], v[240:243]\n\t"
      "v_mfma_f32_16x16x32_bf16 v[244:247], a[28:31], a[68:71], v[244:247]\n\t"
      "v_mfma_f32_16x16x32_bf16 v[248:251], a[28:31], a[72:75], v[248:251]\n\t"
      "v_mfma_f32_16x16x32_bf16 v[252:255], a[28:31], a[76:79], v[252:255]\n\t"
      "s_mov_b32 s48, 4\n\t"
      "G2LOOP_%=:\n\t"
      "s_waitcnt lgkmcnt(0)\n\t"
      "s_waitcnt vmcnt(17)\n\t"
      "v_mfma_f32_16x16x32_bf16 v[128:131], a[32:35], a[80:83], v[128:131]\n\t"
      "v_mfma_f32_16x16x32_bf16 v[132:135], a[32:35], a[84:87], v[132:135]\n\t"
      "v_mfma_f32_16x16x32_bf16 v[136:139], a[32:35], a[88:91], v[136:139]\n\t"
      "s_waitcnt vmcnt(16)\n\t"
      "s_barrier\n\t"
      "global_load_dwordx4 a[64:67], v28, s[42:43] sc1\n\t"
      "v_mfma_f32_16x16x32_bf16 v[140:143], a[32:35], a[92:95], v[140:143]\n\t"
      "ds_read_b64_tr_b16 a[0:1], v116 offset:32768\n\t"
      "v_mfma_f32_16x16x32_bf16 v[144:147], a[36:39], a[80:83], v[144:147]\n\t"
      "ds_read_b64_tr_b16 a[2:3], v116 offset:36864\n\t"
      "v_mfma_f32_16x16x32_bf16 v[148:151], a[36:39], a[84:87], v[148:151]\n\t"
      "global_load_lds_dwordx4 v24, s[40:41]\n\t"
      "s_add_u32 m0, m0, 0x400\n\t"
      "v_mfma_f32_16x16x32_bf16 v[152:155], a[36:39], a[88:91], v[152:155]\n\t"
      "ds_read_b64_tr_b16 a[4:5], v117 offset:32768\n\t"
      "v_mfma_f32_16x16x32_bf16 v[156:159], a[36:39], a[92:95], v[156:159]\n\t"
      "ds_read_b64_tr_b16 a[6:7], v117 offset:36864\n\t"
      "v_mfma_f32_16x16x32_bf16 v[160:163], a[40:43], a[80:83], v[160:163]\n\t"
      "global_load_dwordx4 a[68:71], v29, s[42:43] sc1\n\t"
      "v_mfma_f32_16x16x32_bf16 v[164:167], a[40:43], a[84:87], v[164:167]\n\t"
      "ds_read_b64_tr_b16 a[8:9], v118 offset:32768\n\t"
      "v_mfma_f32_16x16x32_bf16 v[168:171], a[40:43], a[88:91], v[168:171]\n\t"
      "ds_read_b64_tr_b16 a[10:11], v118 offset:36864\n\t"
      "v_mfma_f32_16x16x32_bf16 v[172:175], a[40:43], a[92:95], v[172:175]\n\t"
      "global_load_lds_dwordx4 v25, s[40:41]\n\t"
      "s_add_u32 m0, m0, 0x400\n\t"
      "v_mfma_f32_16x16x32_bf16 v[176:179], a[44:47], a[80:83], v[176:179]\n\t"
      "ds_read_b64_tr_b16 a[12:13], v119 offset:32768\n\t"
      "v_mfma_f32_16x16x32_bf16 v[180:183], a[44:47], a[84:87], v[180:183]\n\t"
      "ds_read_b64_tr_b16 a[14:15], v119 offset:36864\n\t"
      "v_mfma_f32_16x16x32_bf16 v[184:187], a[44:47], a[88:91], v[184:187]\n\t"
      "global_load_dwordx4 a[72:75], v30, s[42:43] sc1\n\t"
      "v_mfma_f32_16x16x32_bf16 v[188:191], a[44:47], a[92:95], v[188:191]\n\t"
      "ds_read_b64_tr_b16 a[16:17], v120 offset:32768\n\t"
      "v_mfma_f32_16x16x32_bf16 v[192:195], a[48:51], a[80:83], v[192:195]\n\t"
      "v_mfma_f32_16x16x32_bf16 v[196:199], a[48:51], a[84:87], v[196:199]\n\t"
      "ds_read_b64_tr_b16 a[18:19], v120 offset:36864\n\t"
      "global_load_lds_dwordx4 v26, s[40:41]\n\t"
      "s_add_u32 m0, m0, 0x400\n\t"
      "v_mfma_f32_16x16x32_bf16 v[200:203], a[48:51], a[88:91], v[200:203]\n\t"
      "ds_read_b64_tr_b16 a[20:21], v121 offset:32768\n\t"
      "v_mfma_f32_16x16x32_bf16 v[204:207], a[48:51], a[92:95], v[204:207]\n\t"
      "v_mfma_f32_16x16x32_bf16 v[208:211], a[52:55], a[80:83], v[208:211]\n\t"
      "ds_read_b64_tr_b16 a[22:23], v121 offset:36864\n\t"
      "global_load_dwordx4 a[76:79], v31, s[42:43] sc1\n\t"
      "v_mfma_f32_16x16x32_bf16 v[212:215], a[52:55], a[84:87], v[212:215]\n\t"
      "ds_read_b64_tr_b16 a[24:25], v122 offset:32768\n\t"
      "v_mfma_f32_16x16x32_bf16 v[216:219], a[52:55], a[88:91], v[216:219]\n\t"
      "s_add_u32 s42, s42, 0x800\n\t"
      "s_addc_u32 s43, s43, 0\n\t"
      "v_mfma_f32_16x16x32_bf16 v[220:223], a[52:55], a[92:95], v[220:223]\n\t"
      "ds_read_b64_tr_b16 a[26:27], v122 offset:36864\n\t"
      "global_load_lds_dwordx4 v27, s[40:41]\n\t"
      "s_add_u32 m0, s46, 32768\n\t"
      "v_mfma_f32_16x16x32_bf16 v[224:227], a[56:59], a[80:83], v[224:227]\n\t"
      "ds_read_b64_tr_b16 a[28:29], v123 offset:32768\n\t"
      "v_mfma_f32_16x16x32_bf16 v[228:231], a[56:59], a[84:87], v[228:231]\n\t"
      "s_add_u32 s40, s40, 0x20000\n\t"
      "s_addc_u32 s41, s41, 0\n\t"
      "v_mfma_f32_16x16x32_bf16 v[232:235], a[56:59], a[88:91], v[232:235]\n\t"
      "ds_read_b64_tr_b16 a[30:31], v123 offset:36864\n\t"
      "v_mfma_f32_16x16x32_bf16 v[236:239], a[56:59], a[92:95], v[236:239]\n\t"
      "v_mfma_f32_16x16x32_bf16 v[240:243], a[60:63], a[80:83], v[240:243]\n\t"
      "v_mfma_f32_16x16x32_bf16 v[244:247], a[60:63], a[84:87], v[244:247]\n\t"
      "v_mfma_f32_16x16x32_bf16 v[248:251], a[60:63], a[88:91], v[248:251]\n\t"
      "v_mfma_f32_16x16x32_bf16 v[252:255], a[60:63], a[92:95], v[252:255]\n\t"
      "s_waitcnt lgkmcnt(0)\n\t"
      "s_waitcnt vmcnt(17)\n\t"
      "v_mfma_f32_16x16x32_bf16 v[128:131], a[0:3], a[96:99], v[128:131]\n\t"
      "v_mfma_f32_16x16x32_bf16 v[132:135], a[0:3], a[100:103], v[132:135]\n\t"
      "v_mfma_f32_16x16x32_bf16 v[136:139], a[0:3], a[104:107], v[136:139]\n\t"
      "s_waitcnt vmcnt(16)\n\t"
      "s_barrier\n\t"
      "global_load_dwordx4 a[80:83], v28, s[42:43] sc1\n\t"
      "v_mfma_f32_16x16x32_bf16 v[140:143], a[0:3], a[108:111], v[140:143]\n\t"
      "ds_read_b64_tr_b16 a[32:33], v116 offset:49152\n\t"
      "v_mfma_f32_16x16x32_bf16 v[144:147], a[4:7], a[96:99], v[144:147]\n\t"
      "ds_read_b64_tr_b16 a[34:35], v116 offset:53248\n\t"
      "v_mfma_f32_16x16x32_bf16 v[148:151], a[4:7], a[100:103], v[148:151]\n\t"
      "global_load_lds_dwordx4 v24, s[40:41]\n\t"
      "s_add_u32 m0, m0, 0x400\n\t"
      "v_mfma_f32_16x16x32_bf16 v[152:155], a[4:7], a[104:107], v[152:155]\n\t"
      "ds_read_b64_tr_b16 a[36:37], v117 offset:49152\n\t"
      "v_mfma_f32_16x16x32_bf16 v[156:159], a[4:7], a[108:111], v[156:159]\n\t"
      "ds_read_b64_tr_b16 a[38:39], v117 offset:53248\n\t"
      "v_mfma_f32_16x16x32_bf16 v[160:163], a[8:11], a[96:99], v[160:163]\n\t"
      "global_load_dwordx4 a[84:87], v29, s[42:43] sc1\n\t"
      "v_mfma_f32_16x16x32_bf16 v[164:167], a[8:11], a[100:103], v[164:167]\n\t"
      "ds_read_b64_tr_b16 a[40:41], v118 offset:49152\n\t"
      "v_mfma_f32_16x16x32_bf16 v[168:171], a[8:11], a[104:107], v[168:171]\n\t"
      "ds_read_b64_tr_b16 a[42:43], v118 offset:53248\n\t"
      "v_mfma_f32_16x16x32_bf16 v[172:175], a[8:11], a[108:111], v[172:175]\n\t"
      "global_load_lds_dwordx4 v25, s[40:41]\n\t"
      "s_add_u32 m0, m0, 0x400\n\t"
      "v_mfma_f32_16x16x32_bf16 v[176:179], a[12:15], a[96:99], v[176:179]\n\t"
      "ds_read_b64_tr_b16 a[44:45], v119 offset:49152\n\t"
      "v_mfma_f32_16x16x32_bf16 v[180:183], a[12:15], a[100:103], v[180:183]\n\t"
      "ds_read_b64_tr_b16 a[46:47], v119 offset:53248\n\t"
      "v_mfma_f32_16x16x32_bf16 v[184:187], a[12:15], a[104:107], v[184:187]\n\t"
      "global_load_dwordx4 a[88:91], v30, s[42:43] sc1\n\t"
      "v_mfma_f32_16x16x32_bf16 v[188:191], a[12:15], a[108:111], v[188:191]\n\t"
      "ds_read_b64_tr_b16 a[48:49], v120 offset:49152\n\t"
      "v_mfma_f32_16x16x32_bf16 v[192:195], a[16:19], a[96:99], v[192:195]\n\t"
      "v_mfma_f32_16x16x32_bf16 v[196:199], a[16:19], a[100:103], v[196:199]\n\t"
      "ds_read_b64_tr_b16 a[50:51], v120 offset:53248\n\t"
      "global_load_lds_dwordx4 v26, s[40:41]\n\t"
      "s_add_u32 m0, m0, 0x400\n\t"
      "v_mfma_f32_16x16x32_bf16 v[200:203], a[16:19], a[104:107], v[200:203]\n\t"
      "ds_read_b64_tr_b16 a[52:53], v121 offset:49152\n\t"
      "v_mfma_f32_16x16x32_bf16 v[204:207], a[16:19], a[108:111], v[204:207]\n\t"
      "v_mfma_f32_16x16x32_bf16 v[208:211], a[20:23], a[96:99], v[208:211]\n\t"
      "ds_read_b64_tr_b16 a[54:55], v121 offset:53248\n\t"
      "global_load_dwordx4 a[92:95], v31, s[42:43] sc1\n\t"
      "v_mfma_f32_16x16x32_bf16 v[212:215], a[20:23], a[100:103], v[212:215]\n\t"
      "ds_read_b64_tr_b16 a[56:57], v122 offset:49152\n\t"
      "v_mfma_f32_16x16x32_bf16 v[216:219], a[20:23], a[104:107], v[216:219]\n\t"
      "s_add_u32 s42, s42, 0x800\n\t"
      "s_addc_u32 s43, s43, 0\n\t"
      "v_mfma_f32_16x16x32_bf16 v[220:223], a[20:23], a[108:111], v[220:223]\n\t"
      "ds_read_b64_tr_b16 a[58:59], v122 offset:53248\n\t"
      "global_load_lds_dwordx4 v27, s[40:41]\n\t"
      "s_add_u32 m0, s46, 49152\n\t"
      "v_mfma_f32_16x16x32_bf16 v[224:227], a[24:27], a[96:99], v[224:227]\n\t"
      "ds_read_b64_tr_b16 a[60:61], v123 offset:49152\n\t"
      "v_mfma_f32_16x16x32_bf16 v[228:231], a[24:27], a[100:103], v[228:231]\n\t"
      "s_add_u32 s40, s40, 0x20000\n\t"
      "s_addc_u32 s41, s41, 0\n\t"
      "v_mfma_f32_16x16x32_bf16 v[232:235], a[24:27], a[104:107], v[232:235]\n\t"
      "ds_read_b64_tr_b16 a[62:63], v123 offset:53248\n\t"
      "v_mfma_f32_16x16x32_bf16 v[236:239], a[24:27], a[108:111], v[236:239]\n\t"
      "v_mfma_f32_16x16x32_bf16 v[240:243], a[28:31], a[96:99], v[240:243]\n\t"
      "v_mfma_f32_16x16x32_bf16 v[244:247], a[28:31], a[100:103], v[244:247]\n\t"
      "v_mfma_f32_16x16x32_bf16 v[248:251], a[28:31], a[104:107], v[248:251]\n\t"
      "v_mfma_f32_16x16x32_bf16 v[252:255], a[28:31], a[108:111], v[252:255]\n\t"
      "s_waitcnt lgkmcnt(0)\n\t"
      "s_waitcnt vmcnt(17)\n\t"
      "v_mfma_f32_16x16x32_bf16 v[128:131], a[32:35], a[112:115], v[128:131]\n\t"
      "v_mfma_f32_16x16x32_bf16 v[132:135], a[32:35], a[116:119], v[132:135]\n\t"
      "v_mfma_f32_16x16x32_bf16 v[136:139], a[32:35], a[120:123], v[136:139]\n\t"
      "s_waitcnt vmcnt(16)\n\t"
      "s_barrier\n\t"
      "global_load_dwordx4 a[96:99], v28, s[42:43] sc1\n\t"
      "v_mfma_f32_16x16x32_bf16 v[140:143], a[32:35], a[124:127], v[140:143]\n\t"
      "ds_read_b64_tr_b16 a[0:1], v116 offset:0\n\t"
      "v_mfma_f32_16x16x32_bf16 v[144:147], a[36:39], a[112:115], v[144:147]\n\t"
      "ds_read_b64_tr_b16 a[2:3], v116 offset:4096\n\t"
      "v_mfma_f32_16x16x32_bf16 v[148:151], a[36:39], a[116:119], v[148:151]\n\t"
      "global_load_lds_dwordx4 v24, s[40:41]\n\t"
      "s_add_u32 m0, m0, 0x400\n\t"
      "v_mfma_f32_16x16x32_bf16 v[152:155], a[36:39], a[120:123], v[152:155]\n\t"
      "ds_read_b64_tr_b16 a[4:5], v117 offset:0\n\t"
      "v_mfma_f32_16x16x32_bf16 v[156:159], a[36:39], a[124:127], v[156:159]\n\t"
      "ds_read_b64_tr_b16 a[6:7], v117 offset:4096\n\t"
      "v_mfma_f32_16x16x32_bf16 v[160:163], a[40:43], a[112:115], v[160:163]\n\t"
      "global_load_dwordx4 a[100:103], v29, s[42:43] sc1\n\t"
      "v_mfma_f32_16x16x32_bf16 v[164:167], a[40:43], a[116:119], v[164:167]\n\t"
      "ds_read_b64_tr_b16 a[8:9], v118 offset:0\n\t"
      "v_mfma_f32_16x16x32_bf16 v[168:171], a[40:43], a[120:123], v[168:171]\n\t"
      "ds_read_b64_tr_b16 a[10:11], v118 offset:4096\n\t"
      "v_mfma_f32_16x16x32_bf16 v[172:175], a[40:43], a[124:127], v[172:175]\n\t"
      "global_load_lds_dwordx4 v25, s[40:41]\n\t"
      "s_add_u32 m0, m0, 0x400\n\t"
      "v_mfma_f32_16x16x32_bf16 v[176:179], a[44:47], a[112:115], v[176:179]\n\t"
      "ds_read_b64_tr_b16 a[12:13], v119 offset:0\n\t"
      "v_mfma_f32_16x16x32_bf16 v[180:183], a[44:47], a[116:119], v[180:183]\n\t"
      "ds_read_b64_tr_b16 a[14:15], v119 offset:4096\n\t"
      "v_mfma_f32_16x16x32_bf16 v[184:187], a[44:47], a[120:123], v[184:187]\n\t"
      "global_load_dwordx4 a[104:107], v30, s[42:43] sc1\n\t"
      "v_mfma_f32_16x16x32_bf16 v[188:191], a[44:47], a[124:127], v[188:191]\n\t"
      "ds_read_b64_tr_b16 a[16:17], v120 offset:0\n\t"
      "v_mfma_f32_16x16x32_bf16 v[192:195], a[48:51], a[112:115], v[192:195]\n\t"
      "v_mfma_f32_16x16x32_bf16 v[196:199], a[48:51], a[116:119], v[196:199]\n\t"
      "ds_read_b64_tr_b16 a[18:19], v120 offset:4096\n\t"
      "global_load_lds_dwordx4 v26, s[40:41]\n\t"
      "s_add_u32 m0, m0, 0x400\n\t"
      "v_mfma_f32_16x16x32_bf16 v[200:203], a[48:51], a[120:123], v[200:203]\n\t"
      "ds_read_b64_tr_b16 a[20:21], v121 offset:0\n\t"
      "v_mfma_f32_16x16x32_bf16 v[204:207], a[48:51], a[124:127], v[204:207]\n\t"
      "v_mfma_f32_16x16x32_bf16 v[208:211], a[52:55], a[112:115], v[208:211]\n\t"
      "ds_read_b64_tr_b16 a[22:23], v121 offset:4096\n\t"
      "global_load_dwordx4 a[108:111], v31, s[42:43] sc1\n\t"
      "v_mfma_f32_16x16x32_bf16 v[212:215], a[52:55], a[116:119], v[212:215]\n\t"
      "ds_read_b64_tr_b16 a[24:25], v122 offset:0\n\t"
      "v_mfma_f32_16x16x32_bf16 v[216:219], a[52:55], a[120:123], v[216:219]\n\t"
      "s_add_u32 s42, s42, 0x800\n\t"
      "s_addc_u32 s43, s43, 0\n\t"
      "v_mfma_f32_16x16x32_bf16 v[220:223], a[52:55], a[124:127], v[220:223]\n\t"
      "ds_read_b64_tr_b16 a[26:27], v122 offset:4096\n\t"
      "global_load_lds_dwordx4 v27, s[40:41]\n\t"
      "s_add_u32 m0, s46, 0\n\t"
      "v_mfma_f32_16x16x32_bf16 v[224:227], a[56:59], a[112:115], v[224:227]\n\t"
      "ds_read_b64_tr_b16 a[28:29], v123 offset:0\n\t"
      "v_mfma_f32_16x16x32_bf16 v[228:231], a[56:59], a[116:119], v[228:231]\n\t"
      "s_add_u32 s40, s40, 0x20000\n\t"
      "s_addc_u32 s41, s41, 0\n\t"
      "v_mfma_f32_16x16x32_bf16 v[232:235], a[56:59], a[120:123], v[232:235]\n\t"
      "ds_read_b64_tr_b16 a[30:31], v123 offset:4096\n\t"
      "v_mfma_f32_16x16x32_bf16 v[236:239], a[56:59], a[124:127], v[236:239]\n\t"
      "v_mfma_f32_16x16x32_bf16 v[240:243], a[60:63], a[112:115], v[240:243]\n\t"
      "v_mfma_f32_16x16x32_bf16 v[244:247], a[60:63], a[116:119], v[244:247]\n\t"
      "v_mfma_f32_16x16x32_bf16 v[248:251], a[60:63], a[120:123], v[248:251]\n\t"
      "v_mfma_f32_16x16x32_bf16 v[252:255], a[60:63], a[124:127], v[252:255]\n\t"
      "s_waitcnt lgkmcnt(0)\n\t"
      "s_waitcnt vmcnt(17)\n\t"
      "v_mfma_f32_16x16x32_bf16 v[128:131], a[0:3], a[64:67], v[128:131]\n\t"
      "v_mfma_f32_16x16x32_bf16 v[132:135], a[0:3], a[68:71], v[132:135]\n\t"
      "v_mfma_f32_16x16x32_bf16 v[136:139], a[0:3], a[72:75], v[136:139]\n\t"
      "s_waitcnt vmcnt(16)\n\t"
      "s_barrier\n\t"
      "global_load_dwordx4 a[112:115], v28, s[42:43] sc1\n\t"
      "v_mfma_f32_16x16x32_bf16 v[140:143], a[0:3], a[76:79], v[140:143]\n\t"
      "ds_read_b64_tr_b16 a[32:33], v116 offset:16384\n\t"
      "v_mfma_f32_16x16x32_bf16 v[144:147], a[4:7], a[64:67], v[144:147]\n\t"
      "ds_read_b64_tr_b16 a[34:35], v116 offset:20480\n\t"
      "v_mfma_f32_16x16x32_bf16 v[148:151], a[4:7], a[68:71], v[148:151]\n\t"
      "global_load_lds_dwordx4 v24, s[40:41]\n\t"
      "s_add_u32 m0, m0, 0x400\n\t"
      "v_mfma_f32_16x16x32_bf16 v[152:155], a[4:7], a[72:75], v[152:155]\n\t"
      "ds_read_b64_tr_b16 a[36:37], v117 offset:16384\n\t"
      "v_mfma_f32_16x16x32_bf16 v[156:159], a[4:7], a[76:79], v[156:159]\n\t"
      "ds_read_b64_tr_b16 a[38:39], v117 offset:20480\n\t"
      "v_mfma_f32_16x16x32_bf16 v[160:163], a[8:11], a[64:67], v[160:163]\n\t"
      "global_load_dwordx4 a[116:119], v29, s[42:43] sc1\n\t"
      "v_mfma_f32_16x16x32_bf16 v[164:167], a[8:11], a[68:71], v[164:167]\n\t"
      "ds_read_b64_tr_b16 a[40:41], v118 offset:16384\n\t"
      "v_mfma_f32_16x16x32_bf16 v[168:171], a[8:11], a[72:75], v[168:171]\n\t"
      "ds_read_b64_tr_b16 a[42:43], v118 offset:20480\n\t"
      "v_mfma_f32_16x16x32_bf16 v[172:175], a[8:11], a[76:79], v[172:175]\n\t"
      "global_load_lds_dwordx4 v25, s[40:41]\n\t"
      "s_add_u32 m0, m0, 0x400\n\t"
      "v_mfma_f32_16x16x32_bf16 v[176:179], a[12:15], a[64:67], v[176:179]\n\t"
      "ds_read_b64_tr_b16 a[44:45], v119 offset:16384\n\t"
      "v_mfma_f32_16x16x32_bf16 v[180:183], a[12:15], a[68:71], v[180:183]\n\t"
      "ds_read_b64_tr_b16 a[46:47], v119 offset:20480\n\t"
      "v_mfma_f32_16x16x32_bf16 v[184:187], a[12:15], a[72:75], v[184:187]\n\t"
      "global_load_dwordx4 a[120:123], v30, s[42:43] sc1\n\t"
      "v_mfma_f32_16x16x32_bf16 v[188:191], a[12:15], a[76:79], v[188:191]\n\t"
      "ds_read_b64_tr_b16 a[48:49], v120 offset:16384\n\t"
      "v_mfma_f32_16x16x32_bf16 v[192:195], a[16:19], a[64:67], v[192:195]\n\t"
      "v_mfma_f32_16x16x32_bf16 v[196:199], a[16:19], a[68:71], v[196:199]\n\t"
      "ds_read_b64_tr_b16 a[50:51], v120 offset:20480\n\t"
      "global_load_lds_dwordx4 v26, s[40:41]\n\t"
      "s_add_u32 m0, m0, 0x400\n\t"
      "v_mfma_f32_16x16x32_bf16 v[200:203], a[16:19], a[72:75], v[200:203]\n\t"
      "ds_read_b64_tr_b16 a[52:53], v121 offset:16384\n\t"
      "v_mfma_f32_16x16x32_bf16 v[204:207], a[16:19], a[76:79], v[204:207]\n\t"
      "v_mfma_f32_16x16x32_bf16 v[208:211], a[20:23], a[64:67], v[208:211]\n\t"
      "ds_read_b64_tr_b16 a[54:55], v121 offset:20480\n\t"
      "global_load_dwordx4 a[124:127], v31, s[42:43] sc1\n\t"
      "v_mfma_f32_16x16x32_bf16 v[212:215], a[20:23], a[68:71], v[212:215]\n\t"
      "ds_read_b64_tr_b16 a[56:57], v122 offset:16384\n\t"
      "v_mfma_f32_16x16x32_bf16 v[216:219], a[20:23], a[72:75], v[216:219]\n\t"
      "s_add_u32 s42, s42, 0x800\n\t"
      "s_addc_u32 s43, s43, 0\n\t"
      "v_mfma_f32_16x16x32_bf16 v[220:223], a[20:23], a[76:79], v[220:223]\n\t"
      "ds_read_b64_tr_b16 a[58:59], v122 offset:20480\n\t"
      "global_load_lds_dwordx4 v27, s[40:41]\n\t"
      "s_add_u32 m0, s46, 16384\n\t"
      "v_mfma_f32_16x16x32_bf16 v[224:227], a[24:27], a[64:67], v[224:227]\n\t"
      "ds_read_b64_tr_b16 a[60:61], v123 offset:16384\n\t"
      "v_mfma_f32_16x16x32_bf16 v[228:231], a[24:27], a[68:71], v[228:231]\n\t"
      "s_add_u32 s40, s40, 0x20000\n\t"
      "s_addc_u32 s41, s41, 0\n\t"
      "v_mfma_f32_16x16x32_bf16 v[232:235], a[24:27], a[72:75], v[232:235]\n\t"
      "ds_read_b64_tr_b16 a[62:63], v123 offset:20480\n\t"
      "v_mfma_f32_16x16x32_bf16 v[236:239], a[24:27], a[76:79], v[236:239]\n\t"
      "v_mfma_f32_16x16x32_bf16 v[240:243], a[28:31], a[64:67], v[240:243]\n\t"
      "v_mfma_f32_16x16x32_bf16 v[244:247], a[28:31], a[68:71], v[244:247]\n\t"
      "v_mfma_f32_16x16x32_bf16 v[248:251], a[28:31], a[72:75], v[248:251]\n\t"
      "v_mfma_f32_16x16x32_bf16 v[252:255], a[28:31], a[76:79], v[252:255]\n\t"
      "s_sub_u32 s48, s48, 1\n\t"
      "s_cmp_lg_u32 s48, 0\n\t"
      "s_cbranch_scc1 G2LOOP_%=\n\t"
      "s_waitcnt lgkmcnt(0)\n\t"
      "s_waitcnt vmcnt(17)\n\t"
      "v_mfma_f32_16x16x32_bf16 v[128:131], a[32:35], a[80:83], v[128:131]\n\t"
      "v_mfma_f32_16x16x32_bf16 v[132:135], a[32:35], a[84:87], v[132:135]\n\t"
      "v_mfma_f32_16x16x32_bf16 v[136:139], a[32:35], a[88:91], v[136:139]\n\t"
      "s_waitcnt vmcnt(16)\n\t"
      "s_barrier\n\t"
      "global_load_dwordx4 a[64:67], v28, s[42:43] sc1\n\t"
      "v_mfma_f32_16x16x32_bf16 v[140:143], a[32:35], a[92:95], v[140:143]\n\t"
      "ds_read_b64_tr_b16 a[0:1], v116 offset:32768\n\t"
      "v_mfma_f32_16x16x32_bf16 v[144:147], a[36:39], a[80:83], v[144:147]\n\t"
      "ds_read_b64_tr_b16 a[2:3], v116 offset:36864\n\t"
      "v_mfma_f32_16x16x32_bf16 v[148:151], a[36:39], a[84:87], v[148:151]\n\t"
      "global_load_lds_dwordx4 v24, s[40:41]\n\t"
      "s_add_u32 m0, m0, 0x400\n\t"
      "v_mfma_f32_16x16x32_bf16 v[152:155], a[36:39], a[88:91], v[152:155]\n\t"
      "ds_read_b64_tr_b16 a[4:5], v117 offset:32768\n\t"
      "v_mfma_f32_16x16x32_bf16 v[156:159], a[36:39], a[92:95], v[156:159]\n\t"
      "ds_read_b64_tr_b16 a[6:7], v117 offset:36864\n\t"
      "v_mfma_f32_16x16x32_bf16 v[160:163], a[40:43], a[80:83], v[160:163]\n\t"
      "global_load_dwordx4 a[68:71], v29, s[42:43] sc1\n\t"
      "v_mfma_f32_16x16x32_bf16 v[164:167], a[40:43], a[84:87], v[164:167]\n\t"
      "ds_read_b64_tr_b16 a[8:9], v118 offset:32768\n\t"
      "v_mfma_f32_16x16x32_bf16 v[168:171], a[40:43], a[88:91], v[168:171]\n\t"
      "ds_read_b64_tr_b16 a[10:11], v118 offset:36864\n\t"
      "v_mfma_f32_16x16x32_bf16 v[172:175], a[40:43], a[92:95], v[172:175]\n\t"
      "global_load_lds_dwordx4 v25, s[40:41]\n\t"
      "s_add_u32 m0, m0, 0x400\n\t"
      "v_mfma_f32_16x16x32_bf16 v[176:179], a[44:47], a[80:83], v[176:179]\n\t"
      "ds_read_b64_tr_b16 a[12:13], v119 offset:32768\n\t"
      "v_mfma_f32_16x16x32_bf16 v[180:183], a[44:47], a[84:87], v[180:183]\n\t"
      "ds_read_b64_tr_b16 a[14:15], v119 offset:36864\n\t"
      "v_mfma_f32_16x16x32_bf16 v[184:187], a[44:47], a[88:91], v[184:187]\n\t"
      "global_load_dwordx4 a[72:75], v30, s[42:43] sc1\n\t"
      "v_mfma_f32_16x16x32_bf16 v[188:191], a[44:47], a[92:95], v[188:191]\n\t"
      "ds_read_b64_tr_b16 a[16:17], v120 offset:32768\n\t"
      "v_mfma_f32_16x16x32_bf16 v[192:195], a[48:51], a[80:83], v[192:195]\n\t"
      "v_mfma_f32_16x16x32_bf16 v[196:199], a[48:51], a[84:87], v[196:199]\n\t"
      "ds_read_b64_tr_b16 a[18:19], v120 offset:36864\n\t"
      "global_load_lds_dwordx4 v26, s[40:41]\n\t"
      "s_add_u32 m0, m0, 0x400\n\t"
      "v_mfma_f32_16x16x32_bf16 v[200:203], a[48:51], a[88:91], v[200:203]\n\t"
      "ds_read_b64_tr_b16 a[20:21], v121 offset:32768\n\t"
      "v_mfma_f32_16x16x32_bf16 v[204:207], a[48:51], a[92:95], v[204:207]\n\t"
      "v_mfma_f32_16x16x32_bf16 v[208:211], a[52:55], a[80:83], v[208:211]\n\t"
      "ds_read_b64_tr_b16 a[22:23], v121 offset:36864\n\t"
      "global_load_dwordx4 a[76:79], v31, s[42:43] sc1\n\t"
      "v_mfma_f32_16x16x32_bf16 v[212:215], a[52:55], a[84:87], v[212:215]\n\t"
      "ds_read_b64_tr_b16 a[24:25], v122 offset:32768\n\t"
      "v_mfma_f32_16x16x32_bf16 v[216:219], a[52:55], a[88:91], v[216:219]\n\t"
      "s_add_u32 s42, s42, 0x800\n\t"
      "s_addc_u32 s43, s43, 0\n\t"
      "v_mfma_f32_16x16x32_bf16 v[220:223], a[52:55], a[92:95], v[220:223]\n\t"
      "ds_read_b64_tr_b16 a[26:27], v122 offset:36864\n\t"
      "global_load_lds_dwordx4 v27, s[40:41]\n\t"
      "s_add_u32 m0, s46, 32768\n\t"
      "v_mfma_f32_16x16x32_bf16 v[224:227], a[56:59], a[80:83], v[224:227]\n\t"
      "ds_read_b64_tr_b16 a[28:29], v123 offset:32768\n\t"
      "v_mfma_f32_16x16x32_bf16 v[228:231], a[56:59], a[84:87], v[228:231]\n\t"
      "s_add_u32 s40, s40, 0x20000\n\t"
      "s_addc_u32 s41, s41, 0\n\t"
      "v_mfma_f32_16x16x32_bf16 v[232:235], a[56:59], a[88:91], v[232:235]\n\t"
      "ds_read_b64_tr_b16 a[30:31], v123 offset:36864\n\t"
      "v_mfma_f32_16x16x32_bf16 v[236:239], a[56:59], a[92:95], v[236:239]\n\t"
      "v_mfma_f32_16x16x32_bf16 v[240:243], a[60:63], a[80:83], v[240:243]\n\t"
      "v_mfma_f32_16x16x32_bf16 v[244:247], a[60:63], a[84:87], v[244:247]\n\t"
      "v_mfma_f32_16x16x32_bf16 v[248:251], a[60:63], a[88:91], v[248:251]\n\t"
      "v_mfma_f32_16x16x32_bf16 v[252:255], a[60:63], a[92:95], v[252:255]\n\t"
      "s_waitcnt lgkmcnt(0)\n\t"
      "s_waitcnt vmcnt(17)\n\t"
      "v_mfma_f32_16x16x32_bf16 v[128:131], a[0:3], a[96:99], v[128:131]\n\t"
      "v_mfma_f32_16x16x32_bf16 v[132:135], a[0:3], a[100:103], v[132:135]\n\t"
      "v_mfma_f32_16x16x32_bf16 v[136:139], a[0:3], a[104:107], v[136:139]\n\t"
      "s_waitcnt vmcnt(16)\n\t"
      "s_barrier\n\t"
      "global_load_dwordx4 a[80:83], v28, s[42:43] sc1\n\t"
      "v_mfma_f32_16x16x32_bf16 v[140:143], a[0:3], a[108:111], v[140:143]\n\t"
      "ds_read_b64_tr_b16 a[32:33], v116 offset:49152\n\t"
      "v_mfma_f32_16x16x32_bf16 v[144:147], a[4:7], a[96:99], v[144:147]\n\t"
      "ds_read_b64_tr_b16 a[34:35], v116 offset:53248\n\t"
      "v_mfma_f32_16x16x32_bf16 v[148:151], a[4:7], a[100:103], v[148:151]\n\t"
      "global_load_lds_dwordx4 v24, s[40:41]\n\t"
      "s_add_u32 m0, m0, 0x400\n\t"
      "v_mfma_f32_16x16x32_bf16 v[152:155], a[4:7], a[104:107], v[152:155]\n\t"
      "ds_read_b64_tr_b16 a[36:37], v117 offset:49152\n\t"
      "v_mfma_f32_16x16x32_bf16 v[156:159], a[4:7], a[108:111], v[156:159]\n\t"
      "ds_read_b64_tr_b16 a[38:39], v117 offset:53248\n\t"
      "v_mfma_f32_16x16x32_bf16 v[160:163], a[8:11], a[96:99], v[160:163]\n\t"
      "global_load_dwordx4 a[84:87], v29, s[42:43] sc1\n\t"
      "v_mfma_f32_16x16x32_bf16 v[164:167], a[8:11], a[100:103], v[164:167]\n\t"
      "global_load_dwordx2 %[cpv], %[zr], %[cpr] sc1\n\t"
      "global_load_dwordx2 v[114:115], %[l8], %[czr] sc1\n\t"
      "ds_read_b64_tr_b16 a[40:41], v118 offset:49152\n\t"
      "v_mfma_f32_16x16x32_bf16 v[168:171], a[8:11], a[104:107], v[168:171]\n\t"
      "ds_read_b64_tr_b16 a[42:43], v118 offset:53248\n\t"
      "v_mfma_f32_16x16x32_bf16 v[172:175], a[8:11], a[108:111], v[172:175]\n\t"
      "global_load_lds_dwordx4 v25, s[40:41]\n\t"
      "s_add_u32 m0, m0, 0x400\n\t"
      "v_mfma_f32_16x16x32_bf16 v[176:179], a[12:15], a[96:99], v[176:179]\n\t"
      "ds_read_b64_tr_b16 a[44:45], v119 offset:49152\n\t"
      "v_mfma_f32_16x16x32_bf16 v[180:183], a[12:15], a[100:103], v[180:183]\n\t"
      "ds_read_b64_tr_b16 a[46:47], v119 offset:53248\n\t"
      "v_mfma_f32_16x16x32_bf16 v[184:187], a[12:15], a[104:107], v[184:187]\n\t"
      "global_load_dwordx4 a[88:91], v30, s[42:43] sc1\n\t"
      "v_mfma_f32_16x16x32_bf16 v[188:191], a[12:15], a[108:111], v[188:191]\n\t"
      "ds_read_b64_tr_b16 a[48:49], v120 offset:49152\n\t"
      "v_mfma_f32_16x16x32_bf16 v[192:195], a[16:19], a[96:99], v[192:195]\n\t"
      "v_mfma_f32_16x16x32_bf16 v[196:199], a[16:19], a[100:103], v[196:199]\n\t"
      "ds_read_b64_tr_b16 a[50:51], v120 offset:53248\n\t"
      "global_load_lds_dwordx4 v26, s[40:41]\n\t"
      "s_add_u32 m0, m0, 0x400\n\t"
      "v_mfma_f32_16x16x32_bf16 v[200:203], a[16:19], a[104:107], v[200:203]\n\t"
      "ds_read_b64_tr_b16 a[52:53], v121 offset:49152\n\t"
      "v_mfma_f32_16x16x32_bf16 v[204:207], a[16:19], a[108:111], v[204:207]\n\t"
      "v_mfma_f32_16x16x32_bf16 v[208:211], a[20:23], a[96:99], v[208:211]\n\t"
      "ds_read_b64_tr_b16 a[54:55], v121 offset:53248\n\t"
      "global_load_dwordx4 a[92:95], v31, s[42:43] sc1\n\t"
      "v_mfma_f32_16x16x32_bf16 v[212:215], a[20:23], a[100:103], v[212:215]\n\t"
      "ds_read_b64_tr_b16 a[56:57], v122 offset:49152\n\t"
      "v_mfma_f32_16x16x32_bf16 v[216:219], a[20:23], a[104:107], v[216:219]\n\t"
      "s_add_u32 s42, s42, 0x800\n\t"
      "s_addc_u32 s43, s43, 0\n\t"
      "v_mfma_f32_16x16x32_bf16 v[220:223], a[20:23], a[108:111], v[220:223]\n\t"
      "ds_read_b64_tr_b16 a[58:59], v122 offset:53248\n\t"
      "global_load_lds_dwordx4 v27, s[40:41]\n\t"
      "s_add_u32 m0, s46, 49152\n\t"
      "v_mfma_f32_16x16x32_bf16 v[224:227], a[24:27], a[96:99], v[224:227]\n\t"
      "ds_read_b64_tr_b16 a[60:61], v123 offset:49152\n\t"
      "v_mfma_f32_16x16x32_bf16 v[228:231], a[24:27], a[100:103], v[228:231]\n\t"
      "s_add_u32 s40, s40, 0x20000\n\t"
      "s_addc_u32 s41, s41, 0\n\t"
      "v_mfma_f32_16x16x32_bf16 v[232:235], a[24:27], a[104:107], v[232:235]\n\t"
      "ds_read_b64_tr_b16 a[62:63], v123 offset:53248\n\t"
      "v_mfma_f32_16x16x32_bf16 v[236:239], a[24:27], a[108:111], v[236:239]\n\t"
      "v_mfma_f32_16x16x32_bf16 v[240:243], a[28:31], a[96:99], v[240:243]\n\t"
      "v_mfma_f32_16x16x32_bf16 v[244:247], a[28:31], a[100:103], v[244:247]\n\t"
      "v_mfma_f32_16x16x32_bf16 v[248:251], a[28:31], a[104:107], v[248:251]\n\t"
      "v_mfma_f32_16x16x32_bf16 v[252:255], a[28:31], a[108:111], v[252:255]\n\t"
      "s_waitcnt lgkmcnt(0)\n\t"
      "s_waitcnt vmcnt(19)\n\t"
      "v_mfma_f32_16x16x32_bf16 v[128:131], a[32:35], a[112:115], v[128:131]\n\t"
      "v_mfma_f32_16x16x32_bf16 v[132:135], a[32:35], a[116:119], v[132:135]\n\t"
      "v_mfma_f32_16x16x32_bf16 v[136:139], a[32:35], a[120:123], v[136:139]\n\t"
      "s_waitcnt vmcnt(18)\n\t"
      "s_barrier\n\t"
      "global_load_dwordx4 a[96:99], v28, s[42:43] sc1\n\t"
      "v_mfma_f32_16x16x32_bf16 v[140:143], a[32:35], a[124:127], v[140:143]\n\t"
      "ds_read_b64_tr_b16 a[0:1], v116 offset:0\n\t"
      "v_mfma_f32_16x16x32_bf16 v[144:147], a[36:39], a[112:115], v[144:147]\n\t"
      "ds_read_b64_tr_b16 a[2:3], v116 offset:4096\n\t"
      "v_mfma_f32_16x16x32_bf16 v[148:151], a[36:39], a[116:119], v[148:151]\n\t"
      "global_load_lds_dwordx4 v24, s[40:41]\n\t"
      "s_add_u32 m0, m0, 0x400\n\t"
      "v_mfma_f32_16x16x32_bf16 v[152:155], a[36:39], a[120:123], v[152:155]\n\t"
      "ds_read_b64_tr_b16 a[4:5], v117 offset:0\n\t"
      "v_mfma_f32_16x16x32_bf16 v[156:159], a[36:39], a[124:127], v[156:159]\n\t"
      "ds_read_b64_tr_b16 a[6:7], v117 offset:4096\n\t"
      "v_mfma_f32_16x16x32_bf16 v[160:163], a[40:43], a[112:115], v[160:163]\n\t"
      "global_load_dwordx4 a[100:103], v29, s[42:43] sc1\n\t"
      "v_mfma_f32_16x16x32_bf16 v[164:167], a[40:43], a[116:119], v[164:167]\n\t"
      "ds_read_b64_tr_b16 a[8:9], v118 offset:0\n\t"
      "v_mfma_f32_16x16x32_bf16 v[168:171], a[40:43], a[120:123], v[168:171]\n\t"
      "ds_read_b64_tr_b16 a[10:11], v118 offset:4096\n\t"
      "v_mfma_f32_16x16x32_bf16 v[172:175], a[40:43], a[124:127], v[172:175]\n\t"
      "global_load_lds_dwordx4 v25, s[40:41]\n\t"
      "s_add_u32 m0, m0, 0x400\n\t"
      "v_mfma_f32_16x16x32_bf16 v[176:179], a[44:47], a[112:115], v[176:179]\n\t"
      "ds_read_b64_tr_b16 a[12:13], v119 offset:0\n\t"
      "v_mfma_f32_16x16x32_bf16 v[180:183], a[44:47], a[116:119], v[180:183]\n\t"
      "ds_read_b64_tr_b16 a[14:15], v119 offset:4096\n\t"
      "v_mfma_f32_16x16x32_bf16 v[184:187], a[44:47], a[120:123], v[184:187]\n\t"
      "global_load_dwordx4 a[104:107], v30, s[42:43] sc1\n\t"
      "v_mfma_f32_16x16x32_bf16 v[188:191], a[44:47], a[124:127], v[188:191]\n\t"
      "ds_read_b64_tr_b16 a[16:17], v120 offset:0\n\t"
      "v_mfma_f32_16x16x32_bf16 v[192:195], a[48:51], a[112:115], v[192:195]\n\t"
      "v_mfma_f32_16x16x32_bf16 v[196:199], a[48:51], a[116:119], v[196:199]\n\t"
      "ds_read_b64_tr_b16 a[18:19], v120 offset:4096\n\t"
      "global_load_lds_dwordx4 v26, s[40:41]\n\t"
      "s_add_u32 m0, m0, 0x400\n\t"
      "v_mfma_f32_16x16x32_bf16 v[200:203], a[48:51], a[120:123], v[200:203]\n\t"
      "ds_read_b64_tr_b16 a[20:21], v121 offset:0\n\t"
      "v_mfma_f32_16x16x32_bf16 v[204:207], a[48:51], a[124:127], v[204:207]\n\t"
      "v_mfma_f32_16x16x32_bf16 v[208:211], a[52:55], a[112:115], v[208:211]\n\t"
      "ds_read_b64_tr_b16 a[22:23], v121 offset:4096\n\t"
      "global_load_dwordx4 a[108:111], v31, s[42:43] sc1\n\t"
      "v_mfma_f32_16x16x32_bf16 v[212:215], a[52:55], a[116:119], v[212:215]\n\t"
      "ds_read_b64_tr_b16 a[24:25], v122 offset:0\n\t"
      "v_mfma_f32_16x16x32_bf16 v[216:219], a[52:55], a[120:123], v[216:219]\n\t"
      "s_add_u32 s42, s42, 0x800\n\t"
      "s_addc_u32 s43, s43, 0\n\t"
      "v_mfma_f32_16x16x32_bf16 v[220:223], a[52:55], a[124:127], v[220:223]\n\t"
      "ds_read_b64_tr_b16 a[26:27], v122 offset:4096\n\t"
      "global_load_lds_dwordx4 v27, s[40:41]\n\t"
      "s_add_u32 m0, s46, 0\n\t"
      "v_mfma_f32_16x16x32_bf16 v[224:227], a[56:59], a[112:115], v[224:227]\n\t"
      "ds_read_b64_tr_b16 a[28:29], v123 offset:0\n\t"
      "v_mfma_f32_16x16x32_bf16 v[228:231], a[56:59], a[116:119], v[228:231]\n\t"
      "s_add_u32 s40, s40, 0x20000\n\t"
      "s_addc_u32 s41, s41, 0\n\t"
      "v_mfma_f32_16x16x32_bf16 v[232:235], a[56:59], a[120:123], v[232:235]\n\t"
      "ds_read_b64_tr_b16 a[30:31], v123 offset:4096\n\t"
      "v_mfma_f32_16x16x32_bf16 v[236:239], a[56:59], a[124:127], v[236:239]\n\t"
      "v_mfma_f32_16x16x32_bf16 v[240:243], a[60:63], a[112:115], v[240:243]\n\t"
      "v_mfma_f32_16x16x32_bf16 v[244:247], a[60:63], a[116:119], v[244:247]\n\t"
      "v_mfma_f32_16x16x32_bf16 v[248:251], a[60:63], a[120:123], v[248:251]\n\t"
      "v_mfma_f32_16x16x32_bf16 v[252:255], a[60:63], a[124:127], v[252:255]\n\t"
      "s_waitcnt lgkmcnt(0)\n\t"
      "s_waitcnt vmcnt(19)\n\t"
      "v_mfma_f32_16x16x32_bf16 v[128:131], a[0:3], a[64:67], v[128:131]\n\t"
      "v_mfma_f32_16x16x32_bf16 v[132:135], a[0:3], a[68:71], v[132:135]\n\t"
      "v_mfma_f32_16x16x32_bf16 v[136:139], a[0:3], a[72:75], v[136:139]\n\t"
      "s_waitcnt vmcnt(18)\n\t"
      "s_barrier\n\t"
      "global_load_dwordx4 a[112:115], v28, s[42:43] sc1\n\t"
      "v_mfma_f32_16x16x32_bf16 v[140:143], a[0:3], a[76:79], v[140:143]\n\t"
      "ds_read_b64_tr_b16 a[32:33], v116 offset:16384\n\t"
      "v_mfma_f32_16x16x32_bf16 v[144:147], a[4:7], a[64:67], v[144:147]\n\t"
      "ds_read_b64_tr_b16 a[34:35], v116 offset:20480\n\t"
      "v_mfma_f32_16x16x32_bf16 v[148:151], a[4:7], a[68:71], v[148:151]\n\t"
      "v_mfma_f32_16x16x32_bf16 v[152:155], a[4:7], a[72:75], v[152:155]\n\t"
      "ds_read_b64_tr_b16 a[36:37], v117 offset:16384\n\t"
      "v_mfma_f32_16x16x32_bf16 v[156:159], a[4:7], a[76:79], v[156:159]\n\t"
      "ds_read_b64_tr_b16 a[38:39], v117 offset:20480\n\t"
      "v_mfma_f32_16x16x32_bf16 v[160:163], a[8:11], a[64:67], v[160:163]\n\t"
      "global_load_dwordx4 a[116:119], v29, s[42:43] sc1\n\t"
      "v_mfma_f32_16x16x32_bf16 v[164:167], a[8:11], a[68:71], v[164:167]\n\t"
      "s_waitcnt vmcnt(15)\n\t"
      "v_cmp_eq_u32 vcc, %[eplo], v114\n\t"
      "v_cmp_eq_u32 s[50:51], %[ephi], v115\n\t"
      "s_nop 1\n\t"
      "s_and_b64 vcc, vcc, s[50:51]\n\t"
      "s_cmp_eq_u64 vcc, exec\n\t"
      "s_cbranch_scc1 PZOK_%=\n\t"
      "s_mov_b32 s52, 0\n\t"
      "PZLOOP_%=:\n\t"
      "s_sleep 1\n\t"
      "global_load_dwordx2 v[114:115], %[l8], %[czr] sc1\n\t"
      "s_waitcnt vmcnt(0)\n\t"
      "v_cmp_eq_u32 vcc, %[eplo], v114\n\t"
      "v_cmp_eq_u32 s[50:51], %[ephi], v115\n\t"
      "s_nop 1\n\t"
      "s_and_b64 vcc, vcc, s[50:51]\n\t"
      "s_cmp_eq_u64 vcc, exec\n\t"
      "s_cbranch_scc1 PZOK_%=\n\t"
      "s_add_u32 s52, s52, 1\n\t"
      "s_cmp_lt_u32 s52, 0x100000\n\t"
      "s_cbranch_scc1 PZLOOP_%=\n\t"
      "v_mov_b32 v114, %[eplo]\n\t"
      "v_mov_b32 v115, %[ephi]\n\t"
      "global_store_dwordx2 %[zr], v[114:115], %[tmo] sc1\n\t"
      "s_waitcnt vmcnt(0)\n\t"
      "PZOK_%=:\n\t"
      "ds_read_b64_tr_b16 a[40:41], v118 offset:16384\n\t"
      "v_mfma_f32_16x16x32_bf16 v[168:171], a[8:11], a[72:75], v[168:171]\n\t"
      "ds_read_b64_tr_b16 a[42:43], v118 offset:20480\n\t"
      "v_mfma_f32_16x16x32_bf16 v[172:175], a[8:11], a[76:79], v[172:175]\n\t"
      "v_mfma_f32_16x16x32_bf16 v[176:179], a[12:15], a[64:67], v[176:179]\n\t"
      "global_load_dwordx4 v[64:67], v104, %[zb] sc1\n\t"
      "global_load_dwordx4 v[68:71], v105, %[zb] sc1\n\t"
      "ds_read_b64_tr_b16 a[44:45], v119 offset:16384\n\t"
      "v_mfma_f32_16x16x32_bf16 v[180:183], a[12:15], a[68:71], v[180:183]\n\t"
      "ds_read_b64_tr_b16 a[46:47], v119 offset:20480\n\t"
      "v_mfma_f32_16x16x32_bf16 v[184:187], a[12:15], a[72:75], v[184:187]\n\t"
      "global_load_dwordx4 a[120:123], v30, s[42:43] sc1\n\t"
      "v_mfma_f32_16x16x32_bf16 v[188:191], a[12:15], a[76:79], v[188:191]\n\t"
      "global_load_dwordx4 v[72:75], v106, %[zb] sc1\n\t"
      "global_load_dwordx4 v[76:79], v107, %[zb] sc1\n\t"
      "ds_read_b64_tr_b16 a[48:49], v120 offset:16384\n\t"
      "v_mfma_f32_16x16x32_bf16 v[192:195], a[16:19], a[64:67], v[192:195]\n\t"
      "v_mfma_f32_16x16x32_bf16 v[196:199], a[16:19], a[68:71], v[196:199]\n\t"
      "ds_read_b64_tr_b16 a[50:51], v120 offset:20480\n\t"
      "v_mfma_f32_16x16x32_bf16 v[200:203], a[16:19], a[72:75], v[200:203]\n\t"
      "global_load_dwordx4 v[80:83], v108, %[xb]\n\t"
      "global_load_dwordx4 v[84:87], v109, %[xb]\n\t"
      "ds_read_b64_tr_b16 a[52:53], v121 offset:16384\n\t"
      "v_mfma_f32_16x16x32_bf16 v[204:207], a[16:19], a[76:79], v[204:207]\n\t"
      "v_mfma_f32_16x16x32_bf16 v[208:211], a[20:23], a[64:67], v[208:211]\n\t"
      "ds_read_b64_tr_b16 a[54:55], v121 offset:20480\n\t"
      "global_load_dwordx4 a[124:127], v31, s[42:43] sc1\n\t"
      "v_mfma_f32_16x16x32_bf16 v[212:215], a[20:23], a[68:71], v[212:215]\n\t"
      "global_load_dwordx4 v[88:91], v108, %[xb] offset:64\n\t"
      "global_load_dwordx4 v[92:95], v109, %[xb] offset:64\n\t"
      "ds_read_b64_tr_b16 a[56:57], v122 offset:16384\n\t"
      "v_mfma_f32_16x16x32_bf16 v[216:219], a[20:23], a[72:75], v[216:219]\n\t"
      "s_add_u32 s42, s42, 0x800\n\t"
      "s_addc_u32 s43, s43, 0\n\t"
      "v_mfma_f32_16x16x32_bf16 v[220:223], a[20:23], a[76:79], v[220:223]\n\t"
      "ds_read_b64_tr_b16 a[58:59], v122 offset:20480\n\t"
      "v_mfma_f32_16x16x32_bf16 v[224:227], a[24:27], a[64:67], v[224:227]\n\t"
      "global_load_dwordx2 v[96:97], v110, %[sb] sc1\n\t"
      "ds_read_b64_tr_b16 a[60:61], v123 offset:16384\n\t"
      "v_mfma_f32_16x16x32_bf16 v[228:231], a[24:27], a[68:71], v[228:231]\n\t"
      "v_mfma_f32_16x16x32_bf16 v[232:235], a[24:27], a[72:75], v[232:235]\n\t"
      "ds_read_b64_tr_b16 a[62:63], v123 offset:20480\n\t"
      "v_mfma_f32_16x16x32_bf16 v[236:239], a[24:27], a[76:79], v[236:239]\n\t"
      "global_load_dwordx2 v[98:99], v110, %[sb] offset:128 sc1\n\t"
      "v_mfma_f32_16x16x32_bf16 v[240:243], a[28:31], a[64:67], v[240:243]\n\t"
      "v_mfma_f32_16x16x32_bf16 v[244:247], a[28:31], a[68:71], v[244:247]\n\t"
      "v_mfma_f32_16x16x32_bf16 v[248:251], a[28:31], a[72:75], v[248:251]\n\t"
      "v_mfma_f32_16x16x32_bf16 v[252:255], a[28:31], a[76:79], v[252:255]\n\t"
      "s_waitcnt lgkmcnt(0)\n\t"
      "s_waitcnt vmcnt(23)\n\t"
      "v_mfma_f32_16x16x32_bf16 v[128:131], a[32:35], a[80:83], v[128:131]\n\t"
      "v_mfma_f32_16x16x32_bf16 v[132:135], a[32:35], a[84:87], v[132:135]\n\t"
      "v_mfma_f32_16x16x32_bf16 v[136:139], a[32:35], a[88:91], v[136:139]\n\t"
      "s_waitcnt vmcnt(22)\n\t"
      "s_barrier\n\t"
      "v_mfma_f32_16x16x32_bf16 v[140:143], a[32:35], a[92:95], v[140:143]\n\t"
      "ds_read_b64_tr_b16 a[0:1], v116 offset:32768\n\t"
      "v_mfma_f32_16x16x32_bf16 v[144:147], a[36:39], a[80:83], v[144:147]\n\t"
      "ds_read_b64_tr_b16 a[2:3], v116 offset:36864\n\t"
      "v_mfma_f32_16x16x32_bf16 v[148:151], a[36:39], a[84:87], v[148:151]\n\t"
      "v_mfma_f32_16x16x32_bf16 v[152:155], a[36:39], a[88:91], v[152:155]\n\t"
      "ds_read_b64_tr_b16 a[4:5], v117 offset:32768\n\t"
      "v_mfma_f32_16x16x32_bf16 v[156:159], a[36:39], a[92:95], v[156:159]\n\t"
      "ds_read_b64_tr_b16 a[6:7], v117 offset:36864\n\t"
      "v_mfma_f32_16x16x32_bf16 v[160:163], a[40:43], a[80:83], v[160:163]\n\t"
      "v_mfma_f32_16x16x32_bf16 v[164:167], a[40:43], a[84:87], v[164:167]\n\t"
      "ds_read_b64_tr_b16 a[8:9], v118 offset:32768\n\t"
      "v_mfma_f32_16x16x32_bf16 v[168:171], a[40:43], a[88:91], v[168:171]\n\t"
      "ds_read_b64_tr_b16 a[10:11], v118 offset:36864\n\t"
      "v_mfma_f32_16x16x32_bf16 v[172:175], a[40:43], a[92:95], v[172:175]\n\t"
      "v_mfma_f32_16x16x32_bf16 v[176:179], a[44:47], a[80:83], v[176:179]\n\t"
      "ds_read_b64_tr_b16 a[12:13], v119 offset:32768\n\t"
      "v_mfma_f32_16x16x32_bf16 v[180:183], a[44:47], a[84:87], v[180:183]\n\t"
      "ds_read_b64_tr_b16 a[14:15], v119 offset:36864\n\t"
      "v_mfma_f32_16x16x32_bf16 v[184:187], a[44:47], a[88:91], v[184:187]\n\t"
      "v_mfma_f32_16x16x32_bf16 v[188:191], a[44:47], a[92:95], v[188:191]\n\t"
      "ds_read_b64_tr_b16 a[16:17], v120 offset:32768\n\t"
      "v_mfma_f32_16x16x32_bf16 v[192:195], a[48:51], a[80:83], v[192:195]\n\t"
      "v_mfma_f32_16x16x32_bf16 v[196:199], a[48:51], a[84:87], v[196:199]\n\t"
      "ds_read_b64_tr_b16 a[18:19], v120 offset:36864\n\t"
      "v_mfma_f32_16x16x32_bf16 v[200:203], a[48:51], a[88:91], v[200:203]\n\t"
      "ds_read_b64_tr_b16 a[20:21], v121 offset:32768\n\t"
      "v_mfma_f32_16x16x32_bf16 v[204:207], a[48:51], a[92:95], v[204:207]\n\t"
      "v_mfma_f32_16x16x32_bf16 v[208:211], a[52:55], a[80:83], v[208:211]\n\t"
      "ds_read_b64_tr_b16 a[22:23], v121 offset:36864\n\t"
      "v_mfma_f32_16x16x32_bf16 v[212:215], a[52:55], a[84:87], v[212:215]\n\t"
      "ds_read_b64_tr_b16 a[24:25], v122 offset:32768\n\t"
      "v_mfma_f32_16x16x32_bf16 v[216:219], a[52:55], a[88:91], v[216:219]\n\t"
      "v_mfma_f32_16x16x32_bf16 v[220:223], a[52:55], a[92:95], v[220:223]\n\t"
      "ds_read_b64_tr_b16 a[26:27], v122 offset:36864\n\t"
      "v_mfma_f32_16x16x32_bf16 v[224:227], a[56:59], a[80:83], v[224:227]\n\t"
      "ds_read_b64_tr_b16 a[28:29], v123 offset:32768\n\t"
      "v_mfma_f32_16x16x32_bf16 v[228:231], a[56:59], a[84:87], v[228:231]\n\t"
      "v_mfma_f32_16x16x32_bf16 v[232:235], a[56:59], a[88:91], v[232:235]\n\t"
      "ds_read_b64_tr_b16 a[30:31], v123 offset:36864\n\t"
      "v_mfma_f32_16x16x32_bf16 v[236:239], a[56:59], a[92:95], v[236:239]\n\t"
      "v_mfma_f32_16x16x32_bf16 v[240:243], a[60:63], a[80:83], v[240:243]\n\t"
      "v_mfma_f32_16x16x32_bf16 v[244:247], a[60:63], a[84:87], v[244:247]\n\t"
      "v_mfma_f32_16x16x32_bf16 v[248:251], a[60:63], a[88:91], v[248:251]\n\t"
      "v_mfma_f32_16x16x32_bf16 v[252:255], a[60:63], a[92:95], v[252:255]\n\t"
      "s_waitcnt lgkmcnt(0)\n\t"
      "s_waitcnt vmcnt(15)\n\t"
      "v_mfma_f32_16x16x32_bf16 v[128:131], a[0:3], a[96:99], v[128:131]\n\t"
      "v_mfma_f32_16x16x32_bf16 v[132:135], a[0:3], a[100:103], v[132:135]\n\t"
      "v_mfma_f32_16x16x32_bf16 v[136:139], a[0:3], a[104:107], v[136:139]\n\t"
      "s_waitcnt vmcnt(14)\n\t"
      "s_barrier\n\t"
      "v_mfma_f32_16x16x32_bf16 v[140:143], a[0:3], a[108:111], v[140:143]\n\t"
      "ds_read_b64_tr_b16 a[32:33], v116 offset:49152\n\t"
      "v_mfma_f32_16x16x32_bf16 v[144:147], a[4:7], a[96:99], v[144:147]\n\t"
      "ds_read_b64_tr_b16 a[34:35], v116 offset:53248\n\t"
      "v_mfma_f32_16x16x32_bf16 v[148:151], a[4:7], a[100:103], v[148:151]\n\t"
      "v_mfma_f32_16x16x32_bf16 v[152:155], a[4:7], a[104:107], v[152:155]\n\t"
      "ds_read_b64_tr_b16 a[36:37], v117 offset:49152\n\t"
      "v_mfma_f32_16x16x32_bf16 v[156:159], a[4:7], a[108:111], v[156:159]\n\t"
      "ds_read_b64_tr_b16 a[38:39], v117 offset:53248\n\t"
      "v_mfma_f32_16x16x32_bf16 v[160:163], a[8:11], a[96:99], v[160:163]\n\t"
      "v_mfma_f32_16x16x32_bf16 v[164:167], a[8:11], a[100:103], v[164:167]\n\t"
      "ds_read_b64_tr_b16 a[40:41], v118 offset:49152\n\t"
      "v_mfma_f32_16x16x32_bf16 v[168:171], a[8:11], a[104:107], v[168:171]\n\t"
      "ds_read_b64_tr_b16 a[42:43], v118 offset:53248\n\t"
      "v_mfma_f32_16x16x32_bf16 v[172:175], a[8:11], a[108:111], v[172:175]\n\t"
      "v_mfma_f32_16x16x32_bf16 v[176:179], a[12:15], a[96:99], v[176:179]\n\t"
      "ds_read_b64_tr_b16 a[44:45], v119 offset:49152\n\t"
      "v_mfma_f32_16x16x32_bf16 v[180:183], a[12:15], a[100:103], v[180:183]\n\t"
      "ds_read_b64_tr_b16 a[46:47], v119 offset:53248\n\t"
      "v_mfma_f32_16x16x32_bf16 v[184:187], a[12:15], a[104:107], v[184:187]\n\t"
      "v_mfma_f32_16x16x32_bf16 v[188:191], a[12:15], a[108:111], v[188:191]\n\t"
      "ds_read_b64_tr_b16 a[48:49], v120 offset:49152\n\t"
      "v_mfma_f32_16x16x32_bf16 v[192:195], a[16:19], a[96:99], v[192:195]\n\t"
      "v_mfma_f32_16x16x32_bf16 v[196:199], a[16:19], a[100:103], v[196:199]\n\t"
      "ds_read_b64_tr_b16 a[50:51], v120 offset:53248\n\t"
      "v_mfma_f32_16x16x32_bf16 v[200:203], a[16:19], a[104:107], v[200:203]\n\t"
      "ds_read_b64_tr_b16 a[52:53], v121 offset:49152\n\t"
      "v_mfma_f32_16x16x32_bf16 v[204:207], a[16:19], a[108:111], v[204:207]\n\t"
      "v_mfma_f32_16x16x32_bf16 v[208:211], a[20:23], a[96:99], v[208:211]\n\t"
      "ds_read_b64_tr_b16 a[54:55], v121 offset:53248\n\t"
      "v_mfma_f32_16x16x32_bf16 v[212:215], a[20:23], a[100:103], v[212:215]\n\t"
      "ds_read_b64_tr_b16 a[56:57], v122 offset:49152\n\t"
      "v_mfma_f32_16x16x32_bf16 v[216:219], a[20:23], a[104:107], v[216:219]\n\t"
      "v_mfma_f32_16x16x32_bf16 v[220:223], a[20:23], a[108:111], v[220:223]\n\t"
      "ds_read_b64_tr_b16 a[58:59], v122 offset:53248\n\t"
      "v_mfma_f32_16x16x32_bf16 v[224:227], a[24:27], a[96:99], v[224:227]\n\t"
      "ds_read_b64_tr_b16 a[60:61], v123 offset:49152\n\t"
      "v_mfma_f32_16x16x32_bf16 v[228:231], a[24:27], a[100:103], v[228:231]\n\t"
      "v_mfma_f32_16x16x32_bf16 v[232:235], a[24:27], a[104:107], v[232:235]\n\t"
      "ds_read_b64_tr_b16 a[62:63], v123 offset:53248\n\t"
      "v_mfma_f32_16x16x32_bf16 v[236:239], a[24:27], a[108:111], v[236:239]\n\t"
      "v_mfma_f32_16x16x32_bf16 v[240:243], a[28:31], a[96:99], v[240:243]\n\t"
      "v_mfma_f32_16x16x32_bf16 v[244:247], a[28:31], a[100:103], v[244:247]\n\t"
      "v_mfma_f32_16x16x32_bf16 v[248:251], a[28:31], a[104:107], v[248:251]\n\t"
      "v_mfma_f32_16x16x32_bf16 v[252:255], a[28:31], a[108:111], v[252:255]\n\t"
      "s_waitcnt lgkmcnt(0)\n\t"
      "s_waitcnt vmcnt(4)\n\t"
      "v_mfma_f32_16x16x32_bf16 v[128:131], a[32:35], a[112:115], v[128:131]\n\t"
      "v_mfma_f32_16x16x32_bf16 v[132:135], a[32:35], a[116:119], v[132:135]\n\t"
      "v_mfma_f32_16x16x32_bf16 v[136:139], a[32:35], a[120:123], v[136:139]\n\t"
      "v_mfma_f32_16x16x32_bf16 v[140:143], a[32:35], a[124:127], v[140:143]\n\t"
      "v_mfma_f32_16x16x32_bf16 v[144:147], a[36:39], a[112:115], v[144:147]\n\t"
      "v_mfma_f32_16x16x32_bf16 v[148:151], a[36:39], a[116:119], v[148:151]\n\t"
      "v_mfma_f32_16x16x32_bf16 v[152:155], a[36:39], a[120:123], v[152:155]\n\t"
      "v_mfma_f32_16x16x32_bf16 v[156:159], a[36:39], a[124:127], v[156:159]\n\t"
      "v_mfma_f32_16x16x32_bf16 v[160:163], a[40:43], a[112:115], v[160:163]\n\t"
      "v_mfma_f32_16x16x32_bf16 v[164:167], a[40:43], a[116:119], v[164:167]\n\t"
      "v_mfma_f32_16x16x32_bf16 v[168:171], a[40:43], a[120:123], v[168:171]\n\t"
      "v_mfma_f32_16x16x32_bf16 v[172:175], a[40:43], a[124:127], v[172:175]\n\t"
      "v_mfma_f32_16x16x32_bf16 v[176:179], a[44:47], a[112:115], v[176:179]\n\t"
      "v_mfma_f32_16x16x32_bf16 v[180:183], a[44:47], a[116:119], v[180:183]\n\t"
      "v_mfma_f32_16x16x32_bf16 v[184:187], a[44:47], a[120:123], v[184:187]\n\t"
      "v_mfma_f32_16x16x32_bf16 v[188:191], a[44:47], a[124:127], v[188:191]\n\t"
      "v_mfma_f32_16x16x32_bf16 v[192:195], a[48:51], a[112:115], v[192:195]\n\t"
      "v_mfma_f32_16x16x32_bf16 v[196:199], a[48:51], a[116:119], v[196:199]\n\t"
      "v_mfma_f32_16x16x32_bf16 v[200:203], a[48:51], a[120:123], v[200:203]\n\t"
      "v_mfma_f32_16x16x32_bf16 v[204:207], a[48:51], a[124:127], v[204:207]\n\t"
      "v_mfma_f32_16x16x32_bf16 v[208:211], a[52:55], a[112:115], v[208:211]\n\t"
      "v_mfma_f32_16x16x32_bf16 v[212:215], a[52:55], a[116:119], v[212:215]\n\t"
      "v_mfma_f32_16x16x32_bf16 v[216:219], a[52:55], a[120:123], v[216:219]\n\t"
      "v_mfma_f32_16x16x32_bf16 v[220:223], a[52:55], a[124:127], v[220:223]\n\t"
      "v_mfma_f32_16x16x32_bf16 v[224:227], a[56:59], a[112:115], v[224:227]\n\t"
      "v_mfma_f32_16x16x32_bf16 v[228:231], a[56:59], a[116:119], v[228:231]\n\t"
      "v_mfma_f32_16x16x32_bf16 v[232:235], a[56:59], a[120:123], v[232:235]\n\t"
      "v_mfma_f32_16x16x32_bf16 v[236:239], a[56:59], a[124:127], v[236:239]\n\t"
      "v_mfma_f32_16x16x32_bf16 v[240:243], a[60:63], a[112:115], v[240:243]\n\t"
      "v_mfma_f32_16x16x32_bf16 v[244:247], a[60:63], a[116:119], v[244:247]\n\t"
      "v_mfma_f32_16x16x32_bf16 v[248:251], a[60:63], a[120:123], v[248:251]\n\t"
      "v_mfma_f32_16x16x32_bf16 v[252:255], a[60:63], a[124:127], v[252:255]\n\t"
      "s_waitcnt vmcnt(0)\n\t"
      "s_nop 7\n\t"
      "s_nop 7\n\t"
      : "=&{v[128:143]}"(acc2[0]), "=&{v[144:159]}"(acc2[1]), "=&{v[160:175]}"(acc2[2]), "=&{v[176:191]}"(acc2[3]), "=&{v[192:207]}"(acc2[4]), "=&{v[208:223]}"(acc2[5]), "=&{v[224:239]}"(acc2[6]), "=&{v[240:255]}"(acc2[7]), [cpv] "=&v"(cpv), "=&{v[64:79]}"(zq), "=&{v[80:95]}"(xq2), "=&{v[96:99]}"(sq), "+{v[32:47]}"(y0), "+{v[48:63]}"(y1)
      : [wb] "s"(wdb2), [hb] "s"(hb), [m0b] "s"(m0b), [va] "v"(va), [l16] "v"(l16), [lane] "v"(lane), [fz] "s"(fz), [czr] "s"(czr), [cpr] "s"(cpr), [l8] "v"(l8), [zr] "v"(0u), [o01] "s"(kh * 229376), [o23] "s"(229376 - kh * 229376),
        [zb] "s"(a.ws + WS_ZT + (size_t)d0 * 4096 + tok0 * 2), [xb] "s"((const char*)(a.q + (size_t)tok0 * D_ + d0)), [sb] "s"(a.ws + WS_STATS + tok0 * 8),
        [vx] "v"(i16 * 2048 + ((((g & 1) << 4) | ((g >> 1) << 3)) << 1)), [tmo] "s"(a.ws + WS_TMO),
        [vu] "v"(vu), [vz] "v"(vz), [zt] "s"(a.ws + WS_ZT + (size_t)ch * 4096), [dsc] "s"(dsc),
        [eplo] "s"(eplo), [ephi] "s"(ephi)
      : "vcc", "v13", "v14", "v15", "v16", "v17", "v18", "v19", "v20", "v21", "v22", "v23", "v24", "v25", "v26", "v27", "v28", "v29", "v30", "v31", "v100", "v101", "v102", "v103", "v104", "v105", "v106", "v107", "v108", "v109", "v110", "v111", "v112", "v113", "v114", "v115", "v116", "v117", "v118", "v119", "v120", "v121", "v122", "v123", "v124", "v125", "v126", "v127", "a0", "a1", "a2", "a3", "a4", "a5", "a6", "a7", "a8", "a9", "a10", "a11", "a12", "a13", "a14", "a15", "a16", "a17", "a18", "a19", "a20", "a21", "a22", "a23", "a24", "a25", "a26", "a27", "a28", "a29", "a30", "a31", "a32", "a33", "a34", "a35", "a36", "a37", "a38", "a39", "a40", "a41", "a42", "a43", "a44", "a45", "a46", "a47", "a48", "a49", "a50", "a51", "a52", "a53", "a54", "a55", "a56", "a57", "a58", "a59", "a60", "a61", "a62", "a63", "a64", "a65", "a66", "a67", "a68", "a69", "a70", "a71", "a72", "a73", "a74", "a75", "a76", "a77", "a78", "a79", "a80", "a81", "a82", "a83", "a84", "a85", "a86", "a87", "a88", "a89", "a90", "a91", "a92", "a93", "a94", "a95", "a96", "a97", "a98", "a99", "a100", "a101", "a102", "a103", "a104", "a105", "a106", "a107", "a108", "a109", "a110", "a111", "a112", "a113", "a114", "a115", "a116", "a117", "a118", "a119", "a120", "a121", "a122", "a123", "a124", "a125", "a126", "a127", "s40", "s41", "s42", "s43", "s44", "s45", "s46", "s47", "s48", "s49", "s50", "s51", "s52", "memory", "scc");
  }
  STAMP(6);
  LANE_VARS();
  f32x4 f[4][2];
  {
    char* mine = lds + w * 16384 + l16;
    const char* other = lds + (w ^ 2) * 16384 + l16;
#pragma unroll
    for (int dt = 0; dt < 4; ++dt)
#pragma unroll
      for (int tt = 0; tt < 4; ++tt) {
        const f32x16& s1 = acc2[4 + dt];
        *(f32x4*)(mine + (dt * 4 + tt) * 1024) = (f32x4){s1[tt * 4], s1[tt * 4 + 1], s1[tt * 4 + 2], s1[tt * 4 + 3]};
      }
    BAR();
    char* pm = a.ws + WS_PART + ((size_t)b * 4 + w) * 8192 + l16;
#pragma unroll
    for (int dt = 0; dt < 4; ++dt) {
      f32x4 snd[2];
#pragma unroll
      for (int t2 = 0; t2 < 2; ++t2) {
        const f32x16& s0 = acc2[dt]; const int o = (2 + t2) * 4;
        snd[t2] = (f32x4){s0[o], s0[o + 1], s0[o + 2], s0[o + 3]} + *(const f32x4*)(other + (dt * 4 + 2 + t2) * 1024);
      }
      st16_sc1(pm + dt * 1024, (u32x4){pk2(snd[0][0], snd[0][1]), pk2(snd[0][2], snd[0][3]), pk2(snd[1][0], snd[1][1]), pk2(snd[1][2], snd[1][3])});
    }
#pragma unroll
    for (int dt = 0; dt < 4; ++dt)
#pragma unroll
      for (int t2 = 0; t2 < 2; ++t2) {
        const f32x16& s0 = acc2[dt]; const int o = t2 * 4;
        f[dt][t2] = (f32x4){s0[o], s0[o + 1], s0[o + 2], s0[o + 3]} + *(const f32x4*)(other + (dt * 4 + t2) * 1024);
      }
  }
  const int doff = ((g & 1) << 4) | ((g >> 1) << 3);
  float base[2][2][8];
  {
    char* zl = lds + 65536 + w * 8192;
#pragma unroll
    for (int i = 0; i < 4; ++i) *(u32x4*)(zl + (16 * i + (lane >> 2)) * 80 + (lane & 3) * 16) = (u32x4){zq[4 * i], zq[4 * i + 1], zq[4 * i + 2], zq[4 * i + 3]};
    asm volatile("s_waitcnt lgkmcnt(0)" ::: "memory");
    const unsigned za = (unsigned)(size_t)zl + (doff + q4) * 80 + p4 * 8;
#pragma unroll
    for (int s = 0; s < 2; ++s)
#pragma unroll
      for (int t2 = 0; t2 < 2; ++t2) {
        u32x2 zlo, zhi;
        asm volatile("ds_read_b64_tr_b16 %0, %2 offset:%c3\n\tds_read_b64_tr_b16 %1, %2 offset:%c4\n\ts_waitcnt lgkmcnt(0)"
                     : "=&v"(zlo), "=&v"(zhi) : "v"(za), "i"(s * 32 * 80 + t2 * 32), "i"(s * 32 * 80 + 4 * 80 + t2 * 32) : "memory");
        const int xo = (2 * s + t2) * 4;
        const u32x4 xw = {xq2[xo], xq2[xo + 1], xq2[xo + 2], xq2[xo + 3]}; const float mb = __uint_as_float(sq[2 * t2]), rs = __uint_as_float(sq[2 * t2 + 1]);
        const unsigned zz[4] = {zlo[0], zlo[1], zhi[0], zhi[1]};
#pragma unroll
        for (int e = 0; e < 4; ++e) {
          const float x0 = bflo(xw[e]), x1 = bfhi(xw[e]);
          const float n0 = rbf(rbf(x0 - mb) * rs), n1 = rbf(rbf(x1 - mb) * rs);
          base[s][t2][2 * e] = x0 + n0 * bflo(zz[e]); base[s][t2][2 * e + 1] = x1 + n1 * bfhi(zz[e]);
        }
      }
  }
  STAMP(7);
  {
    if (!__all(cpv[0] == eplo && cpv[1] == ephi)) wait_flags(cpr, 1, ep, a.ws);
    const char* pp = a.ws + WS_PART + ((size_t)(b ^ 1) * 4 + w) * 8192 + l16;
    u32x4 pv[4];
    for (unsigned spins = 0;; ++spins) {
      asm volatile(
        "global_load_dwordx4 %0, %4, off sc1\n\tglobal_load_dwordx4 %1, %4, off offset:1024 sc1\n\t"
        "global_load_dwordx4 %2, %4, off offset:2048 sc1\n\tglobal_load_dwordx4 %3, %4, off offset:3072 sc1\n\ts_waitcnt vmcnt(0)"
        : "=&v"(pv[0]), "=&v"(pv[1]), "=&v"(pv[2]), "=&v"(pv[3]) : "v"(pp) : "memory");
      bool miss = false;
#pragma unroll
      for (int i = 0; i < 4; ++i) miss |= (pv[i][0] == SENTINEL) | (pv[i][1] == SENTINEL) | (pv[i][2] == SENTINEL) | (pv[i][3] == SENTINEL);
      if (!__any(miss)) break;
      if (spins > (1u << 18)) { st_flag(a.ws + WS_TMO, ep); break; }
    }
    f32x4 pw[4][2];
#pragma unroll
    for (int dt = 0; dt < 4; ++dt)
#pragma unroll
      for (int t2 = 0; t2 < 2; ++t2)
        pw[dt][t2] = (f32x4){bflo(pv[dt][2 * t2]), bfhi(pv[dt][2 * t2]), bflo(pv[dt][2 * t2 + 1]), bfhi(pv[dt][2 * t2 + 1])};
#pragma unroll
    for (int s = 0; s < 2; ++s)
#pragma unroll
      for (int t2 = 0; t2 < 2; ++t2) {
        const f32x4 A = f[2 * s][t2] + pw[2 * s][t2];
        const f32x4 B = f[2 * s + 1][t2] + pw[2 * s + 1][t2];
        float o8[8];
#pragma unroll
        for (int e = 0; e < 4; ++e) {
          auto sw = __builtin_amdgcn_permlane16_swap(__float_as_uint(A[e]), __float_as_uint(B[e]), false, false);
          o8[e] = __uint_as_float(sw[0]); o8[4 + e] = __uint_as_float(sw[1]);
        }
        u32x4 ov;
#pragma unroll
        for (int e = 0; e < 4; ++e) ov[e] = pk2(base[s][t2][2 * e] + o8[2 * e], base[s][t2][2 * e + 1] + o8[2 * e + 1]);
        st16_sc1(a.out + (size_t)(tok0 + 16 * t2 + i16) * D_ + d0 + 32 * s + doff, ov);
      }
  }
  STAMP(8);
}

extern "C" void kernel_launch(void* const* d_in, const int* in_sizes, int n_in, void* d_out, int out_size, void* d_ws, size_t ws_size,
                              hipStream_t stream) {
  (void)in_sizes; (void)n_in; (void)out_size; (void)ws_size;
  (void)hipFuncSetAttribute((const void*)fused_main, hipFuncAttributeMaxDynamicSharedMemorySize, LDS_BYTES);
  KArgs a;
  a.q = (const u16*)d_in[0]; a.k = (const u16*)d_in[1]; a.v = (const u16*)d_in[2]; a.shk = (const u16*)d_in[3]; a.ssk = (const u16*)d_in[4];
  a.ssd = (const u16*)d_in[5]; a.wg = (const u16*)d_in[7]; a.wu = (const u16*)d_in[8]; a.wd = (const u16*)d_in[9]; a.out = (u16*)d_out; a.ws = (char*)d_ws;
  fused_main<<<256, 256, LDS_BYTES, stream>>>(a);
}
```
